```python
import torch

def _softmax(x, axis=-1):
    return torch.softmax(x, dim=axis)

def _gelu(x):
    return torch.nn.functional.gelu(x, approximate='tanh')

def _silu(x):
    return torch.nn.functional.silu(x)

def _sigmoid(x):
    return torch.sigmoid(x)

def _layernorm(x, axis=-1, eps=1e-05):
    m = x.mean(dim=axis, keepdim=True)
    v = x.var(dim=axis, keepdim=True, unbiased=False)
    return (x - m) / torch.sqrt(v + eps)

def _scatter_add(tgt, idx, src, axis=0):
    return tgt.clone().index_add_(axis, idx, src)

def _segment_sum(src, seg, n):
    out = torch.zeros((n,) + src.shape[1:], dtype=src.dtype)
    return out.index_add_(0, seg, src)

def _pad(x, pw):
    flat = tuple((v for pair in reversed(pw) for v in pair))
    return torch.nn.functional.pad(x, flat)
kernel_name = 'adaLN_cond__mha_block_cached__moe_arch_mod__prompt'

def setup_inputs():

    _w = lambda a: (lambda t: t.to(torch.bfloat16) if t.is_floating_point() else t)(torch.as_tensor(a))

    def _A_setup():
        B, Sq, D = (1, 2048, 512)
        H, Sk, Dh = (32, 2048, 128)
        return {'x': _w((lambda *s: torch.randn(*s, dtype=torch.bfloat16) * 0.1)(B, Sq, D)), 'k_cache': _w((lambda *s: torch.randn(*s, dtype=torch.bfloat16) * 0.8)(B, H, Sk, Dh)), 'v_cache': _w((lambda *s: torch.randn(*s, dtype=torch.bfloat16) * 3.2)(B, H, Sk, Dh)), 'wq': _w((lambda *s: torch.randn(*s, dtype=torch.bfloat16) * 0.8)(D, H * Dh) * D ** (-0.5)), 'wo': _w((lambda *s: torch.randn(*s, dtype=torch.bfloat16) * 3.2)(H * Dh, D) * (H * Dh) ** (-0.5))}
    _A_in = _A_setup()

    def _B_setup():
        B, S, D, F, C = (1, 2048, 512, 1792, 1)
        return {'x': _w((lambda *s: torch.randn(*s, dtype=torch.bfloat16) * 0.1)(B, S, D)), 'w_r': _w((lambda *s: torch.randn(*s, dtype=torch.bfloat16) * 3.2)(D)), 'w_gate': _w((lambda *s: torch.randn(*s, dtype=torch.bfloat16) * 3.2)(D, F)), 'w_up': _w((lambda *s: torch.randn(*s, dtype=torch.bfloat16) * 3.2)(D, F)), 'w_down': _w((lambda *s: torch.randn(*s, dtype=torch.bfloat16) * 3.2)(F, D))}
    _B_in = _B_setup()
    return {'A_x': _A_in['x'], 'A_k_cache': _A_in['k_cache'], 'A_v_cache': _A_in['v_cache'], 'A_wq': _A_in['wq'], 'A_wo': _A_in['wo'], 'B_x': _B_in['x'], 'B_w_r': _B_in['w_r'], 'B_w_gate': _B_in['w_gate'], 'B_w_up': _B_in['w_up'], 'B_w_down': _B_in['w_down'], 'c': _w((lambda *s: torch.randn(*s, dtype=torch.bfloat16) * 0.2)(1, 512) * 8.0), 'w_mod': _w((lambda *s: torch.randn(*s, dtype=torch.bfloat16) * 0.2)(512, 6 * 512) * 512 ** (-0.5) * 8.0)}

def reference(A_x, A_k_cache, A_v_cache, A_wq, A_wo, B_x, B_w_r, B_w_gate, B_w_up, B_w_down, c, w_mod):

    def _A_ref(x, k_cache, v_cache, wq, wo):
        B, Sq, D = x.shape
        H, Dh = (32, 128)
        q = (lambda x, *d: x.permute(*d))((x @ wq).reshape(B, Sq, H, Dh), 0, 2, 1, 3)
        scores = torch.einsum('bhqd,bhkd->bhqk', q, k_cache) * Dh ** (-0.5)
        p = _softmax(scores, axis=-1)
        o = torch.einsum('bhqk,bhkd->bhqd', p, v_cache)
        return (lambda x, *d: x.permute(*d))(o, 0, 2, 1, 3).reshape(B, Sq, H * Dh) @ wo

    def _B_ref(x, w_r, w_gate, w_up, w_down):
        B, S, D = x.shape
        C = 1
        r = x @ w_r
        idx = torch.argsort(-r, axis=-1)[:, :C]
        idx_e = (lambda x, n, axis: torch.repeat_interleave(x, n, dim=axis))(idx.reshape(B, C, 1), D, axis=2)
        x_sel = (lambda x, i, axis: torch.take_along_dim(x, i, dim=axis))(x, idx_e, axis=1)
        g = _silu(x_sel @ w_gate)
        u = x_sel @ w_up
        y = g * u @ w_down
        r_sel = _sigmoid((lambda x, i, axis: torch.take_along_dim(x, i, dim=axis))(r, idx, axis=1))
        y = y * r_sel.reshape(B, C, 1)
        b_off = (torch.arange(B) * S).reshape(B, 1)
        flat_idx = (idx + b_off).reshape(B * C)
        zero = (x * 0.0).reshape(B * S, D)
        upd = _scatter_add(zero, flat_idx, y.reshape(B * C, D), axis=0)
        return x + upd.reshape(B, S, D)
    x = A_x
    mod = (_silu(c) @ w_mod).reshape(c.shape[0], 6, 1, -1)
    sa, ca, ga, sb, cb, gb = (mod[:, 0].clone(), mod[:, 1].clone(), mod[:, 2].clone(), mod[:, 3].clone(), mod[:, 4].clone(), mod[:, 5].clone())
    h = _layernorm(x, axis=-1) * (1 + ca) + sa
    x = x + ga * _A_ref(h, A_k_cache, A_v_cache, A_wq, A_wo)
    h = _layernorm(x, axis=-1) * (1 + cb) + sb
    x = x + gb * _B_ref(h, B_w_r, B_w_gate, B_w_up, B_w_down)
    return x
```

```cpp
#include <hip/hip_runtime.h>
#include <stdint.h>

typedef float    f32x4 __attribute__((ext_vector_type(4)));
typedef short    s16x8 __attribute__((ext_vector_type(8)));
typedef short    s16x4 __attribute__((ext_vector_type(4)));
typedef unsigned u32x4 __attribute__((ext_vector_type(4)));
typedef unsigned u32x2 __attribute__((ext_vector_type(2)));
typedef __bf16   bf16x2 __attribute__((ext_vector_type(2)));
typedef float    f32x2 __attribute__((ext_vector_type(2)));
typedef unsigned short u16;
#define DI __device__ __forceinline__

constexpr int SEQ = 2048, DM = 512, NH = 32, DH = 128, FF = 1792;
constexpr size_t WS_MOD = 0;
constexpr size_t WS_TAB = 16384;
constexpr size_t WS_ACT = 3u << 20;
constexpr size_t WS_CAND = (3u << 20) + (256u << 10);
constexpr size_t WS_CH  = 40u << 20;
constexpr size_t WS_HF  = 65536;
constexpr size_t WS_OF  = 4u << 20;
constexpr size_t WS_PP  = 24u << 20;

DI float bf2f(u16 x) { return __uint_as_float((unsigned)x << 16); }
DI unsigned cvtpk(float lo, float hi) { f32x2 v = {lo, hi}; bf16x2 b = __builtin_convertvector(v, bf16x2); return __builtin_bit_cast(unsigned, b); }
DI float rbf(float x) { return __uint_as_float(cvtpk(x, 0.f) << 16); }
DI float lo16(unsigned w) { return __uint_as_float(w << 16); }
DI float hi16(unsigned w) { return __uint_as_float(w & 0xffff0000u); }
template <int CTRL> DI float dpp_add(float x) { return x + __builtin_bit_cast(float, __builtin_amdgcn_update_dpp(0, __builtin_bit_cast(int, x), CTRL, 0xf, 0xf, true)); }
DI float row_sum16(float x) { x = dpp_add<0x128>(x); x = dpp_add<0x124>(x); x = dpp_add<0x122>(x); return dpp_add<0x121>(x); }
DI float xrow_sum(float x) {
  auto a = __builtin_amdgcn_permlane16_swap(__float_as_uint(x), __float_as_uint(x), false, false); x = __uint_as_float(a[0]) + __uint_as_float(a[1]);
  auto b = __builtin_amdgcn_permlane32_swap(__float_as_uint(x), __float_as_uint(x), false, false); return __uint_as_float(b[0]) + __uint_as_float(b[1]);
}
DI float wave_sum(float x) { return xrow_sum(row_sum16(x)); }
DI float silu_f(float v) { return v * __builtin_amdgcn_rcpf(1.f + __expf(-v)); }
DI f32x4 mfma16(s16x8 a, s16x8 b, f32x4 c) { return __builtin_amdgcn_mfma_f32_16x16x32_bf16(a, b, c, 0, 0, 0); }

constexpr int XF_AUX = 16;
constexpr int NMOE_WORDS = 28 * 512;
constexpr int ATTN_PIECES = 640, OPROJ_CHUNKS = 2, TAIL_CHUNKS = 2;
#define CODE_ADDR(sym, out) do { unsigned lo_, hi_; asm volatile("s_getpc_b64 s[92:93]\n\ts_add_u32 s92, s92, " #sym "@rel32@lo+4\n\ts_addc_u32 s93, s93, " #sym "@rel32@hi+12\n\ts_mov_b32 %0, s92\n\ts_mov_b32 %1, s93" \
    : "=s"(lo_), "=s"(hi_) :: "scc", "s92", "s93"); out = (const char*)(((uint64_t)hi_ << 32) | lo_); } while (0)
extern "C" __global__ __launch_bounds__(512, 2) void adaln_pre(const u16* __restrict__ x, const u16* __restrict__ c, const u16* __restrict__ wmod, u16* __restrict__ mod, u16* __restrict__ xf, float* __restrict__ tab,
                                                               const u16* __restrict__ wq,
                                                               unsigned long long* __restrict__ cand, unsigned long long* __restrict__ yg,
                                                               const u16* __restrict__ wr) {
  __shared__ __attribute__((aligned(16))) char lds[16384 + 4096];
  const int tid = threadIdx.x, b = blockIdx.x, lane = tid & 63, w = tid >> 6;
  if (b < 48) {
    float* sc = (float*)lds; float* red = sc + 512;
    const unsigned cw = c[tid];
    const int q8 = tid & 7, rr = tid >> 3;
    u32x4 wa[8];
#pragma unroll
    for (int p = 0; p < 8; ++p) wa[p] = *(const u32x4*)(wmod + (size_t)(p * 64 + rr) * 3072 + b * 64 + q8 * 8);
    sc[tid] = rbf(silu_f(bf2f((u16)cw)));
    __syncthreads();
    float a[8] = {0, 0, 0, 0, 0, 0, 0, 0};
#pragma unroll
    for (int p = 0; p < 8; ++p) {
      const float s = sc[p * 64 + rr];
#pragma unroll
      for (int e = 0; e < 4; ++e) { a[2 * e] += s * lo16(wa[p][e]); a[2 * e + 1] += s * hi16(wa[p][e]); }
    }
#pragma unroll
    for (int i = 0; i < 4; ++i) { auto s = __builtin_amdgcn_permlane32_swap(__float_as_uint(a[i]), __float_as_uint(a[i + 4]), false, false); a[i] = __uint_as_float(s[0]) + __uint_as_float(s[1]); }
#pragma unroll
    for (int i = 0; i < 2; ++i) { auto s = __builtin_amdgcn_permlane16_swap(__float_as_uint(a[i]), __float_as_uint(a[i + 2]), false, false); a[i] = dpp_add<0x128>(__uint_as_float(s[0]) + __uint_as_float(s[1])); }
    if ((lane & 8) == 0) { red[w * 64 + (lane & 7) * 8 + (lane >> 5) * 4 + ((lane >> 4) & 1) * 2] = a[0]; red[w * 64 + (lane & 7) * 8 + (lane >> 5) * 4 + ((lane >> 4) & 1) * 2 + 1] = a[1]; }
    __syncthreads();
    if (tid < 64) {
      float s = 0;
#pragma unroll
      for (int i = 0; i < 8; ++i) s += red[i * 64 + tid];
      s = rbf(s);
      const int col = b * 64 + tid;
      if (col >= 1024) mod[col - 1024] = (u16)(cvtpk(col >= 2048 && col < 2560 ? 1.f + s : s, 0.f) & 0xffff);
      else {
        const float QS = 0.08838834764831845f * 1.4426950408889634f;
        const int f = col & 511, ks = f >> 5, j = ((f >> 4) & 1) * 4 + (f & 3), g = (f >> 2) & 3;
        tab[ks * 64 + g * 16 + (col < 512 ? 8 : 0) + j] = (col < 512 ? s : rbf(1.f + s)) * QS;
      }
    }
    return;
  }
#define WARM_TOUCH() do { \
    const int xg = b & 7, u = ((b - 48) >> 3) * 512 + tid; const char* a = nullptr; \
    if (u < 4096) a = (const char*)(wq + (size_t)(u >> 3) * (NH * DH) + xg * 512 + (u & 7) * 64); \
    else if (u < 4096 + ATTN_PIECES) { const char* ca; CODE_ADDR(adaln_attn, ca); a = ca + (u - 4096) * 64; } \
    else if (u < 4096 + ATTN_PIECES + 16) a = (const char*)wr + (u - 4096 - ATTN_PIECES) * 64; \
    if (a) asm volatile("global_load_dword v127, %0, off" :: "v"(a) : "v127"); } while (0)
  if (b < 176) {
    const int rowblk = b - 48;
    u32x4 xv[2];
#pragma unroll
    for (int r = 0; r < 2; ++r) xv[r] = *(const u32x4*)(x + (size_t)(rowblk * 16 + w * 2 + r) * DM + lane * 8);
#pragma unroll
    for (int r = 0; r < 2; ++r) {
      float v[8], s = 0;
#pragma unroll
      for (int e = 0; e < 4; ++e) { v[2 * e] = lo16(xv[r][e]); v[2 * e + 1] = hi16(xv[r][e]); }
#pragma unroll
      for (int e = 0; e < 8; ++e) s += v[e];
      const float mu = wave_sum(s) * (1.f / DM); float q = 0;
#pragma unroll
      for (int e = 0; e < 8; ++e) { v[e] -= mu; q += v[e] * v[e]; }
      const float rs = rsqrtf(wave_sum(q) * (1.f / DM) + 1e-5f);
      typedef _Float16 h16x2 __attribute__((ext_vector_type(2)));
      unsigned pk[4];
#pragma unroll
      for (int e = 0; e < 4; ++e) { const h16x2 hh = {(_Float16)(v[2 * e] * rs), (_Float16)(v[2 * e + 1] * rs)}; pk[e] = __builtin_bit_cast(unsigned, hh); }
      const int ks = lane >> 2, t = (lane >> 1) & 1, g = (lane & 1) * 2, rc = w * 2 + r;
      *(u32x2*)(lds + (ks * 64 + rc + 16 * g) * 16 + 8 * t) = u32x2{pk[0], pk[1]};
      *(u32x2*)(lds + (ks * 64 + rc + 16 * (g + 1)) * 16 + 8 * t) = u32x2{pk[2], pk[3]};
    }
    __syncthreads();
    const __amdgpu_buffer_rsrc_t rx = __builtin_amdgcn_make_buffer_rsrc((void*)xf, (short)0, 128 * 16384, 0x00020000);
#pragma unroll
    for (int i = 0; i < 2; ++i) __builtin_amdgcn_raw_buffer_store_b128(*(const u32x4*)(lds + (i * 512 + tid) * 16), rx, rowblk * 16384 + (i * 512 + tid) * 16, 0, XF_AUX);
    WARM_TOUCH();
  } else {
    const int k = b - 176;
    WARM_TOUCH();
    if (tid < 3) { const int i = k * 3 + tid; if (i < 228) cand[i * 16] = 0xffffffff00000000ull; }
    for (int i = k * 512 + tid; i < NMOE_WORDS; i += 80 * 512) yg[i] = 0ull;
  }
}

DI void attn_slow_wave(const u16* __restrict__ hf, const float* __restrict__ tab, const u16* __restrict__ wq, const u16* __restrict__ kc, const u16* __restrict__ vc,
                                            u16* __restrict__ of, int head, int rowblk0) {
  const int lane = threadIdx.x & 63, i = lane & 15, g = lane >> 4;
  const u16* K = kc + (size_t)head * SEQ * DH; const u16* V = vc + (size_t)head * SEQ * DH;
#pragma unroll 1
  for (int qb = 0; qb < 4; ++qb) {
    const int rowblk = rowblk0 + qb;
    f32x4 D[8];
#pragma unroll
    for (int t = 0; t < 8; ++t) D[t] = f32x4{0, 0, 0, 0};
#pragma unroll 1
    for (int ks = 0; ks < 16; ++ks) {
      typedef _Float16 h16x8 __attribute__((ext_vector_type(8)));
      const h16x8 xh = *(const h16x8*)(hf + ((size_t)(rowblk * 16 + ks) * 64 + lane) * 8);
      const float* tc = tab + ks * 64 + g * 16;
      u32x4 bw;
#pragma unroll
      for (int e = 0; e < 4; ++e) bw[e] = cvtpk((float)xh[2 * e] * tc[2 * e] + tc[8 + 2 * e], (float)xh[2 * e + 1] * tc[2 * e + 1] + tc[8 + 2 * e + 1]);
      const s16x8 b = __builtin_bit_cast(s16x8, bw);
#pragma unroll
      for (int t = 0; t < 8; ++t) {
        const int s = t >> 1, e = t & 1; s16x8 a;
#pragma unroll
        for (int j = 0; j < 8; ++j) a[j] = (short)wq[(size_t)(32 * ks + 16 * (j >> 2) + 4 * g + (j & 3)) * (NH * DH) + head * DH + 32 * s + 8 * (i >> 2) + 4 * e + (i & 3)];
        D[t] = mfma16(a, b, D[t]);
      }
    }
    s16x8 Q[4];
#pragma unroll
    for (int s = 0; s < 4; ++s) { u32x4 w = {cvtpk(D[2 * s][0], D[2 * s][1]), cvtpk(D[2 * s][2], D[2 * s][3]), cvtpk(D[2 * s + 1][0], D[2 * s + 1][1]), cvtpk(D[2 * s + 1][2], D[2 * s + 1][3])}; Q[s] = __builtin_bit_cast(s16x8, w); }
    f32x4 O[8];
#pragma unroll
    for (int t = 0; t < 8; ++t) O[t] = f32x4{0, 0, 0, 0};
    float m = -3.0e38f, l = 0.f;
#pragma unroll 1
    for (int u = 0; u < SEQ / 32; ++u) {
      f32x4 S[2];
#pragma unroll
      for (int kb = 0; kb < 2; ++kb) {
        S[kb] = f32x4{0, 0, 0, 0};
#pragma unroll
        for (int s = 0; s < 4; ++s) S[kb] = mfma16(*(const s16x8*)(K + (size_t)(32 * u + 16 * kb + i) * DH + 32 * s + 8 * g), Q[s], S[kb]);
      }
      float mx = fmaxf(fmaxf(fmaxf(S[0][0], S[0][1]), fmaxf(S[0][2], S[0][3])), fmaxf(fmaxf(S[1][0], S[1][1]), fmaxf(S[1][2], S[1][3])));
      mx = fmaxf(mx, __shfl_xor(mx, 16)); mx = fmaxf(mx, __shfl_xor(mx, 32));
      const float mn = fmaxf(m, mx), al = exp2f(m - mn); m = mn;
      float p[8];
#pragma unroll
      for (int e = 0; e < 8; ++e) p[e] = exp2f(S[e >> 2][e & 3] - mn);
      l = l * al + (((p[0] + p[1]) + (p[2] + p[3])) + ((p[4] + p[5]) + (p[6] + p[7])));
      u32x4 pw = {cvtpk(p[0], p[1]), cvtpk(p[2], p[3]), cvtpk(p[4], p[5]), cvtpk(p[6], p[7])};
      const s16x8 P = __builtin_bit_cast(s16x8, pw);
#pragma unroll
      for (int db = 0; db < 8; ++db) {
        s16x8 a;
#pragma unroll
        for (int j = 0; j < 8; ++j) a[j] = (short)V[(size_t)(32 * u + 16 * (j >> 2) + 4 * g + (j & 3)) * DH + 16 * db + i];
        O[db] = mfma16(a, P, O[db] * al);
      }
    }
    l += __shfl_xor(l, 16); l += __shfl_xor(l, 32);
    const float il = 1.f / l;
#pragma unroll
    for (int s = 0; s < 4; ++s) {
      u32x4 w = {cvtpk(O[2 * s][0] * il, O[2 * s][1] * il), cvtpk(O[2 * s][2] * il, O[2 * s][3] * il), cvtpk(O[2 * s + 1][0] * il, O[2 * s + 1][1] * il), cvtpk(O[2 * s + 1][2] * il, O[2 * s + 1][3] * il)};
      *(u32x4*)(of + ((size_t)(rowblk * 128 + head * 4 + s) * 64 + lane) * 8) = w;
    }
  }
}
extern "C" __global__ __launch_bounds__(256, 1) void adaln_attn(const u16* __restrict__ hf, const float* __restrict__ tab, const u16* __restrict__ wq, const u16* __restrict__ kc, const u16* __restrict__ vc, u16* __restrict__ of,
                                                                 const u16* __restrict__ wo, const u16* __restrict__ wg, const u16* __restrict__ wu, const u16* __restrict__ wd) {
  extern __shared__ __attribute__((aligned(16))) char smem[];
  const int b = blockIdx.x, head = (b & 7) * 4 + ((b >> 3) & 3), qblk = b >> 5;
  const int wv = __builtin_amdgcn_readfirstlane(threadIdx.x >> 6);
  const int rowblk0 = qblk * 16 + wv * 4;
  const uint64_t p_wq = (uint64_t)(wq + head * DH), p_hf = (uint64_t)(hf + (size_t)rowblk0 * 16 * 512);
  const uint64_t p_k = (uint64_t)(kc + (size_t)head * SEQ * DH), p_v = (uint64_t)(vc + (size_t)head * SEQ * DH);
  const uint64_t p_of = (uint64_t)(of + ((size_t)rowblk0 * 128 + head * 4) * 512);
  const unsigned ldsb = __builtin_amdgcn_readfirstlane((unsigned)(size_t)smem);
  const int wx = (b >> 3) * 4 + wv, wid = b * 4 + wv;
  const uint64_t pf0 = (uint64_t)wo + (size_t)((b & 7) >> 1) * (1u << 20) + (size_t)wx * 8192, pf1 = pf0 + 4096;
  const int c0 = wid, c1 = wid < 320 ? 1024 + wid : wid;
  const uint64_t pf2 = (c0 < 448 ? (uint64_t)wg + (size_t)c0 * 4096 : c0 < 896 ? (uint64_t)wu + (size_t)(c0 - 448) * 4096 : (uint64_t)wd + (size_t)(c0 - 896) * 4096);
  const uint64_t pf3 = (c1 < 448 ? (uint64_t)wg + (size_t)c1 * 4096 : c1 < 896 ? (uint64_t)wu + (size_t)(c1 - 448) * 4096 : (uint64_t)wd + (size_t)(c1 - 896) * 4096);
  const char* cn; CODE_ADDR(adaln_oproj, cn);
  const char* ct; CODE_ADDR(adaln_tail, ct);
  const uint64_t pf4 = wx < OPROJ_CHUNKS ? (uint64_t)cn + (size_t)wx * 4096 : wx < OPROJ_CHUNKS + TAIL_CHUNKS ? (uint64_t)ct + (size_t)(wx - OPROJ_CHUNKS) * 4096 : (uint64_t)cn;
  unsigned flag;
  asm volatile(
    "s_mov_b64 s[48:49], %[wq]\n"
    "s_and_b32 s49, s49, 0xffff\n"
    "s_mov_b32 s50, 4194304\n"
    "s_mov_b32 s51, 0x20000\n"
    "s_mov_b64 s[52:53], %[hf]\n"
    "s_and_b32 s53, s53, 0xffff\n"
    "s_mov_b32 s54, 65536\n"
    "s_mov_b32 s55, 0x20000\n"
    "s_mov_b64 s[56:57], %[tab]\n"
    "s_and_b32 s57, s57, 0xffff\n"
    "s_mov_b32 s58, 4096\n"
    "s_mov_b32 s59, 0x20000\n"
    "s_lshl_b32 s64, %[wv], 12\n"
    "s_add_u32 s61, %[lds], s64\n"
    "s_add_u32 s62, s61, 0x10000\n"
    "v_mbcnt_lo_u32_b32 v204, -1, 0\n"
    "v_mbcnt_hi_u32_b32 v204, -1, v204\n"
    "v_lshlrev_b32 v238, 4, v204\n"
    "v_bfe_u32 v208, v204, 2, 3\n"
    "v_lshrrev_b32 v209, 5, v204\n"
    "v_bfe_u32 v210, v204, 1, 1\n"
    "v_bfe_u32 v211, v204, 4, 1\n"
    "v_xor_b32 v210, v210, v211\n"
    "v_and_b32 v211, 1, v204\n"
    "v_lshlrev_b32 v212, 6, v209\n"
    "v_lshl_add_u32 v212, v210, 5, v212\n"
    "v_lshl_add_u32 v212, v211, 4, v212\n"
    "v_lshl_add_u32 v236, v208, 8, v212\n"
    "v_lshl_add_u32 v237, v208, 13, v212\n"
    "s_lshl_b32 s66, %[wv], 17\n"
    "s_lshl_b32 s60, %[wv], 12\n"
    "s_add_u32 s69, s60, 507904\n"
    "s_lshl_b32 s65, %[wv], 10\n"
    "s_add_u32 s64, s65, %[lds]\n"
    "s_add_u32 m0, s64, 131072\n"
    "s_nop 0\n"
    "buffer_load_dwordx4 v238, s[56:59], s65 offen lds\n"
    "s_add_u32 s65, s66, 0\n"
    "s_add_u32 m0, s61, 0\n"
    "s_nop 0\n"
    "buffer_load_dwordx4 v237, s[48:51], s65 offen lds\n"
    "s_add_u32 m0, s61, 896\n"
    "s_nop 0\n"
    "buffer_load_dwordx4 v237, s[48:51], s65 offen offset:128 lds\n"
    "s_add_u32 s65, s66, 65536\n"
    "s_add_u32 m0, s61, 2048\n"
    "s_nop 0\n"
    "buffer_load_dwordx4 v237, s[48:51], s65 offen lds\n"
    "s_add_u32 m0, s61, 2944\n"
    "s_nop 0\n"
    "buffer_load_dwordx4 v237, s[48:51], s65 offen offset:128 lds\n"
    "s_mov_b32 s67, 0\n"
    "buffer_load_dwordx4 v[64:67], v238, s[52:55], s67 offen\n"
    "s_mov_b32 s67, 16384\n"
    "buffer_load_dwordx4 v[68:71], v238, s[52:55], s67 offen\n"
    "s_mov_b32 s67, 32768\n"
    "buffer_load_dwordx4 v[72:75], v238, s[52:55], s67 offen\n"
    "s_mov_b32 s67, 49152\n"
    "buffer_load_dwordx4 v[76:79], v238, s[52:55], s67 offen\n"
    "s_mov_b32 s67, 0\n"
    "buffer_load_dwordx4 v[80:83], v238, s[52:55], s67 offen offset:1024\n"
    "s_mov_b32 s67, 16384\n"
    "buffer_load_dwordx4 v[84:87], v238, s[52:55], s67 offen offset:1024\n"
    "s_mov_b32 s67, 32768\n"
    "buffer_load_dwordx4 v[88:91], v238, s[52:55], s67 offen offset:1024\n"
    "s_mov_b32 s67, 49152\n"
    "buffer_load_dwordx4 v[92:95], v238, s[52:55], s67 offen offset:1024\n"
    "s_mov_b64 s[40:41], %[kc]\n"
    "s_and_b32 s41, s41, 0xffff\n"
    "s_mov_b32 s42, 524288\n"
    "s_mov_b32 s43, 0x20000\n"
    "s_mov_b64 s[44:45], %[vc]\n"
    "s_and_b32 s45, s45, 0xffff\n"
    "s_mov_b32 s46, 524288\n"
    "s_mov_b32 s47, 0x20000\n"
    "v_lshrrev_b32 v244, 4, v238\n"
    "v_and_b32 v245, 15, v244\n"
    "v_lshrrev_b32 v243, 4, v244\n"
    "v_add_u32 v240, 0, v243\n"
    "v_xor_b32 v240, v240, v245\n"
    "v_lshlrev_b32 v240, 4, v240\n"
    "v_lshl_add_u32 v240, v245, 8, v240\n"
    "v_add_u32 v224, %[lds], v240\n"
    "v_add_u32 v240, 4, v243\n"
    "v_xor_b32 v240, v240, v245\n"
    "v_lshlrev_b32 v240, 4, v240\n"
    "v_lshl_add_u32 v240, v245, 8, v240\n"
    "v_add_u32 v225, %[lds], v240\n"
    "v_add_u32 v240, 8, v243\n"
    "v_xor_b32 v240, v240, v245\n"
    "v_lshlrev_b32 v240, 4, v240\n"
    "v_lshl_add_u32 v240, v245, 8, v240\n"
    "v_add_u32 v226, %[lds], v240\n"
    "v_add_u32 v240, 12, v243\n"
    "v_xor_b32 v240, v240, v245\n"
    "v_lshlrev_b32 v240, 4, v240\n"
    "v_lshl_add_u32 v240, v245, 8, v240\n"
    "v_add_u32 v227, %[lds], v240\n"
    "v_xor_b32 v240, v245, v243\n"
    "v_xor_b32 v240, 0, v240\n"
    "v_lshlrev_b32 v240, 4, v240\n"
    "v_lshl_add_u32 v232, v243, 8, v240\n"
    "v_xor_b32 v240, v245, v243\n"
    "v_xor_b32 v240, 4, v240\n"
    "v_lshlrev_b32 v240, 4, v240\n"
    "v_lshl_add_u32 v233, v243, 8, v240\n"
    "v_xor_b32 v240, v245, v243\n"
    "v_xor_b32 v240, 8, v240\n"
    "v_lshlrev_b32 v240, 4, v240\n"
    "v_lshl_add_u32 v234, v243, 8, v240\n"
    "v_xor_b32 v240, v245, v243\n"
    "v_xor_b32 v240, 12, v240\n"
    "v_lshlrev_b32 v240, 4, v240\n"
    "v_lshl_add_u32 v235, v243, 8, v240\n"
    "v_bfe_u32 v241, v244, 2, 2\n"
    "v_and_b32 v242, 3, v244\n"
    "v_and_b32 v247, 1, v243\n"
    "v_lshrrev_b32 v223, 1, v243\n"
    "v_lshlrev_b32 v222, 11, v223\n"
    "v_lshl_add_u32 v221, v247, 2, v241\n"
    "v_lshl_add_u32 v222, v221, 6, v222\n"
    "v_xor_b32 v220, 0, v247\n"
    "v_lshl_add_u32 v220, v220, 5, v222\n"
    "v_lshl_add_u32 v220, v242, 3, v220\n"
    "v_add_u32 v220, 0x10000, v220\n"
    "v_add_u32 v228, %[lds], v220\n"
    "v_xor_b32 v220, 1, v247\n"
    "v_lshl_add_u32 v220, v220, 5, v222\n"
    "v_lshl_add_u32 v220, v242, 3, v220\n"
    "v_add_u32 v220, 0x10000, v220\n"
    "v_add_u32 v229, %[lds], v220\n"
    "v_lshrrev_b32 v220, 1, v242\n"
    "v_xor_b32 v220, v220, v247\n"
    "v_lshl_add_u32 v220, v220, 5, v222\n"
    "v_and_b32 v221, 1, v242\n"
    "v_lshl_add_u32 v220, v221, 4, v220\n"
    "v_add_u32 v230, %[lds], v220\n"
    "v_lshlrev_b32 v220, 6, v243\n"
    "v_add_u32 v220, 131072, v220\n"
    "v_add_u32 v239, %[lds], v220\n"
    "s_add_u32 s65, s66, 524288\n"
    "s_add_u32 m0, s61, 65536\n"
    "s_nop 0\n"
    "buffer_load_dwordx4 v237, s[48:51], s65 offen lds\n"
    "s_add_u32 m0, s61, 66432\n"
    "s_nop 0\n"
    "buffer_load_dwordx4 v237, s[48:51], s65 offen offset:128 lds\n"
    "s_add_u32 s65, s66, 589824\n"
    "s_add_u32 m0, s61, 67584\n"
    "s_nop 0\n"
    "buffer_load_dwordx4 v237, s[48:51], s65 offen lds\n"
    "s_add_u32 m0, s61, 68480\n"
    "s_nop 0\n"
    "buffer_load_dwordx4 v237, s[48:51], s65 offen offset:128 lds\n"
    "s_mov_b32 s67, 0\n"
    "buffer_load_dwordx4 v[96:99], v238, s[52:55], s67 offen offset:2048\n"
    "s_mov_b32 s67, 16384\n"
    "buffer_load_dwordx4 v[100:103], v238, s[52:55], s67 offen offset:2048\n"
    "s_mov_b32 s67, 32768\n"
    "buffer_load_dwordx4 v[104:107], v238, s[52:55], s67 offen offset:2048\n"
    "s_mov_b32 s67, 49152\n"
    "buffer_load_dwordx4 v[108:111], v238, s[52:55], s67 offen offset:2048\n"
    "s_mov_b32 s67, 0\n"
    "buffer_load_dwordx4 v[112:115], v238, s[52:55], s67 offen offset:3072\n"
    "s_mov_b32 s67, 16384\n"
    "buffer_load_dwordx4 v[116:119], v238, s[52:55], s67 offen offset:3072\n"
    "s_mov_b32 s67, 32768\n"
    "buffer_load_dwordx4 v[120:123], v238, s[52:55], s67 offen offset:3072\n"
    "s_mov_b32 s67, 49152\n"
    "buffer_load_dwordx4 v[124:127], v238, s[52:55], s67 offen offset:3072\n"
    "v_add_u32 v231, 0x10000, v230\n"
    "s_waitcnt vmcnt(20)\n"
    "s_barrier\n"
    "ds_read_b64_tr_b16 v[0:1], v230 offset:0\n"
    "ds_read_b64_tr_b16 v[2:3], v230 offset:4096\n"
    "ds_read_b64_tr_b16 v[4:5], v230 offset:8\n"
    "ds_read_b64_tr_b16 v[6:7], v230 offset:4104\n"
    "ds_read_b64_tr_b16 v[8:9], v230 offset:512\n"
    "ds_read_b64_tr_b16 v[10:11], v230 offset:4608\n"
    "ds_read_b64_tr_b16 v[12:13], v230 offset:520\n"
    "ds_read_b64_tr_b16 v[14:15], v230 offset:4616\n"
    "ds_read_b64_tr_b16 v[16:17], v230 offset:1024\n"
    "ds_read_b64_tr_b16 v[18:19], v230 offset:5120\n"
    "ds_read_b64_tr_b16 v[20:21], v230 offset:1032\n"
    "ds_read_b64_tr_b16 v[22:23], v230 offset:5128\n"
    "ds_read_b64_tr_b16 v[24:25], v230 offset:1536\n"
    "ds_read_b64_tr_b16 v[26:27], v230 offset:5632\n"
    "ds_read_b64_tr_b16 v[28:29], v230 offset:1544\n"
    "ds_read_b64_tr_b16 v[30:31], v230 offset:5640\n"
    "ds_read_b128 v[192:195], v239 offset:0\n"
    "ds_read_b128 v[196:199], v239 offset:16\n"
    "ds_read_b128 v[200:203], v239 offset:32\n"
    "ds_read_b128 v[204:207], v239 offset:48\n"
    "ds_read_b128 v[208:211], v239 offset:256\n"
    "ds_read_b128 v[212:215], v239 offset:272\n"
    "ds_read_b128 v[216:219], v239 offset:288\n"
    "ds_read_b128 v[220:223], v239 offset:304\n"
    "s_waitcnt vmcnt(16) lgkmcnt(0)\n"
    "v_fma_mix_f32 v240, v64, v192, v200 op_sel:[0,0,0] op_sel_hi:[1,0,0]\n"
    "v_fma_mix_f32 v241, v64, v193, v201 op_sel:[1,0,0] op_sel_hi:[1,0,0]\n"
    "v_fma_mix_f32 v242, v65, v194, v202 op_sel:[0,0,0] op_sel_hi:[1,0,0]\n"
    "v_fma_mix_f32 v243, v65, v195, v203 op_sel:[1,0,0] op_sel_hi:[1,0,0]\n"
    "v_cvt_pk_bf16_f32 v64, v240, v241\n"
    "v_cvt_pk_bf16_f32 v65, v242, v243\n"
    "v_fma_mix_f32 v244, v66, v196, v204 op_sel:[0,0,0] op_sel_hi:[1,0,0]\n"
    "v_fma_mix_f32 v245, v66, v197, v205 op_sel:[1,0,0] op_sel_hi:[1,0,0]\n"
    "v_fma_mix_f32 v246, v67, v198, v206 op_sel:[0,0,0] op_sel_hi:[1,0,0]\n"
    "v_fma_mix_f32 v247, v67, v199, v207 op_sel:[1,0,0] op_sel_hi:[1,0,0]\n"
    "v_cvt_pk_bf16_f32 v66, v244, v245\n"
    "v_cvt_pk_bf16_f32 v67, v246, v247\n"
    "v_fma_mix_f32 v240, v68, v192, v200 op_sel:[0,0,0] op_sel_hi:[1,0,0]\n"
    "v_fma_mix_f32 v241, v68, v193, v201 op_sel:[1,0,0] op_sel_hi:[1,0,0]\n"
    "v_fma_mix_f32 v242, v69, v194, v202 op_sel:[0,0,0] op_sel_hi:[1,0,0]\n"
    "v_fma_mix_f32 v243, v69, v195, v203 op_sel:[1,0,0] op_sel_hi:[1,0,0]\n"
    "v_cvt_pk_bf16_f32 v68, v240, v241\n"
    "v_cvt_pk_bf16_f32 v69, v242, v243\n"
    "v_fma_mix_f32 v244, v70, v196, v204 op_sel:[0,0,0] op_sel_hi:[1,0,0]\n"
    "v_fma_mix_f32 v245, v70, v197, v205 op_sel:[1,0,0] op_sel_hi:[1,0,0]\n"
    "v_fma_mix_f32 v246, v71, v198, v206 op_sel:[0,0,0] op_sel_hi:[1,0,0]\n"
    "v_fma_mix_f32 v247, v71, v199, v207 op_sel:[1,0,0] op_sel_hi:[1,0,0]\n"
    "v_cvt_pk_bf16_f32 v70, v244, v245\n"
    "v_cvt_pk_bf16_f32 v71, v246, v247\n"
    "v_fma_mix_f32 v240, v72, v192, v200 op_sel:[0,0,0] op_sel_hi:[1,0,0]\n"
    "v_fma_mix_f32 v241, v72, v193, v201 op_sel:[1,0,0] op_sel_hi:[1,0,0]\n"
    "v_fma_mix_f32 v242, v73, v194, v202 op_sel:[0,0,0] op_sel_hi:[1,0,0]\n"
    "v_fma_mix_f32 v243, v73, v195, v203 op_sel:[1,0,0] op_sel_hi:[1,0,0]\n"
    "v_cvt_pk_bf16_f32 v72, v240, v241\n"
    "v_cvt_pk_bf16_f32 v73, v242, v243\n"
    "v_fma_mix_f32 v244, v74, v196, v204 op_sel:[0,0,0] op_sel_hi:[1,0,0]\n"
    "v_fma_mix_f32 v245, v74, v197, v205 op_sel:[1,0,0] op_sel_hi:[1,0,0]\n"
    "v_fma_mix_f32 v246, v75, v198, v206 op_sel:[0,0,0] op_sel_hi:[1,0,0]\n"
    "v_fma_mix_f32 v247, v75, v199, v207 op_sel:[1,0,0] op_sel_hi:[1,0,0]\n"
    "v_cvt_pk_bf16_f32 v74, v244, v245\n"
    "v_cvt_pk_bf16_f32 v75, v246, v247\n"
    "v_fma_mix_f32 v240, v76, v192, v200 op_sel:[0,0,0] op_sel_hi:[1,0,0]\n"
    "v_fma_mix_f32 v241, v76, v193, v201 op_sel:[1,0,0] op_sel_hi:[1,0,0]\n"
    "v_fma_mix_f32 v242, v77, v194, v202 op_sel:[0,0,0] op_sel_hi:[1,0,0]\n"
    "v_fma_mix_f32 v243, v77, v195, v203 op_sel:[1,0,0] op_sel_hi:[1,0,0]\n"
    "v_cvt_pk_bf16_f32 v76, v240, v241\n"
    "v_cvt_pk_bf16_f32 v77, v242, v243\n"
    "v_fma_mix_f32 v244, v78, v196, v204 op_sel:[0,0,0] op_sel_hi:[1,0,0]\n"
    "v_fma_mix_f32 v245, v78, v197, v205 op_sel:[1,0,0] op_sel_hi:[1,0,0]\n"
    "v_fma_mix_f32 v246, v79, v198, v206 op_sel:[0,0,0] op_sel_hi:[1,0,0]\n"
    "v_fma_mix_f32 v247, v79, v199, v207 op_sel:[1,0,0] op_sel_hi:[1,0,0]\n"
    "v_cvt_pk_bf16_f32 v78, v244, v245\n"
    "v_cvt_pk_bf16_f32 v79, v246, v247\n"
    "s_nop 1\n"
    "s_waitcnt vmcnt(12)\n"
    "v_mfma_f32_16x16x32_bf16 a[0:3], v[0:3], v[64:67], 0\n"
    "ds_read_b128 v[192:195], v239 offset:512\n"
    "v_fma_mix_f32 v240, v80, v208, v216 op_sel:[0,0,0] op_sel_hi:[1,0,0]\n"
    "v_fma_mix_f32 v241, v80, v209, v217 op_sel:[1,0,0] op_sel_hi:[1,0,0]\n"
    "v_mfma_f32_16x16x32_bf16 a[4:7], v[0:3], v[68:71], 0\n"
    "ds_read_b128 v[196:199], v239 offset:528\n"
    "v_fma_mix_f32 v242, v81, v210, v218 op_sel:[0,0,0] op_sel_hi:[1,0,0]\n"
    "v_fma_mix_f32 v243, v81, v211, v219 op_sel:[1,0,0] op_sel_hi:[1,0,0]\n"
    "v_mfma_f32_16x16x32_bf16 a[8:11], v[0:3], v[72:75], 0\n"
    "s_add_u32 s65, s66, 1048576\n"
    "s_add_u32 m0, s61, 16384\n"
    "ds_read_b128 v[200:203], v239 offset:544\n"
    "v_cvt_pk_bf16_f32 v80, v240, v241\n"
    "v_cvt_pk_bf16_f32 v81, v242, v243\n"
    "v_mfma_f32_16x16x32_bf16 a[12:15], v[0:3], v[76:79], 0\n"
    "buffer_load_dwordx4 v237, s[48:51], s65 offen lds\n"
    "v_fma_mix_f32 v244, v82, v212, v220 op_sel:[0,0,0] op_sel_hi:[1,0,0]\n"
    "v_fma_mix_f32 v245, v82, v213, v221 op_sel:[1,0,0] op_sel_hi:[1,0,0]\n"
    "v_mfma_f32_16x16x32_bf16 a[16:19], v[4:7], v[64:67], 0\n"
    "ds_read_b128 v[204:207], v239 offset:560\n"
    "v_fma_mix_f32 v246, v83, v214, v222 op_sel:[0,0,0] op_sel_hi:[1,0,0]\n"
    "v_fma_mix_f32 v247, v83, v215, v223 op_sel:[1,0,0] op_sel_hi:[1,0,0]\n"
    "v_mfma_f32_16x16x32_bf16 a[20:23], v[4:7], v[68:71], 0\n"
    "s_add_u32 m0, s61, 17280\n"
    "ds_read_b64_tr_b16 v[32:33], v230 offset:8192\n"
    "v_cvt_pk_bf16_f32 v82, v244, v245\n"
    "v_cvt_pk_bf16_f32 v83, v246, v247\n"
    "v_mfma_f32_16x16x32_bf16 a[24:27], v[4:7], v[72:75], 0\n"
    "buffer_load_dwordx4 v237, s[48:51], s65 offen offset:128 lds\n"
    "v_fma_mix_f32 v240, v84, v208, v216 op_sel:[0,0,0] op_sel_hi:[1,0,0]\n"
    "v_fma_mix_f32 v241, v84, v209, v217 op_sel:[1,0,0] op_sel_hi:[1,0,0]\n"
    "v_mfma_f32_16x16x32_bf16 a[28:31], v[4:7], v[76:79], 0\n"
    "ds_read_b64_tr_b16 v[34:35], v230 offset:12288\n"
    "v_fma_mix_f32 v242, v85, v210, v218 op_sel:[0,0,0] op_sel_hi:[1,0,0]\n"
    "v_fma_mix_f32 v243, v85, v211, v219 op_sel:[1,0,0] op_sel_hi:[1,0,0]\n"
    "v_mfma_f32_16x16x32_bf16 a[32:35], v[8:11], v[64:67], 0\n"
    "s_add_u32 s65, s66, 1114112\n"
    "s_add_u32 m0, s61, 18432\n"
    "ds_read_b64_tr_b16 v[36:37], v230 offset:8200\n"
    "v_cvt_pk_bf16_f32 v84, v240, v241\n"
    "v_cvt_pk_bf16_f32 v85, v242, v243\n"
    "v_mfma_f32_16x16x32_bf16 a[36:39], v[8:11], v[68:71], 0\n"
    "buffer_load_dwordx4 v237, s[48:51], s65 offen lds\n"
    "v_fma_mix_f32 v244, v86, v212, v220 op_sel:[0,0,0] op_sel_hi:[1,0,0]\n"
    "v_fma_mix_f32 v245, v86, v213, v221 op_sel:[1,0,0] op_sel_hi:[1,0,0]\n"
    "v_mfma_f32_16x16x32_bf16 a[40:43], v[8:11], v[72:75], 0\n"
    "ds_read_b64_tr_b16 v[38:39], v230 offset:12296\n"
    "v_fma_mix_f32 v246, v87, v214, v222 op_sel:[0,0,0] op_sel_hi:[1,0,0]\n"
    "v_fma_mix_f32 v247, v87, v215, v223 op_sel:[1,0,0] op_sel_hi:[1,0,0]\n"
    "v_mfma_f32_16x16x32_bf16 a[44:47], v[8:11], v[76:79], 0\n"
    "s_add_u32 m0, s61, 19328\n"
    "ds_read_b64_tr_b16 v[40:41], v230 offset:8704\n"
    "v_cvt_pk_bf16_f32 v86, v244, v245\n"
    "v_cvt_pk_bf16_f32 v87, v246, v247\n"
    "v_mfma_f32_16x16x32_bf16 a[48:51], v[12:15], v[64:67], 0\n"
    "buffer_load_dwordx4 v237, s[48:51], s65 offen offset:128 lds\n"
    "v_fma_mix_f32 v240, v88, v208, v216 op_sel:[0,0,0] op_sel_hi:[1,0,0]\n"
    "v_mfma_f32_16x16x32_bf16 a[52:55], v[12:15], v[68:71], 0\n"
    "ds_read_b64_tr_b16 v[42:43], v230 offset:12800\n"
    "v_fma_mix_f32 v241, v88, v209, v217 op_sel:[1,0,0] op_sel_hi:[1,0,0]\n"
    "v_mfma_f32_16x16x32_bf16 a[56:59], v[12:15], v[72:75], 0\n"
    "s_mov_b32 s67, 4096\n"
    "ds_read_b64_tr_b16 v[44:45], v230 offset:8712\n"
    "v_fma_mix_f32 v242, v89, v210, v218 op_sel:[0,0,0] op_sel_hi:[1,0,0]\n"
    "v_mfma_f32_16x16x32_bf16 a[60:63], v[12:15], v[76:79], 0\n"
    "buffer_load_dwordx4 v[128:131], v238, s[52:55], s67 offen\n"
    "v_fma_mix_f32 v243, v89, v211, v219 op_sel:[1,0,0] op_sel_hi:[1,0,0]\n"
    "v_mfma_f32_16x16x32_bf16 a[64:67], v[16:19], v[64:67], 0\n"
    "ds_read_b64_tr_b16 v[46:47], v230 offset:12808\n"
    "v_cvt_pk_bf16_f32 v88, v240, v241\n"
    "v_mfma_f32_16x16x32_bf16 a[68:71], v[16:19], v[68:71], 0\n"
    "s_mov_b32 s67, 20480\n"
    "ds_read_b64_tr_b16 v[48:49], v230 offset:9216\n"
    "v_cvt_pk_bf16_f32 v89, v242, v243\n"
    "v_mfma_f32_16x16x32_bf16 a[72:75], v[16:19], v[72:75], 0\n"
    "buffer_load_dwordx4 v[132:135], v238, s[52:55], s67 offen\n"
    "v_fma_mix_f32 v244, v90, v212, v220 op_sel:[0,0,0] op_sel_hi:[1,0,0]\n"
    "v_mfma_f32_16x16x32_bf16 a[76:79], v[16:19], v[76:79], 0\n"
    "ds_read_b64_tr_b16 v[50:51], v230 offset:13312\n"
    "v_fma_mix_f32 v245, v90, v213, v221 op_sel:[1,0,0] op_sel_hi:[1,0,0]\n"
    "v_mfma_f32_16x16x32_bf16 a[80:83], v[20:23], v[64:67], 0\n"
    "s_mov_b32 s67, 36864\n"
    "ds_read_b64_tr_b16 v[52:53], v230 offset:9224\n"
    "v_fma_mix_f32 v246, v91, v214, v222 op_sel:[0,0,0] op_sel_hi:[1,0,0]\n"
    "v_mfma_f32_16x16x32_bf16 a[84:87], v[20:23], v[68:71], 0\n"
    "buffer_load_dwordx4 v[136:139], v238, s[52:55], s67 offen\n"
    "v_fma_mix_f32 v247, v91, v215, v223 op_sel:[1,0,0] op_sel_hi:[1,0,0]\n"
    "v_mfma_f32_16x16x32_bf16 a[88:91], v[20:23], v[72:75], 0\n"
    "ds_read_b64_tr_b16 v[54:55], v230 offset:13320\n"
    "v_cvt_pk_bf16_f32 v90, v244, v245\n"
    "v_mfma_f32_16x16x32_bf16 a[92:95], v[20:23], v[76:79], 0\n"
    "s_mov_b32 s67, 53248\n"
    "ds_read_b64_tr_b16 v[56:57], v230 offset:9728\n"
    "v_cvt_pk_bf16_f32 v91, v246, v247\n"
    "v_mfma_f32_16x16x32_bf16 a[96:99], v[24:27], v[64:67], 0\n"
    "buffer_load_dwordx4 v[140:143], v238, s[52:55], s67 offen\n"
    "v_fma_mix_f32 v240, v92, v208, v216 op_sel:[0,0,0] op_sel_hi:[1,0,0]\n"
    "v_mfma_f32_16x16x32_bf16 a[100:103], v[24:27], v[68:71], 0\n"
    "ds_read_b64_tr_b16 v[58:59], v230 offset:13824\n"
    "v_fma_mix_f32 v241, v92, v209, v217 op_sel:[1,0,0] op_sel_hi:[1,0,0]\n"
    "v_mfma_f32_16x16x32_bf16 a[104:107], v[24:27], v[72:75], 0\n"
    "ds_read_b64_tr_b16 v[60:61], v230 offset:9736\n"
    "v_fma_mix_f32 v242, v93, v210, v218 op_sel:[0,0,0] op_sel_hi:[1,0,0]\n"
    "v_mfma_f32_16x16x32_bf16 a[108:111], v[24:27], v[76:79], 0\n"
    "ds_read_b64_tr_b16 v[62:63], v230 offset:13832\n"
    "v_fma_mix_f32 v243, v93, v211, v219 op_sel:[1,0,0] op_sel_hi:[1,0,0]\n"
    "v_mfma_f32_16x16x32_bf16 a[112:115], v[28:31], v[64:67], 0\n"
    "v_cvt_pk_bf16_f32 v92, v240, v241\n"
    "v_cvt_pk_bf16_f32 v93, v242, v243\n"
    "v_mfma_f32_16x16x32_bf16 a[116:119], v[28:31], v[68:71], 0\n"
    "v_fma_mix_f32 v244, v94, v212, v220 op_sel:[0,0,0] op_sel_hi:[1,0,0]\n"
    "v_fma_mix_f32 v245, v94, v213, v221 op_sel:[1,0,0] op_sel_hi:[1,0,0]\n"
    "v_mfma_f32_16x16x32_bf16 a[120:123], v[28:31], v[72:75], 0\n"
    "v_fma_mix_f32 v246, v95, v214, v222 op_sel:[0,0,0] op_sel_hi:[1,0,0]\n"
    "v_fma_mix_f32 v247, v95, v215, v223 op_sel:[1,0,0] op_sel_hi:[1,0,0]\n"
    "v_mfma_f32_16x16x32_bf16 a[124:127], v[28:31], v[76:79], 0\n"
    "v_cvt_pk_bf16_f32 v94, v244, v245\n"
    "v_cvt_pk_bf16_f32 v95, v246, v247\n"
    "s_waitcnt lgkmcnt(0)\n"
    "s_waitcnt vmcnt(12)\n"
    "s_barrier\n"
    "v_mfma_f32_16x16x32_bf16 a[0:3], v[32:35], v[80:83], a[0:3]\n"
    "ds_read_b128 v[208:211], v239 offset:768\n"
    "v_fma_mix_f32 v240, v96, v192, v200 op_sel:[0,0,0] op_sel_hi:[1,0,0]\n"
    "v_fma_mix_f32 v241, v96, v193, v201 op_sel:[1,0,0] op_sel_hi:[1,0,0]\n"
    "v_mfma_f32_16x16x32_bf16 a[4:7], v[32:35], v[84:87], a[4:7]\n"
    "ds_read_b128 v[212:215], v239 offset:784\n"
    "v_fma_mix_f32 v242, v97, v194, v202 op_sel:[0,0,0] op_sel_hi:[1,0,0]\n"
    "v_fma_mix_f32 v243, v97, v195, v203 op_sel:[1,0,0] op_sel_hi:[1,0,0]\n"
    "v_mfma_f32_16x16x32_bf16 a[8:11], v[32:35], v[88:91], a[8:11]\n"
    "s_mov_b32 s67, 4096\n"
    "ds_read_b128 v[216:219], v239 offset:800\n"
    "v_cvt_pk_bf16_f32 v96, v240, v241\n"
    "v_cvt_pk_bf16_f32 v97, v242, v243\n"
    "v_mfma_f32_16x16x32_bf16 a[12:15], v[32:35], v[92:95], a[12:15]\n"
    "buffer_load_dwordx4 v[144:147], v238, s[52:55], s67 offen offset:1024\n"
    "v_fma_mix_f32 v244, v98, v196, v204 op_sel:[0,0,0] op_sel_hi:[1,0,0]\n"
    "v_fma_mix_f32 v245, v98, v197, v205 op_sel:[1,0,0] op_sel_hi:[1,0,0]\n"
    "v_mfma_f32_16x16x32_bf16 a[16:19], v[36:39], v[80:83], a[16:19]\n"
    "ds_read_b128 v[220:223], v239 offset:816\n"
    "v_fma_mix_f32 v246, v99, v198, v206 op_sel:[0,0,0] op_sel_hi:[1,0,0]\n"
    "v_fma_mix_f32 v247, v99, v199, v207 op_sel:[1,0,0] op_sel_hi:[1,0,0]\n"
    "v_mfma_f32_16x16x32_bf16 a[20:23], v[36:39], v[84:87], a[20:23]\n"
    "s_mov_b32 s67, 20480\n"
    "ds_read_b64_tr_b16 v[0:1], v231 offset:0\n"
    "v_cvt_pk_bf16_f32 v98, v244, v245\n"
    "v_cvt_pk_bf16_f32 v99, v246, v247\n"
    "v_mfma_f32_16x16x32_bf16 a[24:27], v[36:39], v[88:91], a[24:27]\n"
    "buffer_load_dwordx4 v[148:151], v238, s[52:55], s67 offen offset:1024\n"
    "v_fma_mix_f32 v240, v100, v192, v200 op_sel:[0,0,0] op_sel_hi:[1,0,0]\n"
    "v_fma_mix_f32 v241, v100, v193, v201 op_sel:[1,0,0] op_sel_hi:[1,0,0]\n"
    "v_mfma_f32_16x16x32_bf16 a[28:31], v[36:39], v[92:95], a[28:31]\n"
    "ds_read_b64_tr_b16 v[2:3], v231 offset:4096\n"
    "v_fma_mix_f32 v242, v101, v194, v202 op_sel:[0,0,0] op_sel_hi:[1,0,0]\n"
    "v_fma_mix_f32 v243, v101, v195, v203 op_sel:[1,0,0] op_sel_hi:[1,0,0]\n"
    "v_mfma_f32_16x16x32_bf16 a[32:35], v[40:43], v[80:83], a[32:35]\n"
    "s_mov_b32 s67, 36864\n"
    "ds_read_b64_tr_b16 v[4:5], v231 offset:8\n"
    "v_cvt_pk_bf16_f32 v100, v240, v241\n"
    "v_mfma_f32_16x16x32_bf16 a[36:39], v[40:43], v[84:87], a[36:39]\n"
    "buffer_load_dwordx4 v[152:155], v238, s[52:55], s67 offen offset:1024\n"
    "v_cvt_pk_bf16_f32 v101, v242, v243\n"
    "v_mfma_f32_16x16x32_bf16 a[40:43], v[40:43], v[88:91], a[40:43]\n"
    "ds_read_b64_tr_b16 v[6:7], v231 offset:4104\n"
    "v_fma_mix_f32 v244, v102, v196, v204 op_sel:[0,0,0] op_sel_hi:[1,0,0]\n"
    "v_mfma_f32_16x16x32_bf16 a[44:47], v[40:43], v[92:95], a[44:47]\n"
    "s_mov_b32 s67, 53248\n"
    "ds_read_b64_tr_b16 v[8:9], v231 offset:512\n"
    "v_fma_mix_f32 v245, v102, v197, v205 op_sel:[1,0,0] op_sel_hi:[1,0,0]\n"
    "v_mfma_f32_16x16x32_bf16 a[48:51], v[44:47], v[80:83], a[48:51]\n"
    "buffer_load_dwordx4 v[156:159], v238, s[52:55], s67 offen offset:1024\n"
    "v_fma_mix_f32 v246, v103, v198, v206 op_sel:[0,0,0] op_sel_hi:[1,0,0]\n"
    "v_mfma_f32_16x16x32_bf16 a[52:55], v[44:47], v[84:87], a[52:55]\n"
    "ds_read_b64_tr_b16 v[10:11], v231 offset:4608\n"
    "v_fma_mix_f32 v247, v103, v199, v207 op_sel:[1,0,0] op_sel_hi:[1,0,0]\n"
    "v_mfma_f32_16x16x32_bf16 a[56:59], v[44:47], v[88:91], a[56:59]\n"
    "ds_read_b64_tr_b16 v[12:13], v231 offset:520\n"
    "v_cvt_pk_bf16_f32 v102, v244, v245\n"
    "v_mfma_f32_16x16x32_bf16 a[60:63], v[44:47], v[92:95], a[60:63]\n"
    "ds_read_b64_tr_b16 v[14:15], v231 offset:4616\n"
    "v_cvt_pk_bf16_f32 v103, v246, v247\n"
    "v_mfma_f32_16x16x32_bf16 a[64:67], v[48:51], v[80:83], a[64:67]\n"
    "ds_read_b64_tr_b16 v[16:17], v231 offset:1024\n"
    "v_fma_mix_f32 v240, v104, v192, v200 op_sel:[0,0,0] op_sel_hi:[1,0,0]\n"
    "v_mfma_f32_16x16x32_bf16 a[68:71], v[48:51], v[84:87], a[68:71]\n"
    "ds_read_b64_tr_b16 v[18:19], v231 offset:5120\n"
    "v_fma_mix_f32 v241, v104, v193, v201 op_sel:[1,0,0] op_sel_hi:[1,0,0]\n"
    "v_mfma_f32_16x16x32_bf16 a[72:75], v[48:51], v[88:91], a[72:75]\n"
    "ds_read_b64_tr_b16 v[20:21], v231 offset:1032\n"
    "v_fma_mix_f32 v242, v105, v194, v202 op_sel:[0,0,0] op_sel_hi:[1,0,0]\n"
    "v_mfma_f32_16x16x32_bf16 a[76:79], v[48:51], v[92:95], a[76:79]\n"
    "ds_read_b64_tr_b16 v[22:23], v231 offset:5128\n"
    "v_fma_mix_f32 v243, v105, v195, v203 op_sel:[1,0,0] op_sel_hi:[1,0,0]\n"
    "v_mfma_f32_16x16x32_bf16 a[80:83], v[52:55], v[80:83], a[80:83]\n"
    "ds_read_b64_tr_b16 v[24:25], v231 offset:1536\n"
    "v_cvt_pk_bf16_f32 v104, v240, v241\n"
    "v_mfma_f32_16x16x32_bf16 a[84:87], v[52:55], v[84:87], a[84:87]\n"
    "ds_read_b64_tr_b16 v[26:27], v231 offset:5632\n"
    "v_cvt_pk_bf16_f32 v105, v242, v243\n"
    "v_mfma_f32_16x16x32_bf16 a[88:91], v[52:55], v[88:91], a[88:91]\n"
    "ds_read_b64_tr_b16 v[28:29], v231 offset:1544\n"
    "v_fma_mix_f32 v244, v106, v196, v204 op_sel:[0,0,0] op_sel_hi:[1,0,0]\n"
    "v_mfma_f32_16x16x32_bf16 a[92:95], v[52:55], v[92:95], a[92:95]\n"
    "ds_read_b64_tr_b16 v[30:31], v231 offset:5640\n"
    "v_fma_mix_f32 v245, v106, v197, v205 op_sel:[1,0,0] op_sel_hi:[1,0,0]\n"
    "v_mfma_f32_16x16x32_bf16 a[96:99], v[56:59], v[80:83], a[96:99]\n"
    "v_fma_mix_f32 v246, v107, v198, v206 op_sel:[0,0,0] op_sel_hi:[1,0,0]\n"
    "v_fma_mix_f32 v247, v107, v199, v207 op_sel:[1,0,0] op_sel_hi:[1,0,0]\n"
    "v_mfma_f32_16x16x32_bf16 a[100:103], v[56:59], v[84:87], a[100:103]\n"
    "v_cvt_pk_bf16_f32 v106, v244, v245\n"
    "v_cvt_pk_bf16_f32 v107, v246, v247\n"
    "v_mfma_f32_16x16x32_bf16 a[104:107], v[56:59], v[88:91], a[104:107]\n"
    "v_fma_mix_f32 v240, v108, v192, v200 op_sel:[0,0,0] op_sel_hi:[1,0,0]\n"
    "v_fma_mix_f32 v241, v108, v193, v201 op_sel:[1,0,0] op_sel_hi:[1,0,0]\n"
    "v_mfma_f32_16x16x32_bf16 a[108:111], v[56:59], v[92:95], a[108:111]\n"
    "v_fma_mix_f32 v242, v109, v194, v202 op_sel:[0,0,0] op_sel_hi:[1,0,0]\n"
    "v_fma_mix_f32 v243, v109, v195, v203 op_sel:[1,0,0] op_sel_hi:[1,0,0]\n"
    "v_mfma_f32_16x16x32_bf16 a[112:115], v[60:63], v[80:83], a[112:115]\n"
    "v_cvt_pk_bf16_f32 v108, v240, v241\n"
    "v_cvt_pk_bf16_f32 v109, v242, v243\n"
    "v_mfma_f32_16x16x32_bf16 a[116:119], v[60:63], v[84:87], a[116:119]\n"
    "v_fma_mix_f32 v244, v110, v196, v204 op_sel:[0,0,0] op_sel_hi:[1,0,0]\n"
    "v_fma_mix_f32 v245, v110, v197, v205 op_sel:[1,0,0] op_sel_hi:[1,0,0]\n"
    "v_mfma_f32_16x16x32_bf16 a[120:123], v[60:63], v[88:91], a[120:123]\n"
    "v_fma_mix_f32 v246, v111, v198, v206 op_sel:[0,0,0] op_sel_hi:[1,0,0]\n"
    "v_fma_mix_f32 v247, v111, v199, v207 op_sel:[1,0,0] op_sel_hi:[1,0,0]\n"
    "v_mfma_f32_16x16x32_bf16 a[124:127], v[60:63], v[92:95], a[124:127]\n"
    "v_cvt_pk_bf16_f32 v110, v244, v245\n"
    "v_cvt_pk_bf16_f32 v111, v246, v247\n"
    "s_waitcnt lgkmcnt(0)\n"
    "s_waitcnt vmcnt(12)\n"
    "v_mfma_f32_16x16x32_bf16 a[0:3], v[0:3], v[96:99], a[0:3]\n"
    "ds_read_b128 v[192:195], v239 offset:1024\n"
    "v_fma_mix_f32 v240, v112, v208, v216 op_sel:[0,0,0] op_sel_hi:[1,0,0]\n"
    "v_fma_mix_f32 v241, v112, v209, v217 op_sel:[1,0,0] op_sel_hi:[1,0,0]\n"
    "v_mfma_f32_16x16x32_bf16 a[4:7], v[0:3], v[100:103], a[4:7]\n"
    "ds_read_b128 v[196:199], v239 offset:1040\n"
    "v_fma_mix_f32 v242, v113, v210, v218 op_sel:[0,0,0] op_sel_hi:[1,0,0]\n"
    "v_fma_mix_f32 v243, v113, v211, v219 op_sel:[1,0,0] op_sel_hi:[1,0,0]\n"
    "v_mfma_f32_16x16x32_bf16 a[8:11], v[0:3], v[104:107], a[8:11]\n"
    "s_add_u32 s65, s66, 1572864\n"
    "s_add_u32 m0, s61, 81920\n"
    "ds_read_b128 v[200:203], v239 offset:1056\n"
    "v_cvt_pk_bf16_f32 v112, v240, v241\n"
    "v_cvt_pk_bf16_f32 v113, v242, v243\n"
    "v_mfma_f32_16x16x32_bf16 a[12:15], v[0:3], v[108:111], a[12:15]\n"
    "buffer_load_dwordx4 v237, s[48:51], s65 offen lds\n"
    "v_fma_mix_f32 v244, v114, v212, v220 op_sel:[0,0,0] op_sel_hi:[1,0,0]\n"
    "v_fma_mix_f32 v245, v114, v213, v221 op_sel:[1,0,0] op_sel_hi:[1,0,0]\n"
    "v_mfma_f32_16x16x32_bf16 a[16:19], v[4:7], v[96:99], a[16:19]\n"
    "ds_read_b128 v[204:207], v239 offset:1072\n"
    "v_fma_mix_f32 v246, v115, v214, v222 op_sel:[0,0,0] op_sel_hi:[1,0,0]\n"
    "v_fma_mix_f32 v247, v115, v215, v223 op_sel:[1,0,0] op_sel_hi:[1,0,0]\n"
    "v_mfma_f32_16x16x32_bf16 a[20:23], v[4:7], v[100:103], a[20:23]\n"
    "s_add_u32 m0, s61, 82816\n"
    "ds_read_b64_tr_b16 v[32:33], v231 offset:8192\n"
    "v_cvt_pk_bf16_f32 v114, v244, v245\n"
    "v_cvt_pk_bf16_f32 v115, v246, v247\n"
    "v_mfma_f32_16x16x32_bf16 a[24:27], v[4:7], v[104:107], a[24:27]\n"
    "buffer_load_dwordx4 v237, s[48:51], s65 offen offset:128 lds\n"
    "v_fma_mix_f32 v240, v116, v208, v216 op_sel:[0,0,0] op_sel_hi:[1,0,0]\n"
    "v_fma_mix_f32 v241, v116, v209, v217 op_sel:[1,0,0] op_sel_hi:[1,0,0]\n"
    "v_mfma_f32_16x16x32_bf16 a[28:31], v[4:7], v[108:111], a[28:31]\n"
    "ds_read_b64_tr_b16 v[34:35], v231 offset:12288\n"
    "v_fma_mix_f32 v242, v117, v210, v218 op_sel:[0,0,0] op_sel_hi:[1,0,0]\n"
    "v_fma_mix_f32 v243, v117, v211, v219 op_sel:[1,0,0] op_sel_hi:[1,0,0]\n"
    "v_mfma_f32_16x16x32_bf16 a[32:35], v[8:11], v[96:99], a[32:35]\n"
    "s_add_u32 s65, s66, 1638400\n"
    "s_add_u32 m0, s61, 83968\n"
    "ds_read_b64_tr_b16 v[36:37], v231 offset:8200\n"
    "v_cvt_pk_bf16_f32 v116, v240, v241\n"
    "v_cvt_pk_bf16_f32 v117, v242, v243\n"
    "v_mfma_f32_16x16x32_bf16 a[36:39], v[8:11], v[100:103], a[36:39]\n"
    "buffer_load_dwordx4 v237, s[48:51], s65 offen lds\n"
    "v_fma_mix_f32 v244, v118, v212, v220 op_sel:[0,0,0] op_sel_hi:[1,0,0]\n"
    "v_fma_mix_f32 v245, v118, v213, v221 op_sel:[1,0,0] op_sel_hi:[1,0,0]\n"
    "v_mfma_f32_16x16x32_bf16 a[40:43], v[8:11], v[104:107], a[40:43]\n"
    "ds_read_b64_tr_b16 v[38:39], v231 offset:12296\n"
    "v_fma_mix_f32 v246, v119, v214, v222 op_sel:[0,0,0] op_sel_hi:[1,0,0]\n"
    "v_fma_mix_f32 v247, v119, v215, v223 op_sel:[1,0,0] op_sel_hi:[1,0,0]\n"
    "v_mfma_f32_16x16x32_bf16 a[44:47], v[8:11], v[108:111], a[44:47]\n"
    "s_add_u32 m0, s61, 84864\n"
    "ds_read_b64_tr_b16 v[40:41], v231 offset:8704\n"
    "v_cvt_pk_bf16_f32 v118, v244, v245\n"
    "v_cvt_pk_bf16_f32 v119, v246, v247\n"
    "v_mfma_f32_16x16x32_bf16 a[48:51], v[12:15], v[96:99], a[48:51]\n"
    "buffer_load_dwordx4 v237, s[48:51], s65 offen offset:128 lds\n"
    "v_fma_mix_f32 v240, v120, v208, v216 op_sel:[0,0,0] op_sel_hi:[1,0,0]\n"
    "v_mfma_f32_16x16x32_bf16 a[52:55], v[12:15], v[100:103], a[52:55]\n"
    "ds_read_b64_tr_b16 v[42:43], v231 offset:12800\n"
    "v_fma_mix_f32 v241, v120, v209, v217 op_sel:[1,0,0] op_sel_hi:[1,0,0]\n"
    "v_mfma_f32_16x16x32_bf16 a[56:59], v[12:15], v[104:107], a[56:59]\n"
    "s_mov_b32 s67, 4096\n"
    "ds_read_b64_tr_b16 v[44:45], v231 offset:8712\n"
    "v_fma_mix_f32 v242, v121, v210, v218 op_sel:[0,0,0] op_sel_hi:[1,0,0]\n"
    "v_mfma_f32_16x16x32_bf16 a[60:63], v[12:15], v[108:111], a[60:63]\n"
    "buffer_load_dwordx4 v[160:163], v238, s[52:55], s67 offen offset:2048\n"
    "v_fma_mix_f32 v243, v121, v211, v219 op_sel:[1,0,0] op_sel_hi:[1,0,0]\n"
    "v_mfma_f32_16x16x32_bf16 a[64:67], v[16:19], v[96:99], a[64:67]\n"
    "ds_read_b64_tr_b16 v[46:47], v231 offset:12808\n"
    "v_cvt_pk_bf16_f32 v120, v240, v241\n"
    "v_mfma_f32_16x16x32_bf16 a[68:71], v[16:19], v[100:103], a[68:71]\n"
    "s_mov_b32 s67, 20480\n"
    "ds_read_b64_tr_b16 v[48:49], v231 offset:9216\n"
    "v_cvt_pk_bf16_f32 v121, v242, v243\n"
    "v_mfma_f32_16x16x32_bf16 a[72:75], v[16:19], v[104:107], a[72:75]\n"
    "buffer_load_dwordx4 v[164:167], v238, s[52:55], s67 offen offset:2048\n"
    "v_fma_mix_f32 v244, v122, v212, v220 op_sel:[0,0,0] op_sel_hi:[1,0,0]\n"
    "v_mfma_f32_16x16x32_bf16 a[76:79], v[16:19], v[108:111], a[76:79]\n"
    "ds_read_b64_tr_b16 v[50:51], v231 offset:13312\n"
    "v_fma_mix_f32 v245, v122, v213, v221 op_sel:[1,0,0] op_sel_hi:[1,0,0]\n"
    "v_mfma_f32_16x16x32_bf16 a[80:83], v[20:23], v[96:99], a[80:83]\n"
    "s_mov_b32 s67, 36864\n"
    "ds_read_b64_tr_b16 v[52:53], v231 offset:9224\n"
    "v_fma_mix_f32 v246, v123, v214, v222 op_sel:[0,0,0] op_sel_hi:[1,0,0]\n"
    "v_mfma_f32_16x16x32_bf16 a[84:87], v[20:23], v[100:103], a[84:87]\n"
    "buffer_load_dwordx4 v[168:171], v238, s[52:55], s67 offen offset:2048\n"
    "v_fma_mix_f32 v247, v123, v215, v223 op_sel:[1,0,0] op_sel_hi:[1,0,0]\n"
    "v_mfma_f32_16x16x32_bf16 a[88:91], v[20:23], v[104:107], a[88:91]\n"
    "ds_read_b64_tr_b16 v[54:55], v231 offset:13320\n"
    "v_cvt_pk_bf16_f32 v122, v244, v245\n"
    "v_mfma_f32_16x16x32_bf16 a[92:95], v[20:23], v[108:111], a[92:95]\n"
    "s_mov_b32 s67, 53248\n"
    "ds_read_b64_tr_b16 v[56:57], v231 offset:9728\n"
    "v_cvt_pk_bf16_f32 v123, v246, v247\n"
    "v_mfma_f32_16x16x32_bf16 a[96:99], v[24:27], v[96:99], a[96:99]\n"
    "buffer_load_dwordx4 v[172:175], v238, s[52:55], s67 offen offset:2048\n"
    "v_fma_mix_f32 v240, v124, v208, v216 op_sel:[0,0,0] op_sel_hi:[1,0,0]\n"
    "v_mfma_f32_16x16x32_bf16 a[100:103], v[24:27], v[100:103], a[100:103]\n"
    "ds_read_b64_tr_b16 v[58:59], v231 offset:13824\n"
    "v_fma_mix_f32 v241, v124, v209, v217 op_sel:[1,0,0] op_sel_hi:[1,0,0]\n"
    "v_mfma_f32_16x16x32_bf16 a[104:107], v[24:27], v[104:107], a[104:107]\n"
    "ds_read_b64_tr_b16 v[60:61], v231 offset:9736\n"
    "v_fma_mix_f32 v242, v125, v210, v218 op_sel:[0,0,0] op_sel_hi:[1,0,0]\n"
    "v_mfma_f32_16x16x32_bf16 a[108:111], v[24:27], v[108:111], a[108:111]\n"
    "ds_read_b64_tr_b16 v[62:63], v231 offset:13832\n"
    "v_fma_mix_f32 v243, v125, v211, v219 op_sel:[1,0,0] op_sel_hi:[1,0,0]\n"
    "v_mfma_f32_16x16x32_bf16 a[112:115], v[28:31], v[96:99], a[112:115]\n"
    "v_cvt_pk_bf16_f32 v124, v240, v241\n"
    "v_cvt_pk_bf16_f32 v125, v242, v243\n"
    "v_mfma_f32_16x16x32_bf16 a[116:119], v[28:31], v[100:103], a[116:119]\n"
    "v_fma_mix_f32 v244, v126, v212, v220 op_sel:[0,0,0] op_sel_hi:[1,0,0]\n"
    "v_fma_mix_f32 v245, v126, v213, v221 op_sel:[1,0,0] op_sel_hi:[1,0,0]\n"
    "v_mfma_f32_16x16x32_bf16 a[120:123], v[28:31], v[104:107], a[120:123]\n"
    "v_fma_mix_f32 v246, v127, v214, v222 op_sel:[0,0,0] op_sel_hi:[1,0,0]\n"
    "v_fma_mix_f32 v247, v127, v215, v223 op_sel:[1,0,0] op_sel_hi:[1,0,0]\n"
    "v_mfma_f32_16x16x32_bf16 a[124:127], v[28:31], v[108:111], a[124:127]\n"
    "v_cvt_pk_bf16_f32 v126, v244, v245\n"
    "v_cvt_pk_bf16_f32 v127, v246, v247\n"
    "s_waitcnt lgkmcnt(0)\n"
    "s_waitcnt vmcnt(12)\n"
    "s_barrier\n"
    "v_mfma_f32_16x16x32_bf16 a[0:3], v[32:35], v[112:115], a[0:3]\n"
    "ds_read_b128 v[208:211], v239 offset:1280\n"
    "v_fma_mix_f32 v240, v128, v192, v200 op_sel:[0,0,0] op_sel_hi:[1,0,0]\n"
    "v_fma_mix_f32 v241, v128, v193, v201 op_sel:[1,0,0] op_sel_hi:[1,0,0]\n"
    "v_mfma_f32_16x16x32_bf16 a[4:7], v[32:35], v[116:119], a[4:7]\n"
    "ds_read_b128 v[212:215], v239 offset:1296\n"
    "v_fma_mix_f32 v242, v129, v194, v202 op_sel:[0,0,0] op_sel_hi:[1,0,0]\n"
    "v_fma_mix_f32 v243, v129, v195, v203 op_sel:[1,0,0] op_sel_hi:[1,0,0]\n"
    "v_mfma_f32_16x16x32_bf16 a[8:11], v[32:35], v[120:123], a[8:11]\n"
    "s_mov_b32 s67, 4096\n"
    "ds_read_b128 v[216:219], v239 offset:1312\n"
    "v_cvt_pk_bf16_f32 v128, v240, v241\n"
    "v_cvt_pk_bf16_f32 v129, v242, v243\n"
    "v_mfma_f32_16x16x32_bf16 a[12:15], v[32:35], v[124:127], a[12:15]\n"
    "buffer_load_dwordx4 v[176:179], v238, s[52:55], s67 offen offset:3072\n"
    "v_fma_mix_f32 v244, v130, v196, v204 op_sel:[0,0,0] op_sel_hi:[1,0,0]\n"
    "v_fma_mix_f32 v245, v130, v197, v205 op_sel:[1,0,0] op_sel_hi:[1,0,0]\n"
    "v_mfma_f32_16x16x32_bf16 a[16:19], v[36:39], v[112:115], a[16:19]\n"
    "ds_read_b128 v[220:223], v239 offset:1328\n"
    "v_fma_mix_f32 v246, v131, v198, v206 op_sel:[0,0,0] op_sel_hi:[1,0,0]\n"
    "v_fma_mix_f32 v247, v131, v199, v207 op_sel:[1,0,0] op_sel_hi:[1,0,0]\n"
    "v_mfma_f32_16x16x32_bf16 a[20:23], v[36:39], v[116:119], a[20:23]\n"
    "s_mov_b32 s67, 20480\n"
    "ds_read_b64_tr_b16 v[0:1], v230 offset:16384\n"
    "v_cvt_pk_bf16_f32 v130, v244, v245\n"
    "v_cvt_pk_bf16_f32 v131, v246, v247\n"
    "v_mfma_f32_16x16x32_bf16 a[24:27], v[36:39], v[120:123], a[24:27]\n"
    "buffer_load_dwordx4 v[180:183], v238, s[52:55], s67 offen offset:3072\n"
    "v_fma_mix_f32 v240, v132, v192, v200 op_sel:[0,0,0] op_sel_hi:[1,0,0]\n"
    "v_fma_mix_f32 v241, v132, v193, v201 op_sel:[1,0,0] op_sel_hi:[1,0,0]\n"
    "v_mfma_f32_16x16x32_bf16 a[28:31], v[36:39], v[124:127], a[28:31]\n"
    "ds_read_b64_tr_b16 v[2:3], v230 offset:20480\n"
    "v_fma_mix_f32 v242, v133, v194, v202 op_sel:[0,0,0] op_sel_hi:[1,0,0]\n"
    "v_fma_mix_f32 v243, v133, v195, v203 op_sel:[1,0,0] op_sel_hi:[1,0,0]\n"
    "v_mfma_f32_16x16x32_bf16 a[32:35], v[40:43], v[112:115], a[32:35]\n"
    "s_mov_b32 s67, 36864\n"
    "ds_read_b64_tr_b16 v[4:5], v230 offset:16392\n"
    "v_cvt_pk_bf16_f32 v132, v240, v241\n"
    "v_mfma_f32_16x16x32_bf16 a[36:39], v[40:43], v[116:119], a[36:39]\n"
    "buffer_load_dwordx4 v[184:187], v238, s[52:55], s67 offen offset:3072\n"
    "v_cvt_pk_bf16_f32 v133, v242, v243\n"
    "v_mfma_f32_16x16x32_bf16 a[40:43], v[40:43], v[120:123], a[40:43]\n"
    "ds_read_b64_tr_b16 v[6:7], v230 offset:20488\n"
    "v_fma_mix_f32 v244, v134, v196, v204 op_sel:[0,0,0] op_sel_hi:[1,0,0]\n"
    "v_mfma_f32_16x16x32_bf16 a[44:47], v[40:43], v[124:127], a[44:47]\n"
    "s_mov_b32 s67, 53248\n"
    "ds_read_b64_tr_b16 v[8:9], v230 offset:16896\n"
    "v_fma_mix_f32 v245, v134, v197, v205 op_sel:[1,0,0] op_sel_hi:[1,0,0]\n"
    "v_mfma_f32_16x16x32_bf16 a[48:51], v[44:47], v[112:115], a[48:51]\n"
    "buffer_load_dwordx4 v[188:191], v238, s[52:55], s67 offen offset:3072\n"
    "v_fma_mix_f32 v246, v135, v198, v206 op_sel:[0,0,0] op_sel_hi:[1,0,0]\n"
    "v_mfma_f32_16x16x32_bf16 a[52:55], v[44:47], v[116:119], a[52:55]\n"
    "ds_read_b64_tr_b16 v[10:11], v230 offset:20992\n"
    "v_fma_mix_f32 v247, v135, v199, v207 op_sel:[1,0,0] op_sel_hi:[1,0,0]\n"
    "v_mfma_f32_16x16x32_bf16 a[56:59], v[44:47], v[120:123], a[56:59]\n"
    "ds_read_b64_tr_b16 v[12:13], v230 offset:16904\n"
    "v_cvt_pk_bf16_f32 v134, v244, v245\n"
    "v_mfma_f32_16x16x32_bf16 a[60:63], v[44:47], v[124:127], a[60:63]\n"
    "ds_read_b64_tr_b16 v[14:15], v230 offset:21000\n"
    "v_cvt_pk_bf16_f32 v135, v246, v247\n"
    "v_mfma_f32_16x16x32_bf16 a[64:67], v[48:51], v[112:115], a[64:67]\n"
    "ds_read_b64_tr_b16 v[16:17], v230 offset:17408\n"
    "v_fma_mix_f32 v240, v136, v192, v200 op_sel:[0,0,0] op_sel_hi:[1,0,0]\n"
    "v_mfma_f32_16x16x32_bf16 a[68:71], v[48:51], v[116:119], a[68:71]\n"
    "ds_read_b64_tr_b16 v[18:19], v230 offset:21504\n"
    "v_fma_mix_f32 v241, v136, v193, v201 op_sel:[1,0,0] op_sel_hi:[1,0,0]\n"
    "v_mfma_f32_16x16x32_bf16 a[72:75], v[48:51], v[120:123], a[72:75]\n"
    "ds_read_b64_tr_b16 v[20:21], v230 offset:17416\n"
    "v_fma_mix_f32 v242, v137, v194, v202 op_sel:[0,0,0] op_sel_hi:[1,0,0]\n"
    "v_mfma_f32_16x16x32_bf16 a[76:79], v[48:51], v[124:127], a[76:79]\n"
    "ds_read_b64_tr_b16 v[22:23], v230 offset:21512\n"
    "v_fma_mix_f32 v243, v137, v195, v203 op_sel:[1,0,0] op_sel_hi:[1,0,0]\n"
    "v_mfma_f32_16x16x32_bf16 a[80:83], v[52:55], v[112:115], a[80:83]\n"
    "ds_read_b64_tr_b16 v[24:25], v230 offset:17920\n"
    "v_cvt_pk_bf16_f32 v136, v240, v241\n"
    "v_mfma_f32_16x16x32_bf16 a[84:87], v[52:55], v[116:119], a[84:87]\n"
    "ds_read_b64_tr_b16 v[26:27], v230 offset:22016\n"
    "v_cvt_pk_bf16_f32 v137, v242, v243\n"
    "v_mfma_f32_16x16x32_bf16 a[88:91], v[52:55], v[120:123], a[88:91]\n"
    "ds_read_b64_tr_b16 v[28:29], v230 offset:17928\n"
    "v_fma_mix_f32 v244, v138, v196, v204 op_sel:[0,0,0] op_sel_hi:[1,0,0]\n"
    "v_mfma_f32_16x16x32_bf16 a[92:95], v[52:55], v[124:127], a[92:95]\n"
    "ds_read_b64_tr_b16 v[30:31], v230 offset:22024\n"
    "v_fma_mix_f32 v245, v138, v197, v205 op_sel:[1,0,0] op_sel_hi:[1,0,0]\n"
    "v_mfma_f32_16x16x32_bf16 a[96:99], v[56:59], v[112:115], a[96:99]\n"
    "v_fma_mix_f32 v246, v139, v198, v206 op_sel:[0,0,0] op_sel_hi:[1,0,0]\n"
    "v_fma_mix_f32 v247, v139, v199, v207 op_sel:[1,0,0] op_sel_hi:[1,0,0]\n"
    "v_mfma_f32_16x16x32_bf16 a[100:103], v[56:59], v[116:119], a[100:103]\n"
    "v_cvt_pk_bf16_f32 v138, v244, v245\n"
    "v_cvt_pk_bf16_f32 v139, v246, v247\n"
    "v_mfma_f32_16x16x32_bf16 a[104:107], v[56:59], v[120:123], a[104:107]\n"
    "v_fma_mix_f32 v240, v140, v192, v200 op_sel:[0,0,0] op_sel_hi:[1,0,0]\n"
    "v_fma_mix_f32 v241, v140, v193, v201 op_sel:[1,0,0] op_sel_hi:[1,0,0]\n"
    "v_mfma_f32_16x16x32_bf16 a[108:111], v[56:59], v[124:127], a[108:111]\n"
    "v_fma_mix_f32 v242, v141, v194, v202 op_sel:[0,0,0] op_sel_hi:[1,0,0]\n"
    "v_fma_mix_f32 v243, v141, v195, v203 op_sel:[1,0,0] op_sel_hi:[1,0,0]\n"
    "v_mfma_f32_16x16x32_bf16 a[112:115], v[60:63], v[112:115], a[112:115]\n"
    "v_cvt_pk_bf16_f32 v140, v240, v241\n"
    "v_cvt_pk_bf16_f32 v141, v242, v243\n"
    "v_mfma_f32_16x16x32_bf16 a[116:119], v[60:63], v[116:119], a[116:119]\n"
    "v_fma_mix_f32 v244, v142, v196, v204 op_sel:[0,0,0] op_sel_hi:[1,0,0]\n"
    "v_fma_mix_f32 v245, v142, v197, v205 op_sel:[1,0,0] op_sel_hi:[1,0,0]\n"
    "v_mfma_f32_16x16x32_bf16 a[120:123], v[60:63], v[120:123], a[120:123]\n"
    "v_fma_mix_f32 v246, v143, v198, v206 op_sel:[0,0,0] op_sel_hi:[1,0,0]\n"
    "v_fma_mix_f32 v247, v143, v199, v207 op_sel:[1,0,0] op_sel_hi:[1,0,0]\n"
    "v_mfma_f32_16x16x32_bf16 a[124:127], v[60:63], v[124:127], a[124:127]\n"
    "v_cvt_pk_bf16_f32 v142, v244, v245\n"
    "v_cvt_pk_bf16_f32 v143, v246, v247\n"
    "s_waitcnt lgkmcnt(0)\n"
    "s_waitcnt vmcnt(12)\n"
    "v_mfma_f32_16x16x32_bf16 a[0:3], v[0:3], v[128:131], a[0:3]\n"
    "ds_read_b128 v[192:195], v239 offset:1536\n"
    "v_fma_mix_f32 v240, v144, v208, v216 op_sel:[0,0,0] op_sel_hi:[1,0,0]\n"
    "v_fma_mix_f32 v241, v144, v209, v217 op_sel:[1,0,0] op_sel_hi:[1,0,0]\n"
    "v_mfma_f32_16x16x32_bf16 a[4:7], v[0:3], v[132:135], a[4:7]\n"
    "ds_read_b128 v[196:199], v239 offset:1552\n"
    "v_fma_mix_f32 v242, v145, v210, v218 op_sel:[0,0,0] op_sel_hi:[1,0,0]\n"
    "v_fma_mix_f32 v243, v145, v211, v219 op_sel:[1,0,0] op_sel_hi:[1,0,0]\n"
    "v_mfma_f32_16x16x32_bf16 a[8:11], v[0:3], v[136:139], a[8:11]\n"
    "s_add_u32 s65, s66, 2097152\n"
    "s_add_u32 m0, s61, 32768\n"
    "ds_read_b128 v[200:203], v239 offset:1568\n"
    "v_cvt_pk_bf16_f32 v144, v240, v241\n"
    "v_cvt_pk_bf16_f32 v145, v242, v243\n"
    "v_mfma_f32_16x16x32_bf16 a[12:15], v[0:3], v[140:143], a[12:15]\n"
    "buffer_load_dwordx4 v237, s[48:51], s65 offen lds\n"
    "v_fma_mix_f32 v244, v146, v212, v220 op_sel:[0,0,0] op_sel_hi:[1,0,0]\n"
    "v_fma_mix_f32 v245, v146, v213, v221 op_sel:[1,0,0] op_sel_hi:[1,0,0]\n"
    "v_mfma_f32_16x16x32_bf16 a[16:19], v[4:7], v[128:131], a[16:19]\n"
    "ds_read_b128 v[204:207], v239 offset:1584\n"
    "v_fma_mix_f32 v246, v147, v214, v222 op_sel:[0,0,0] op_sel_hi:[1,0,0]\n"
    "v_fma_mix_f32 v247, v147, v215, v223 op_sel:[1,0,0] op_sel_hi:[1,0,0]\n"
    "v_mfma_f32_16x16x32_bf16 a[20:23], v[4:7], v[132:135], a[20:23]\n"
    "s_add_u32 m0, s61, 33664\n"
    "ds_read_b64_tr_b16 v[32:33], v230 offset:24576\n"
    "v_cvt_pk_bf16_f32 v146, v244, v245\n"
    "v_cvt_pk_bf16_f32 v147, v246, v247\n"
    "v_mfma_f32_16x16x32_bf16 a[24:27], v[4:7], v[136:139], a[24:27]\n"
    "buffer_load_dwordx4 v237, s[48:51], s65 offen offset:128 lds\n"
    "v_fma_mix_f32 v240, v148, v208, v216 op_sel:[0,0,0] op_sel_hi:[1,0,0]\n"
    "v_fma_mix_f32 v241, v148, v209, v217 op_sel:[1,0,0] op_sel_hi:[1,0,0]\n"
    "v_mfma_f32_16x16x32_bf16 a[28:31], v[4:7], v[140:143], a[28:31]\n"
    "ds_read_b64_tr_b16 v[34:35], v230 offset:28672\n"
    "v_fma_mix_f32 v242, v149, v210, v218 op_sel:[0,0,0] op_sel_hi:[1,0,0]\n"
    "v_fma_mix_f32 v243, v149, v211, v219 op_sel:[1,0,0] op_sel_hi:[1,0,0]\n"
    "v_mfma_f32_16x16x32_bf16 a[32:35], v[8:11], v[128:131], a[32:35]\n"
    "s_add_u32 s65, s66, 2162688\n"
    "s_add_u32 m0, s61, 34816\n"
    "ds_read_b64_tr_b16 v[36:37], v230 offset:24584\n"
    "v_cvt_pk_bf16_f32 v148, v240, v241\n"
    "v_cvt_pk_bf16_f32 v149, v242, v243\n"
    "v_mfma_f32_16x16x32_bf16 a[36:39], v[8:11], v[132:135], a[36:39]\n"
    "buffer_load_dwordx4 v237, s[48:51], s65 offen lds\n"
    "v_fma_mix_f32 v244, v150, v212, v220 op_sel:[0,0,0] op_sel_hi:[1,0,0]\n"
    "v_fma_mix_f32 v245, v150, v213, v221 op_sel:[1,0,0] op_sel_hi:[1,0,0]\n"
    "v_mfma_f32_16x16x32_bf16 a[40:43], v[8:11], v[136:139], a[40:43]\n"
    "ds_read_b64_tr_b16 v[38:39], v230 offset:28680\n"
    "v_fma_mix_f32 v246, v151, v214, v222 op_sel:[0,0,0] op_sel_hi:[1,0,0]\n"
    "v_fma_mix_f32 v247, v151, v215, v223 op_sel:[1,0,0] op_sel_hi:[1,0,0]\n"
    "v_mfma_f32_16x16x32_bf16 a[44:47], v[8:11], v[140:143], a[44:47]\n"
    "s_add_u32 m0, s61, 35712\n"
    "ds_read_b64_tr_b16 v[40:41], v230 offset:25088\n"
    "v_cvt_pk_bf16_f32 v150, v244, v245\n"
    "v_cvt_pk_bf16_f32 v151, v246, v247\n"
    "v_mfma_f32_16x16x32_bf16 a[48:51], v[12:15], v[128:131], a[48:51]\n"
    "buffer_load_dwordx4 v237, s[48:51], s65 offen offset:128 lds\n"
    "v_fma_mix_f32 v240, v152, v208, v216 op_sel:[0,0,0] op_sel_hi:[1,0,0]\n"
    "v_mfma_f32_16x16x32_bf16 a[52:55], v[12:15], v[132:135], a[52:55]\n"
    "ds_read_b64_tr_b16 v[42:43], v230 offset:29184\n"
    "v_fma_mix_f32 v241, v152, v209, v217 op_sel:[1,0,0] op_sel_hi:[1,0,0]\n"
    "v_mfma_f32_16x16x32_bf16 a[56:59], v[12:15], v[136:139], a[56:59]\n"
    "s_mov_b32 s67, 8192\n"
    "ds_read_b64_tr_b16 v[44:45], v230 offset:25096\n"
    "v_fma_mix_f32 v242, v153, v210, v218 op_sel:[0,0,0] op_sel_hi:[1,0,0]\n"
    "v_mfma_f32_16x16x32_bf16 a[60:63], v[12:15], v[140:143], a[60:63]\n"
    "buffer_load_dwordx4 v[64:67], v238, s[52:55], s67 offen\n"
    "v_fma_mix_f32 v243, v153, v211, v219 op_sel:[1,0,0] op_sel_hi:[1,0,0]\n"
    "v_mfma_f32_16x16x32_bf16 a[64:67], v[16:19], v[128:131], a[64:67]\n"
    "ds_read_b64_tr_b16 v[46:47], v230 offset:29192\n"
    "v_cvt_pk_bf16_f32 v152, v240, v241\n"
    "v_mfma_f32_16x16x32_bf16 a[68:71], v[16:19], v[132:135], a[68:71]\n"
    "s_mov_b32 s67, 24576\n"
    "ds_read_b64_tr_b16 v[48:49], v230 offset:25600\n"
    "v_cvt_pk_bf16_f32 v153, v242, v243\n"
    "v_mfma_f32_16x16x32_bf16 a[72:75], v[16:19], v[136:139], a[72:75]\n"
    "buffer_load_dwordx4 v[68:71], v238, s[52:55], s67 offen\n"
    "v_fma_mix_f32 v244, v154, v212, v220 op_sel:[0,0,0] op_sel_hi:[1,0,0]\n"
    "v_mfma_f32_16x16x32_bf16 a[76:79], v[16:19], v[140:143], a[76:79]\n"
    "ds_read_b64_tr_b16 v[50:51], v230 offset:29696\n"
    "v_fma_mix_f32 v245, v154, v213, v221 op_sel:[1,0,0] op_sel_hi:[1,0,0]\n"
    "v_mfma_f32_16x16x32_bf16 a[80:83], v[20:23], v[128:131], a[80:83]\n"
    "s_mov_b32 s67, 40960\n"
    "ds_read_b64_tr_b16 v[52:53], v230 offset:25608\n"
    "v_fma_mix_f32 v246, v155, v214, v222 op_sel:[0,0,0] op_sel_hi:[1,0,0]\n"
    "v_mfma_f32_16x16x32_bf16 a[84:87], v[20:23], v[132:135], a[84:87]\n"
    "buffer_load_dwordx4 v[72:75], v238, s[52:55], s67 offen\n"
    "v_fma_mix_f32 v247, v155, v215, v223 op_sel:[1,0,0] op_sel_hi:[1,0,0]\n"
    "v_mfma_f32_16x16x32_bf16 a[88:91], v[20:23], v[136:139], a[88:91]\n"
    "ds_read_b64_tr_b16 v[54:55], v230 offset:29704\n"
    "v_cvt_pk_bf16_f32 v154, v244, v245\n"
    "v_mfma_f32_16x16x32_bf16 a[92:95], v[20:23], v[140:143], a[92:95]\n"
    "s_mov_b32 s67, 57344\n"
    "ds_read_b64_tr_b16 v[56:57], v230 offset:26112\n"
    "v_cvt_pk_bf16_f32 v155, v246, v247\n"
    "v_mfma_f32_16x16x32_bf16 a[96:99], v[24:27], v[128:131], a[96:99]\n"
    "buffer_load_dwordx4 v[76:79], v238, s[52:55], s67 offen\n"
    "v_fma_mix_f32 v240, v156, v208, v216 op_sel:[0,0,0] op_sel_hi:[1,0,0]\n"
    "v_mfma_f32_16x16x32_bf16 a[100:103], v[24:27], v[132:135], a[100:103]\n"
    "ds_read_b64_tr_b16 v[58:59], v230 offset:30208\n"
    "v_fma_mix_f32 v241, v156, v209, v217 op_sel:[1,0,0] op_sel_hi:[1,0,0]\n"
    "v_mfma_f32_16x16x32_bf16 a[104:107], v[24:27], v[136:139], a[104:107]\n"
    "ds_read_b64_tr_b16 v[60:61], v230 offset:26120\n"
    "v_fma_mix_f32 v242, v157, v210, v218 op_sel:[0,0,0] op_sel_hi:[1,0,0]\n"
    "v_mfma_f32_16x16x32_bf16 a[108:111], v[24:27], v[140:143], a[108:111]\n"
    "ds_read_b64_tr_b16 v[62:63], v230 offset:30216\n"
    "v_fma_mix_f32 v243, v157, v211, v219 op_sel:[1,0,0] op_sel_hi:[1,0,0]\n"
    "v_mfma_f32_16x16x32_bf16 a[112:115], v[28:31], v[128:131], a[112:115]\n"
    "v_cvt_pk_bf16_f32 v156, v240, v241\n"
    "v_cvt_pk_bf16_f32 v157, v242, v243\n"
    "v_mfma_f32_16x16x32_bf16 a[116:119], v[28:31], v[132:135], a[116:119]\n"
    "v_fma_mix_f32 v244, v158, v212, v220 op_sel:[0,0,0] op_sel_hi:[1,0,0]\n"
    "v_fma_mix_f32 v245, v158, v213, v221 op_sel:[1,0,0] op_sel_hi:[1,0,0]\n"
    "v_mfma_f32_16x16x32_bf16 a[120:123], v[28:31], v[136:139], a[120:123]\n"
    "v_fma_mix_f32 v246, v159, v214, v222 op_sel:[0,0,0] op_sel_hi:[1,0,0]\n"
    "v_fma_mix_f32 v247, v159, v215, v223 op_sel:[1,0,0] op_sel_hi:[1,0,0]\n"
    "v_mfma_f32_16x16x32_bf16 a[124:127], v[28:31], v[140:143], a[124:127]\n"
    "v_cvt_pk_bf16_f32 v158, v244, v245\n"
    "v_cvt_pk_bf16_f32 v159, v246, v247\n"
    "s_waitcnt lgkmcnt(0)\n"
    "s_waitcnt vmcnt(12)\n"
    "s_barrier\n"
    "s_add_u32 s68, s60, 0\n"
    "v_mfma_f32_16x16x32_bf16 a[0:3], v[32:35], v[144:147], a[0:3]\n"
    "ds_read_b128 v[208:211], v239 offset:1792\n"
    "v_fma_mix_f32 v240, v160, v192, v200 op_sel:[0,0,0] op_sel_hi:[1,0,0]\n"
    "v_fma_mix_f32 v241, v160, v193, v201 op_sel:[1,0,0] op_sel_hi:[1,0,0]\n"
    "v_mfma_f32_16x16x32_bf16 a[4:7], v[32:35], v[148:151], a[4:7]\n"
    "ds_read_b128 v[212:215], v239 offset:1808\n"
    "v_fma_mix_f32 v242, v161, v194, v202 op_sel:[0,0,0] op_sel_hi:[1,0,0]\n"
    "v_fma_mix_f32 v243, v161, v195, v203 op_sel:[1,0,0] op_sel_hi:[1,0,0]\n"
    "v_mfma_f32_16x16x32_bf16 a[8:11], v[32:35], v[152:155], a[8:11]\n"
    "s_add_u32 m0, s61, 0\n"
    "ds_read_b128 v[216:219], v239 offset:1824\n"
    "v_cvt_pk_bf16_f32 v160, v240, v241\n"
    "v_cvt_pk_bf16_f32 v161, v242, v243\n"
    "v_mfma_f32_16x16x32_bf16 a[12:15], v[32:35], v[156:159], a[12:15]\n"
    "buffer_load_dwordx4 v232, s[40:43], s68 offen lds\n"
    "v_fma_mix_f32 v244, v162, v196, v204 op_sel:[0,0,0] op_sel_hi:[1,0,0]\n"
    "v_fma_mix_f32 v245, v162, v197, v205 op_sel:[1,0,0] op_sel_hi:[1,0,0]\n"
    "v_mfma_f32_16x16x32_bf16 a[16:19], v[36:39], v[144:147], a[16:19]\n"
    "ds_read_b128 v[220:223], v239 offset:1840\n"
    "v_fma_mix_f32 v246, v163, v198, v206 op_sel:[0,0,0] op_sel_hi:[1,0,0]\n"
    "v_fma_mix_f32 v247, v163, v199, v207 op_sel:[1,0,0] op_sel_hi:[1,0,0]\n"
    "v_mfma_f32_16x16x32_bf16 a[20:23], v[36:39], v[148:151], a[20:23]\n"
    "buffer_load_dwordx4 v233, s[40:43], s68 offen offset:1024 lds\n"
    "v_cvt_pk_bf16_f32 v162, v244, v245\n"
    "v_cvt_pk_bf16_f32 v163, v246, v247\n"
    "v_mfma_f32_16x16x32_bf16 a[24:27], v[36:39], v[152:155], a[24:27]\n"
    "ds_read_b64_tr_b16 v[0:1], v231 offset:16384\n"
    "v_fma_mix_f32 v240, v164, v192, v200 op_sel:[0,0,0] op_sel_hi:[1,0,0]\n"
    "v_fma_mix_f32 v241, v164, v193, v201 op_sel:[1,0,0] op_sel_hi:[1,0,0]\n"
    "v_mfma_f32_16x16x32_bf16 a[28:31], v[36:39], v[156:159], a[28:31]\n"
    "ds_read_b64_tr_b16 v[2:3], v231 offset:20480\n"
    "v_fma_mix_f32 v242, v165, v194, v202 op_sel:[0,0,0] op_sel_hi:[1,0,0]\n"
    "v_fma_mix_f32 v243, v165, v195, v203 op_sel:[1,0,0] op_sel_hi:[1,0,0]\n"
    "v_mfma_f32_16x16x32_bf16 a[32:35], v[40:43], v[144:147], a[32:35]\n"
    "buffer_load_dwordx4 v234, s[40:43], s68 offen offset:2048 lds\n"
    "v_cvt_pk_bf16_f32 v164, v240, v241\n"
    "v_cvt_pk_bf16_f32 v165, v242, v243\n"
    "v_mfma_f32_16x16x32_bf16 a[36:39], v[40:43], v[148:151], a[36:39]\n"
    "ds_read_b64_tr_b16 v[4:5], v231 offset:16392\n"
    "v_fma_mix_f32 v244, v166, v196, v204 op_sel:[0,0,0] op_sel_hi:[1,0,0]\n"
    "v_fma_mix_f32 v245, v166, v197, v205 op_sel:[1,0,0] op_sel_hi:[1,0,0]\n"
    "v_mfma_f32_16x16x32_bf16 a[40:43], v[40:43], v[152:155], a[40:43]\n"
    "ds_read_b64_tr_b16 v[6:7], v231 offset:20488\n"
    "v_fma_mix_f32 v246, v167, v198, v206 op_sel:[0,0,0] op_sel_hi:[1,0,0]\n"
    "v_fma_mix_f32 v247, v167, v199, v207 op_sel:[1,0,0] op_sel_hi:[1,0,0]\n"
    "v_mfma_f32_16x16x32_bf16 a[44:47], v[40:43], v[156:159], a[44:47]\n"
    "buffer_load_dwordx4 v235, s[40:43], s68 offen offset:3072 lds\n"
    "v_cvt_pk_bf16_f32 v166, v244, v245\n"
    "v_cvt_pk_bf16_f32 v167, v246, v247\n"
    "v_mfma_f32_16x16x32_bf16 a[48:51], v[44:47], v[144:147], a[48:51]\n"
    "ds_read_b64_tr_b16 v[8:9], v231 offset:16896\n"
    "v_fma_mix_f32 v240, v168, v192, v200 op_sel:[0,0,0] op_sel_hi:[1,0,0]\n"
    "v_mfma_f32_16x16x32_bf16 a[52:55], v[44:47], v[148:151], a[52:55]\n"
    "ds_read_b64_tr_b16 v[10:11], v231 offset:20992\n"
    "v_fma_mix_f32 v241, v168, v193, v201 op_sel:[1,0,0] op_sel_hi:[1,0,0]\n"
    "v_mfma_f32_16x16x32_bf16 a[56:59], v[44:47], v[152:155], a[56:59]\n"
    "s_mov_b32 s67, 8192\n"
    "ds_read_b64_tr_b16 v[12:13], v231 offset:16904\n"
    "v_fma_mix_f32 v242, v169, v194, v202 op_sel:[0,0,0] op_sel_hi:[1,0,0]\n"
    "v_mfma_f32_16x16x32_bf16 a[60:63], v[44:47], v[156:159], a[60:63]\n"
    "buffer_load_dwordx4 v[80:83], v238, s[52:55], s67 offen offset:1024\n"
    "v_fma_mix_f32 v243, v169, v195, v203 op_sel:[1,0,0] op_sel_hi:[1,0,0]\n"
    "v_mfma_f32_16x16x32_bf16 a[64:67], v[48:51], v[144:147], a[64:67]\n"
    "ds_read_b64_tr_b16 v[14:15], v231 offset:21000\n"
    "v_cvt_pk_bf16_f32 v168, v240, v241\n"
    "v_mfma_f32_16x16x32_bf16 a[68:71], v[48:51], v[148:151], a[68:71]\n"
    "s_mov_b32 s67, 24576\n"
    "ds_read_b64_tr_b16 v[16:17], v231 offset:17408\n"
    "v_cvt_pk_bf16_f32 v169, v242, v243\n"
    "v_mfma_f32_16x16x32_bf16 a[72:75], v[48:51], v[152:155], a[72:75]\n"
    "buffer_load_dwordx4 v[84:87], v238, s[52:55], s67 offen offset:1024\n"
    "v_fma_mix_f32 v244, v170, v196, v204 op_sel:[0,0,0] op_sel_hi:[1,0,0]\n"
    "v_mfma_f32_16x16x32_bf16 a[76:79], v[48:51], v[156:159], a[76:79]\n"
    "ds_read_b64_tr_b16 v[18:19], v231 offset:21504\n"
    "v_fma_mix_f32 v245, v170, v197, v205 op_sel:[1,0,0] op_sel_hi:[1,0,0]\n"
    "v_mfma_f32_16x16x32_bf16 a[80:83], v[52:55], v[144:147], a[80:83]\n"
    "s_mov_b32 s67, 40960\n"
    "ds_read_b64_tr_b16 v[20:21], v231 offset:17416\n"
    "v_fma_mix_f32 v246, v171, v198, v206 op_sel:[0,0,0] op_sel_hi:[1,0,0]\n"
    "v_mfma_f32_16x16x32_bf16 a[84:87], v[52:55], v[148:151], a[84:87]\n"
    "buffer_load_dwordx4 v[88:91], v238, s[52:55], s67 offen offset:1024\n"
    "v_fma_mix_f32 v247, v171, v199, v207 op_sel:[1,0,0] op_sel_hi:[1,0,0]\n"
    "v_mfma_f32_16x16x32_bf16 a[88:91], v[52:55], v[152:155], a[88:91]\n"
    "ds_read_b64_tr_b16 v[22:23], v231 offset:21512\n"
    "v_cvt_pk_bf16_f32 v170, v244, v245\n"
    "v_mfma_f32_16x16x32_bf16 a[92:95], v[52:55], v[156:159], a[92:95]\n"
    "s_mov_b32 s67, 57344\n"
    "ds_read_b64_tr_b16 v[24:25], v231 offset:17920\n"
    "v_cvt_pk_bf16_f32 v171, v246, v247\n"
    "v_mfma_f32_16x16x32_bf16 a[96:99], v[56:59], v[144:147], a[96:99]\n"
    "buffer_load_dwordx4 v[92:95], v238, s[52:55], s67 offen offset:1024\n"
    "v_fma_mix_f32 v240, v172, v192, v200 op_sel:[0,0,0] op_sel_hi:[1,0,0]\n"
    "v_mfma_f32_16x16x32_bf16 a[100:103], v[56:59], v[148:151], a[100:103]\n"
    "ds_read_b64_tr_b16 v[26:27], v231 offset:22016\n"
    "v_fma_mix_f32 v241, v172, v193, v201 op_sel:[1,0,0] op_sel_hi:[1,0,0]\n"
    "v_mfma_f32_16x16x32_bf16 a[104:107], v[56:59], v[152:155], a[104:107]\n"
    "ds_read_b64_tr_b16 v[28:29], v231 offset:17928\n"
    "v_fma_mix_f32 v242, v173, v194, v202 op_sel:[0,0,0] op_sel_hi:[1,0,0]\n"
    "v_mfma_f32_16x16x32_bf16 a[108:111], v[56:59], v[156:159], a[108:111]\n"
    "ds_read_b64_tr_b16 v[30:31], v231 offset:22024\n"
    "v_fma_mix_f32 v243, v173, v195, v203 op_sel:[1,0,0] op_sel_hi:[1,0,0]\n"
    "v_mfma_f32_16x16x32_bf16 a[112:115], v[60:63], v[144:147], a[112:115]\n"
    "v_cvt_pk_bf16_f32 v172, v240, v241\n"
    "v_cvt_pk_bf16_f32 v173, v242, v243\n"
    "v_mfma_f32_16x16x32_bf16 a[116:119], v[60:63], v[148:151], a[116:119]\n"
    "v_fma_mix_f32 v244, v174, v196, v204 op_sel:[0,0,0] op_sel_hi:[1,0,0]\n"
    "v_fma_mix_f32 v245, v174, v197, v205 op_sel:[1,0,0] op_sel_hi:[1,0,0]\n"
    "v_mfma_f32_16x16x32_bf16 a[120:123], v[60:63], v[152:155], a[120:123]\n"
    "v_fma_mix_f32 v246, v175, v198, v206 op_sel:[0,0,0] op_sel_hi:[1,0,0]\n"
    "v_fma_mix_f32 v247, v175, v199, v207 op_sel:[1,0,0] op_sel_hi:[1,0,0]\n"
    "v_mfma_f32_16x16x32_bf16 a[124:127], v[60:63], v[156:159], a[124:127]\n"
    "v_cvt_pk_bf16_f32 v174, v244, v245\n"
    "v_cvt_pk_bf16_f32 v175, v246, v247\n"
    "s_waitcnt lgkmcnt(0)\n"
    "s_waitcnt vmcnt(16)\n"
    "v_mfma_f32_16x16x32_bf16 a[0:3], v[0:3], v[160:163], a[0:3]\n"
    "ds_read_b128 v[192:195], v239 offset:2048\n"
    "v_fma_mix_f32 v240, v176, v208, v216 op_sel:[0,0,0] op_sel_hi:[1,0,0]\n"
    "v_fma_mix_f32 v241, v176, v209, v217 op_sel:[1,0,0] op_sel_hi:[1,0,0]\n"
    "v_mfma_f32_16x16x32_bf16 a[4:7], v[0:3], v[164:167], a[4:7]\n"
    "ds_read_b128 v[196:199], v239 offset:2064\n"
    "v_fma_mix_f32 v242, v177, v210, v218 op_sel:[0,0,0] op_sel_hi:[1,0,0]\n"
    "v_fma_mix_f32 v243, v177, v211, v219 op_sel:[1,0,0] op_sel_hi:[1,0,0]\n"
    "v_mfma_f32_16x16x32_bf16 a[8:11], v[0:3], v[168:171], a[8:11]\n"
    "s_add_u32 s65, s66, 2621440\n"
    "s_add_u32 m0, s61, 98304\n"
    "ds_read_b128 v[200:203], v239 offset:2080\n"
    "v_cvt_pk_bf16_f32 v176, v240, v241\n"
    "v_cvt_pk_bf16_f32 v177, v242, v243\n"
    "v_mfma_f32_16x16x32_bf16 a[12:15], v[0:3], v[172:175], a[12:15]\n"
    "buffer_load_dwordx4 v237, s[48:51], s65 offen lds\n"
    "v_fma_mix_f32 v244, v178, v212, v220 op_sel:[0,0,0] op_sel_hi:[1,0,0]\n"
    "v_fma_mix_f32 v245, v178, v213, v221 op_sel:[1,0,0] op_sel_hi:[1,0,0]\n"
    "v_mfma_f32_16x16x32_bf16 a[16:19], v[4:7], v[160:163], a[16:19]\n"
    "ds_read_b128 v[204:207], v239 offset:2096\n"
    "v_fma_mix_f32 v246, v179, v214, v222 op_sel:[0,0,0] op_sel_hi:[1,0,0]\n"
    "v_fma_mix_f32 v247, v179, v215, v223 op_sel:[1,0,0] op_sel_hi:[1,0,0]\n"
    "v_mfma_f32_16x16x32_bf16 a[20:23], v[4:7], v[164:167], a[20:23]\n"
    "s_add_u32 m0, s61, 99200\n"
    "ds_read_b64_tr_b16 v[32:33], v231 offset:24576\n"
    "v_cvt_pk_bf16_f32 v178, v244, v245\n"
    "v_cvt_pk_bf16_f32 v179, v246, v247\n"
    "v_mfma_f32_16x16x32_bf16 a[24:27], v[4:7], v[168:171], a[24:27]\n"
    "buffer_load_dwordx4 v237, s[48:51], s65 offen offset:128 lds\n"
    "v_fma_mix_f32 v240, v180, v208, v216 op_sel:[0,0,0] op_sel_hi:[1,0,0]\n"
    "v_fma_mix_f32 v241, v180, v209, v217 op_sel:[1,0,0] op_sel_hi:[1,0,0]\n"
    "v_mfma_f32_16x16x32_bf16 a[28:31], v[4:7], v[172:175], a[28:31]\n"
    "ds_read_b64_tr_b16 v[34:35], v231 offset:28672\n"
    "v_fma_mix_f32 v242, v181, v210, v218 op_sel:[0,0,0] op_sel_hi:[1,0,0]\n"
    "v_fma_mix_f32 v243, v181, v211, v219 op_sel:[1,0,0] op_sel_hi:[1,0,0]\n"
    "v_mfma_f32_16x16x32_bf16 a[32:35], v[8:11], v[160:163], a[32:35]\n"
    "s_add_u32 s65, s66, 2686976\n"
    "s_add_u32 m0, s61, 100352\n"
    "ds_read_b64_tr_b16 v[36:37], v231 offset:24584\n"
    "v_cvt_pk_bf16_f32 v180, v240, v241\n"
    "v_cvt_pk_bf16_f32 v181, v242, v243\n"
    "v_mfma_f32_16x16x32_bf16 a[36:39], v[8:11], v[164:167], a[36:39]\n"
    "buffer_load_dwordx4 v237, s[48:51], s65 offen lds\n"
    "v_fma_mix_f32 v244, v182, v212, v220 op_sel:[0,0,0] op_sel_hi:[1,0,0]\n"
    "v_fma_mix_f32 v245, v182, v213, v221 op_sel:[1,0,0] op_sel_hi:[1,0,0]\n"
    "v_mfma_f32_16x16x32_bf16 a[40:43], v[8:11], v[168:171], a[40:43]\n"
    "ds_read_b64_tr_b16 v[38:39], v231 offset:28680\n"
    "v_fma_mix_f32 v246, v183, v214, v222 op_sel:[0,0,0] op_sel_hi:[1,0,0]\n"
    "v_fma_mix_f32 v247, v183, v215, v223 op_sel:[1,0,0] op_sel_hi:[1,0,0]\n"
    "v_mfma_f32_16x16x32_bf16 a[44:47], v[8:11], v[172:175], a[44:47]\n"
    "s_add_u32 m0, s61, 101248\n"
    "ds_read_b64_tr_b16 v[40:41], v231 offset:25088\n"
    "v_cvt_pk_bf16_f32 v182, v244, v245\n"
    "v_cvt_pk_bf16_f32 v183, v246, v247\n"
    "v_mfma_f32_16x16x32_bf16 a[48:51], v[12:15], v[160:163], a[48:51]\n"
    "buffer_load_dwordx4 v237, s[48:51], s65 offen offset:128 lds\n"
    "v_fma_mix_f32 v240, v184, v208, v216 op_sel:[0,0,0] op_sel_hi:[1,0,0]\n"
    "v_mfma_f32_16x16x32_bf16 a[52:55], v[12:15], v[164:167], a[52:55]\n"
    "ds_read_b64_tr_b16 v[42:43], v231 offset:29184\n"
    "v_fma_mix_f32 v241, v184, v209, v217 op_sel:[1,0,0] op_sel_hi:[1,0,0]\n"
    "v_mfma_f32_16x16x32_bf16 a[56:59], v[12:15], v[168:171], a[56:59]\n"
    "s_mov_b32 s67, 8192\n"
    "ds_read_b64_tr_b16 v[44:45], v231 offset:25096\n"
    "v_fma_mix_f32 v242, v185, v210, v218 op_sel:[0,0,0] op_sel_hi:[1,0,0]\n"
    "v_mfma_f32_16x16x32_bf16 a[60:63], v[12:15], v[172:175], a[60:63]\n"
    "buffer_load_dwordx4 v[96:99], v238, s[52:55], s67 offen offset:2048\n"
    "v_fma_mix_f32 v243, v185, v211, v219 op_sel:[1,0,0] op_sel_hi:[1,0,0]\n"
    "v_mfma_f32_16x16x32_bf16 a[64:67], v[16:19], v[160:163], a[64:67]\n"
    "ds_read_b64_tr_b16 v[46:47], v231 offset:29192\n"
    "v_cvt_pk_bf16_f32 v184, v240, v241\n"
    "v_mfma_f32_16x16x32_bf16 a[68:71], v[16:19], v[164:167], a[68:71]\n"
    "s_mov_b32 s67, 24576\n"
    "ds_read_b64_tr_b16 v[48:49], v231 offset:25600\n"
    "v_cvt_pk_bf16_f32 v185, v242, v243\n"
    "v_mfma_f32_16x16x32_bf16 a[72:75], v[16:19], v[168:171], a[72:75]\n"
    "buffer_load_dwordx4 v[100:103], v238, s[52:55], s67 offen offset:2048\n"
    "v_fma_mix_f32 v244, v186, v212, v220 op_sel:[0,0,0] op_sel_hi:[1,0,0]\n"
    "v_mfma_f32_16x16x32_bf16 a[76:79], v[16:19], v[172:175], a[76:79]\n"
    "ds_read_b64_tr_b16 v[50:51], v231 offset:29696\n"
    "v_fma_mix_f32 v245, v186, v213, v221 op_sel:[1,0,0] op_sel_hi:[1,0,0]\n"
    "v_mfma_f32_16x16x32_bf16 a[80:83], v[20:23], v[160:163], a[80:83]\n"
    "s_mov_b32 s67, 40960\n"
    "ds_read_b64_tr_b16 v[52:53], v231 offset:25608\n"
    "v_fma_mix_f32 v246, v187, v214, v222 op_sel:[0,0,0] op_sel_hi:[1,0,0]\n"
    "v_mfma_f32_16x16x32_bf16 a[84:87], v[20:23], v[164:167], a[84:87]\n"
    "buffer_load_dwordx4 v[104:107], v238, s[52:55], s67 offen offset:2048\n"
    "v_fma_mix_f32 v247, v187, v215, v223 op_sel:[1,0,0] op_sel_hi:[1,0,0]\n"
    "v_mfma_f32_16x16x32_bf16 a[88:91], v[20:23], v[168:171], a[88:91]\n"
    "ds_read_b64_tr_b16 v[54:55], v231 offset:29704\n"
    "v_cvt_pk_bf16_f32 v186, v244, v245\n"
    "v_mfma_f32_16x16x32_bf16 a[92:95], v[20:23], v[172:175], a[92:95]\n"
    "s_mov_b32 s67, 57344\n"
    "ds_read_b64_tr_b16 v[56:57], v231 offset:26112\n"
    "v_cvt_pk_bf16_f32 v187, v246, v247\n"
    "v_mfma_f32_16x16x32_bf16 a[96:99], v[24:27], v[160:163], a[96:99]\n"
    "buffer_load_dwordx4 v[108:111], v238, s[52:55], s67 offen offset:2048\n"
    "v_fma_mix_f32 v240, v188, v208, v216 op_sel:[0,0,0] op_sel_hi:[1,0,0]\n"
    "v_mfma_f32_16x16x32_bf16 a[100:103], v[24:27], v[164:167], a[100:103]\n"
    "ds_read_b64_tr_b16 v[58:59], v231 offset:30208\n"
    "v_fma_mix_f32 v241, v188, v209, v217 op_sel:[1,0,0] op_sel_hi:[1,0,0]\n"
    "v_mfma_f32_16x16x32_bf16 a[104:107], v[24:27], v[168:171], a[104:107]\n"
    "ds_read_b64_tr_b16 v[60:61], v231 offset:26120\n"
    "v_fma_mix_f32 v242, v189, v210, v218 op_sel:[0,0,0] op_sel_hi:[1,0,0]\n"
    "v_mfma_f32_16x16x32_bf16 a[108:111], v[24:27], v[172:175], a[108:111]\n"
    "ds_read_b64_tr_b16 v[62:63], v231 offset:30216\n"
    "v_fma_mix_f32 v243, v189, v211, v219 op_sel:[1,0,0] op_sel_hi:[1,0,0]\n"
    "v_mfma_f32_16x16x32_bf16 a[112:115], v[28:31], v[160:163], a[112:115]\n"
    "v_cvt_pk_bf16_f32 v188, v240, v241\n"
    "v_cvt_pk_bf16_f32 v189, v242, v243\n"
    "v_mfma_f32_16x16x32_bf16 a[116:119], v[28:31], v[164:167], a[116:119]\n"
    "v_fma_mix_f32 v244, v190, v212, v220 op_sel:[0,0,0] op_sel_hi:[1,0,0]\n"
    "v_fma_mix_f32 v245, v190, v213, v221 op_sel:[1,0,0] op_sel_hi:[1,0,0]\n"
    "v_mfma_f32_16x16x32_bf16 a[120:123], v[28:31], v[168:171], a[120:123]\n"
    "v_fma_mix_f32 v246, v191, v214, v222 op_sel:[0,0,0] op_sel_hi:[1,0,0]\n"
    "v_fma_mix_f32 v247, v191, v215, v223 op_sel:[1,0,0] op_sel_hi:[1,0,0]\n"
    "v_mfma_f32_16x16x32_bf16 a[124:127], v[28:31], v[172:175], a[124:127]\n"
    "v_cvt_pk_bf16_f32 v190, v244, v245\n"
    "v_cvt_pk_bf16_f32 v191, v246, v247\n"
    "s_waitcnt lgkmcnt(0)\n"
    "s_waitcnt vmcnt(16)\n"
    "s_barrier\n"
    "s_add_u32 s68, s60, 0\n"
    "v_mfma_f32_16x16x32_bf16 a[0:3], v[32:35], v[176:179], a[0:3]\n"
    "ds_read_b128 v[208:211], v239 offset:2304\n"
    "v_fma_mix_f32 v240, v64, v192, v200 op_sel:[0,0,0] op_sel_hi:[1,0,0]\n"
    "v_fma_mix_f32 v241, v64, v193, v201 op_sel:[1,0,0] op_sel_hi:[1,0,0]\n"
    "v_mfma_f32_16x16x32_bf16 a[4:7], v[32:35], v[180:183], a[4:7]\n"
    "ds_read_b128 v[212:215], v239 offset:2320\n"
    "v_fma_mix_f32 v242, v65, v194, v202 op_sel:[0,0,0] op_sel_hi:[1,0,0]\n"
    "v_fma_mix_f32 v243, v65, v195, v203 op_sel:[1,0,0] op_sel_hi:[1,0,0]\n"
    "v_mfma_f32_16x16x32_bf16 a[8:11], v[32:35], v[184:187], a[8:11]\n"
    "s_add_u32 m0, s62, 0\n"
    "ds_read_b128 v[216:219], v239 offset:2336\n"
    "v_cvt_pk_bf16_f32 v64, v240, v241\n"
    "v_cvt_pk_bf16_f32 v65, v242, v243\n"
    "v_mfma_f32_16x16x32_bf16 a[12:15], v[32:35], v[188:191], a[12:15]\n"
    "buffer_load_dwordx4 v236, s[44:47], s68 offen lds\n"
    "v_fma_mix_f32 v244, v66, v196, v204 op_sel:[0,0,0] op_sel_hi:[1,0,0]\n"
    "v_fma_mix_f32 v245, v66, v197, v205 op_sel:[1,0,0] op_sel_hi:[1,0,0]\n"
    "v_mfma_f32_16x16x32_bf16 a[16:19], v[36:39], v[176:179], a[16:19]\n"
    "ds_read_b128 v[220:223], v239 offset:2352\n"
    "v_fma_mix_f32 v246, v67, v198, v206 op_sel:[0,0,0] op_sel_hi:[1,0,0]\n"
    "v_fma_mix_f32 v247, v67, v199, v207 op_sel:[1,0,0] op_sel_hi:[1,0,0]\n"
    "v_mfma_f32_16x16x32_bf16 a[20:23], v[36:39], v[180:183], a[20:23]\n"
    "s_add_u32 m0, s62, 896\n"
    "ds_read_b64_tr_b16 v[0:1], v230 offset:32768\n"
    "v_cvt_pk_bf16_f32 v66, v244, v245\n"
    "v_cvt_pk_bf16_f32 v67, v246, v247\n"
    "v_mfma_f32_16x16x32_bf16 a[24:27], v[36:39], v[184:187], a[24:27]\n"
    "buffer_load_dwordx4 v236, s[44:47], s68 offen offset:128 lds\n"
    "v_fma_mix_f32 v240, v68, v192, v200 op_sel:[0,0,0] op_sel_hi:[1,0,0]\n"
    "v_fma_mix_f32 v241, v68, v193, v201 op_sel:[1,0,0] op_sel_hi:[1,0,0]\n"
    "v_mfma_f32_16x16x32_bf16 a[28:31], v[36:39], v[188:191], a[28:31]\n"
    "ds_read_b64_tr_b16 v[2:3], v230 offset:36864\n"
    "v_fma_mix_f32 v242, v69, v194, v202 op_sel:[0,0,0] op_sel_hi:[1,0,0]\n"
    "v_fma_mix_f32 v243, v69, v195, v203 op_sel:[1,0,0] op_sel_hi:[1,0,0]\n"
    "v_mfma_f32_16x16x32_bf16 a[32:35], v[40:43], v[176:179], a[32:35]\n"
    "s_add_u32 m0, s62, 0\n"
    "ds_read_b64_tr_b16 v[4:5], v230 offset:32776\n"
    "v_cvt_pk_bf16_f32 v68, v240, v241\n"
    "v_cvt_pk_bf16_f32 v69, v242, v243\n"
    "v_mfma_f32_16x16x32_bf16 a[36:39], v[40:43], v[180:183], a[36:39]\n"
    "buffer_load_dwordx4 v236, s[44:47], s68 offen offset:2048 lds\n"
    "v_fma_mix_f32 v244, v70, v196, v204 op_sel:[0,0,0] op_sel_hi:[1,0,0]\n"
    "v_fma_mix_f32 v245, v70, v197, v205 op_sel:[1,0,0] op_sel_hi:[1,0,0]\n"
    "v_mfma_f32_16x16x32_bf16 a[40:43], v[40:43], v[184:187], a[40:43]\n"
    "ds_read_b64_tr_b16 v[6:7], v230 offset:36872\n"
    "v_fma_mix_f32 v246, v71, v198, v206 op_sel:[0,0,0] op_sel_hi:[1,0,0]\n"
    "v_fma_mix_f32 v247, v71, v199, v207 op_sel:[1,0,0] op_sel_hi:[1,0,0]\n"
    "v_mfma_f32_16x16x32_bf16 a[44:47], v[40:43], v[188:191], a[44:47]\n"
    "s_add_u32 m0, s62, 896\n"
    "ds_read_b64_tr_b16 v[8:9], v230 offset:33280\n"
    "v_cvt_pk_bf16_f32 v70, v244, v245\n"
    "v_cvt_pk_bf16_f32 v71, v246, v247\n"
    "v_mfma_f32_16x16x32_bf16 a[48:51], v[44:47], v[176:179], a[48:51]\n"
    "buffer_load_dwordx4 v236, s[44:47], s68 offen offset:2176 lds\n"
    "v_fma_mix_f32 v240, v72, v192, v200 op_sel:[0,0,0] op_sel_hi:[1,0,0]\n"
    "v_mfma_f32_16x16x32_bf16 a[52:55], v[44:47], v[180:183], a[52:55]\n"
    "ds_read_b64_tr_b16 v[10:11], v230 offset:37376\n"
    "v_fma_mix_f32 v241, v72, v193, v201 op_sel:[1,0,0] op_sel_hi:[1,0,0]\n"
    "v_mfma_f32_16x16x32_bf16 a[56:59], v[44:47], v[184:187], a[56:59]\n"
    "s_mov_b32 s67, 8192\n"
    "ds_read_b64_tr_b16 v[12:13], v230 offset:33288\n"
    "v_fma_mix_f32 v242, v73, v194, v202 op_sel:[0,0,0] op_sel_hi:[1,0,0]\n"
    "v_mfma_f32_16x16x32_bf16 a[60:63], v[44:47], v[188:191], a[60:63]\n"
    "buffer_load_dwordx4 v[112:115], v238, s[52:55], s67 offen offset:3072\n"
    "v_fma_mix_f32 v243, v73, v195, v203 op_sel:[1,0,0] op_sel_hi:[1,0,0]\n"
    "v_mfma_f32_16x16x32_bf16 a[64:67], v[48:51], v[176:179], a[64:67]\n"
    "ds_read_b64_tr_b16 v[14:15], v230 offset:37384\n"
    "v_cvt_pk_bf16_f32 v72, v240, v241\n"
    "v_mfma_f32_16x16x32_bf16 a[68:71], v[48:51], v[180:183], a[68:71]\n"
    "s_mov_b32 s67, 24576\n"
    "ds_read_b64_tr_b16 v[16:17], v230 offset:33792\n"
    "v_cvt_pk_bf16_f32 v73, v242, v243\n"
    "v_mfma_f32_16x16x32_bf16 a[72:75], v[48:51], v[184:187], a[72:75]\n"
    "buffer_load_dwordx4 v[116:119], v238, s[52:55], s67 offen offset:3072\n"
    "v_fma_mix_f32 v244, v74, v196, v204 op_sel:[0,0,0] op_sel_hi:[1,0,0]\n"
    "v_mfma_f32_16x16x32_bf16 a[76:79], v[48:51], v[188:191], a[76:79]\n"
    "ds_read_b64_tr_b16 v[18:19], v230 offset:37888\n"
    "v_fma_mix_f32 v245, v74, v197, v205 op_sel:[1,0,0] op_sel_hi:[1,0,0]\n"
    "v_mfma_f32_16x16x32_bf16 a[80:83], v[52:55], v[176:179], a[80:83]\n"
    "s_mov_b32 s67, 40960\n"
    "ds_read_b64_tr_b16 v[20:21], v230 offset:33800\n"
    "v_fma_mix_f32 v246, v75, v198, v206 op_sel:[0,0,0] op_sel_hi:[1,0,0]\n"
    "v_mfma_f32_16x16x32_bf16 a[84:87], v[52:55], v[180:183], a[84:87]\n"
    "buffer_load_dwordx4 v[120:123], v238, s[52:55], s67 offen offset:3072\n"
    "v_fma_mix_f32 v247, v75, v199, v207 op_sel:[1,0,0] op_sel_hi:[1,0,0]\n"
    "v_mfma_f32_16x16x32_bf16 a[88:91], v[52:55], v[184:187], a[88:91]\n"
    "ds_read_b64_tr_b16 v[22:23], v230 offset:37896\n"
    "v_cvt_pk_bf16_f32 v74, v244, v245\n"
    "v_mfma_f32_16x16x32_bf16 a[92:95], v[52:55], v[188:191], a[92:95]\n"
    "s_mov_b32 s67, 57344\n"
    "ds_read_b64_tr_b16 v[24:25], v230 offset:34304\n"
    "v_cvt_pk_bf16_f32 v75, v246, v247\n"
    "v_mfma_f32_16x16x32_bf16 a[96:99], v[56:59], v[176:179], a[96:99]\n"
    "buffer_load_dwordx4 v[124:127], v238, s[52:55], s67 offen offset:3072\n"
    "v_fma_mix_f32 v240, v76, v192, v200 op_sel:[0,0,0] op_sel_hi:[1,0,0]\n"
    "v_mfma_f32_16x16x32_bf16 a[100:103], v[56:59], v[180:183], a[100:103]\n"
    "ds_read_b64_tr_b16 v[26:27], v230 offset:38400\n"
    "v_fma_mix_f32 v241, v76, v193, v201 op_sel:[1,0,0] op_sel_hi:[1,0,0]\n"
    "v_mfma_f32_16x16x32_bf16 a[104:107], v[56:59], v[184:187], a[104:107]\n"
    "ds_read_b64_tr_b16 v[28:29], v230 offset:34312\n"
    "v_fma_mix_f32 v242, v77, v194, v202 op_sel:[0,0,0] op_sel_hi:[1,0,0]\n"
    "v_mfma_f32_16x16x32_bf16 a[108:111], v[56:59], v[188:191], a[108:111]\n"
    "ds_read_b64_tr_b16 v[30:31], v230 offset:38408\n"
    "v_fma_mix_f32 v243, v77, v195, v203 op_sel:[1,0,0] op_sel_hi:[1,0,0]\n"
    "v_mfma_f32_16x16x32_bf16 a[112:115], v[60:63], v[176:179], a[112:115]\n"
    "v_cvt_pk_bf16_f32 v76, v240, v241\n"
    "v_cvt_pk_bf16_f32 v77, v242, v243\n"
    "v_mfma_f32_16x16x32_bf16 a[116:119], v[60:63], v[180:183], a[116:119]\n"
    "v_fma_mix_f32 v244, v78, v196, v204 op_sel:[0,0,0] op_sel_hi:[1,0,0]\n"
    "v_fma_mix_f32 v245, v78, v197, v205 op_sel:[1,0,0] op_sel_hi:[1,0,0]\n"
    "v_mfma_f32_16x16x32_bf16 a[120:123], v[60:63], v[184:187], a[120:123]\n"
    "v_fma_mix_f32 v246, v79, v198, v206 op_sel:[0,0,0] op_sel_hi:[1,0,0]\n"
    "v_fma_mix_f32 v247, v79, v199, v207 op_sel:[1,0,0] op_sel_hi:[1,0,0]\n"
    "v_mfma_f32_16x16x32_bf16 a[124:127], v[60:63], v[188:191], a[124:127]\n"
    "v_cvt_pk_bf16_f32 v78, v244, v245\n"
    "v_cvt_pk_bf16_f32 v79, v246, v247\n"
    "s_waitcnt lgkmcnt(0)\n"
    "s_waitcnt vmcnt(16)\n"
    "v_mfma_f32_16x16x32_bf16 a[0:3], v[0:3], v[64:67], a[0:3]\n"
    "ds_read_b128 v[192:195], v239 offset:2560\n"
    "v_fma_mix_f32 v240, v80, v208, v216 op_sel:[0,0,0] op_sel_hi:[1,0,0]\n"
    "v_fma_mix_f32 v241, v80, v209, v217 op_sel:[1,0,0] op_sel_hi:[1,0,0]\n"
    "v_mfma_f32_16x16x32_bf16 a[4:7], v[0:3], v[68:71], a[4:7]\n"
    "ds_read_b128 v[196:199], v239 offset:2576\n"
    "v_fma_mix_f32 v242, v81, v210, v218 op_sel:[0,0,0] op_sel_hi:[1,0,0]\n"
    "v_fma_mix_f32 v243, v81, v211, v219 op_sel:[1,0,0] op_sel_hi:[1,0,0]\n"
    "v_mfma_f32_16x16x32_bf16 a[8:11], v[0:3], v[72:75], a[8:11]\n"
    "s_add_u32 s65, s66, 3145728\n"
    "s_add_u32 m0, s61, 49152\n"
    "ds_read_b128 v[200:203], v239 offset:2592\n"
    "v_cvt_pk_bf16_f32 v80, v240, v241\n"
    "v_cvt_pk_bf16_f32 v81, v242, v243\n"
    "v_mfma_f32_16x16x32_bf16 a[12:15], v[0:3], v[76:79], a[12:15]\n"
    "buffer_load_dwordx4 v237, s[48:51], s65 offen lds\n"
    "v_fma_mix_f32 v244, v82, v212, v220 op_sel:[0,0,0] op_sel_hi:[1,0,0]\n"
    "v_fma_mix_f32 v245, v82, v213, v221 op_sel:[1,0,0] op_sel_hi:[1,0,0]\n"
    "v_mfma_f32_16x16x32_bf16 a[16:19], v[4:7], v[64:67], a[16:19]\n"
    "ds_read_b128 v[204:207], v239 offset:2608\n"
    "v_fma_mix_f32 v246, v83, v214, v222 op_sel:[0,0,0] op_sel_hi:[1,0,0]\n"
    "v_fma_mix_f32 v247, v83, v215, v223 op_sel:[1,0,0] op_sel_hi:[1,0,0]\n"
    "v_mfma_f32_16x16x32_bf16 a[20:23], v[4:7], v[68:71], a[20:23]\n"
    "s_add_u32 m0, s61, 50048\n"
    "ds_read_b64_tr_b16 v[32:33], v230 offset:40960\n"
    "v_cvt_pk_bf16_f32 v82, v244, v245\n"
    "v_cvt_pk_bf16_f32 v83, v246, v247\n"
    "v_mfma_f32_16x16x32_bf16 a[24:27], v[4:7], v[72:75], a[24:27]\n"
    "buffer_load_dwordx4 v237, s[48:51], s65 offen offset:128 lds\n"
    "v_fma_mix_f32 v240, v84, v208, v216 op_sel:[0,0,0] op_sel_hi:[1,0,0]\n"
    "v_fma_mix_f32 v241, v84, v209, v217 op_sel:[1,0,0] op_sel_hi:[1,0,0]\n"
    "v_mfma_f32_16x16x32_bf16 a[28:31], v[4:7], v[76:79], a[28:31]\n"
    "ds_read_b64_tr_b16 v[34:35], v230 offset:45056\n"
    "v_fma_mix_f32 v242, v85, v210, v218 op_sel:[0,0,0] op_sel_hi:[1,0,0]\n"
    "v_fma_mix_f32 v243, v85, v211, v219 op_sel:[1,0,0] op_sel_hi:[1,0,0]\n"
    "v_mfma_f32_16x16x32_bf16 a[32:35], v[8:11], v[64:67], a[32:35]\n"
    "s_add_u32 s65, s66, 3211264\n"
    "s_add_u32 m0, s61, 51200\n"
    "ds_read_b64_tr_b16 v[36:37], v230 offset:40968\n"
    "v_cvt_pk_bf16_f32 v84, v240, v241\n"
    "v_cvt_pk_bf16_f32 v85, v242, v243\n"
    "v_mfma_f32_16x16x32_bf16 a[36:39], v[8:11], v[68:71], a[36:39]\n"
    "buffer_load_dwordx4 v237, s[48:51], s65 offen lds\n"
    "v_fma_mix_f32 v244, v86, v212, v220 op_sel:[0,0,0] op_sel_hi:[1,0,0]\n"
    "v_fma_mix_f32 v245, v86, v213, v221 op_sel:[1,0,0] op_sel_hi:[1,0,0]\n"
    "v_mfma_f32_16x16x32_bf16 a[40:43], v[8:11], v[72:75], a[40:43]\n"
    "ds_read_b64_tr_b16 v[38:39], v230 offset:45064\n"
    "v_fma_mix_f32 v246, v87, v214, v222 op_sel:[0,0,0] op_sel_hi:[1,0,0]\n"
    "v_fma_mix_f32 v247, v87, v215, v223 op_sel:[1,0,0] op_sel_hi:[1,0,0]\n"
    "v_mfma_f32_16x16x32_bf16 a[44:47], v[8:11], v[76:79], a[44:47]\n"
    "s_add_u32 m0, s61, 52096\n"
    "ds_read_b64_tr_b16 v[40:41], v230 offset:41472\n"
    "v_cvt_pk_bf16_f32 v86, v244, v245\n"
    "v_cvt_pk_bf16_f32 v87, v246, v247\n"
    "v_mfma_f32_16x16x32_bf16 a[48:51], v[12:15], v[64:67], a[48:51]\n"
    "buffer_load_dwordx4 v237, s[48:51], s65 offen offset:128 lds\n"
    "v_fma_mix_f32 v240, v88, v208, v216 op_sel:[0,0,0] op_sel_hi:[1,0,0]\n"
    "v_mfma_f32_16x16x32_bf16 a[52:55], v[12:15], v[68:71], a[52:55]\n"
    "ds_read_b64_tr_b16 v[42:43], v230 offset:45568\n"
    "v_fma_mix_f32 v241, v88, v209, v217 op_sel:[1,0,0] op_sel_hi:[1,0,0]\n"
    "v_mfma_f32_16x16x32_bf16 a[56:59], v[12:15], v[72:75], a[56:59]\n"
    "s_mov_b32 s67, 12288\n"
    "ds_read_b64_tr_b16 v[44:45], v230 offset:41480\n"
    "v_fma_mix_f32 v242, v89, v210, v218 op_sel:[0,0,0] op_sel_hi:[1,0,0]\n"
    "v_mfma_f32_16x16x32_bf16 a[60:63], v[12:15], v[76:79], a[60:63]\n"
    "buffer_load_dwordx4 v[128:131], v238, s[52:55], s67 offen\n"
    "v_fma_mix_f32 v243, v89, v211, v219 op_sel:[1,0,0] op_sel_hi:[1,0,0]\n"
    "v_mfma_f32_16x16x32_bf16 a[64:67], v[16:19], v[64:67], a[64:67]\n"
    "ds_read_b64_tr_b16 v[46:47], v230 offset:45576\n"
    "v_cvt_pk_bf16_f32 v88, v240, v241\n"
    "v_mfma_f32_16x16x32_bf16 a[68:71], v[16:19], v[68:71], a[68:71]\n"
    "s_mov_b32 s67, 28672\n"
    "ds_read_b64_tr_b16 v[48:49], v230 offset:41984\n"
    "v_cvt_pk_bf16_f32 v89, v242, v243\n"
    "v_mfma_f32_16x16x32_bf16 a[72:75], v[16:19], v[72:75], a[72:75]\n"
    "buffer_load_dwordx4 v[132:135], v238, s[52:55], s67 offen\n"
    "v_fma_mix_f32 v244, v90, v212, v220 op_sel:[0,0,0] op_sel_hi:[1,0,0]\n"
    "v_mfma_f32_16x16x32_bf16 a[76:79], v[16:19], v[76:79], a[76:79]\n"
    "ds_read_b64_tr_b16 v[50:51], v230 offset:46080\n"
    "v_fma_mix_f32 v245, v90, v213, v221 op_sel:[1,0,0] op_sel_hi:[1,0,0]\n"
    "v_mfma_f32_16x16x32_bf16 a[80:83], v[20:23], v[64:67], a[80:83]\n"
    "s_mov_b32 s67, 45056\n"
    "ds_read_b64_tr_b16 v[52:53], v230 offset:41992\n"
    "v_fma_mix_f32 v246, v91, v214, v222 op_sel:[0,0,0] op_sel_hi:[1,0,0]\n"
    "v_mfma_f32_16x16x32_bf16 a[84:87], v[20:23], v[68:71], a[84:87]\n"
    "buffer_load_dwordx4 v[136:139], v238, s[52:55], s67 offen\n"
    "v_fma_mix_f32 v247, v91, v215, v223 op_sel:[1,0,0] op_sel_hi:[1,0,0]\n"
    "v_mfma_f32_16x16x32_bf16 a[88:91], v[20:23], v[72:75], a[88:91]\n"
    "ds_read_b64_tr_b16 v[54:55], v230 offset:46088\n"
    "v_cvt_pk_bf16_f32 v90, v244, v245\n"
    "v_mfma_f32_16x16x32_bf16 a[92:95], v[20:23], v[76:79], a[92:95]\n"
    "s_mov_b32 s67, 61440\n"
    "ds_read_b64_tr_b16 v[56:57], v230 offset:42496\n"
    "v_cvt_pk_bf16_f32 v91, v246, v247\n"
    "v_mfma_f32_16x16x32_bf16 a[96:99], v[24:27], v[64:67], a[96:99]\n"
    "buffer_load_dwordx4 v[140:143], v238, s[52:55], s67 offen\n"
    "v_fma_mix_f32 v240, v92, v208, v216 op_sel:[0,0,0] op_sel_hi:[1,0,0]\n"
    "v_mfma_f32_16x16x32_bf16 a[100:103], v[24:27], v[68:71], a[100:103]\n"
    "ds_read_b64_tr_b16 v[58:59], v230 offset:46592\n"
    "v_fma_mix_f32 v241, v92, v209, v217 op_sel:[1,0,0] op_sel_hi:[1,0,0]\n"
    "v_mfma_f32_16x16x32_bf16 a[104:107], v[24:27], v[72:75], a[104:107]\n"
    "ds_read_b64_tr_b16 v[60:61], v230 offset:42504\n"
    "v_fma_mix_f32 v242, v93, v210, v218 op_sel:[0,0,0] op_sel_hi:[1,0,0]\n"
    "v_mfma_f32_16x16x32_bf16 a[108:111], v[24:27], v[76:79], a[108:111]\n"
    "ds_read_b64_tr_b16 v[62:63], v230 offset:46600\n"
    "v_fma_mix_f32 v243, v93, v211, v219 op_sel:[1,0,0] op_sel_hi:[1,0,0]\n"
    "v_mfma_f32_16x16x32_bf16 a[112:115], v[28:31], v[64:67], a[112:115]\n"
    "v_cvt_pk_bf16_f32 v92, v240, v241\n"
    "v_cvt_pk_bf16_f32 v93, v242, v243\n"
    "v_mfma_f32_16x16x32_bf16 a[116:119], v[28:31], v[68:71], a[116:119]\n"
    "v_fma_mix_f32 v244, v94, v212, v220 op_sel:[0,0,0] op_sel_hi:[1,0,0]\n"
    "v_fma_mix_f32 v245, v94, v213, v221 op_sel:[1,0,0] op_sel_hi:[1,0,0]\n"
    "v_mfma_f32_16x16x32_bf16 a[120:123], v[28:31], v[72:75], a[120:123]\n"
    "v_fma_mix_f32 v246, v95, v214, v222 op_sel:[0,0,0] op_sel_hi:[1,0,0]\n"
    "v_fma_mix_f32 v247, v95, v215, v223 op_sel:[1,0,0] op_sel_hi:[1,0,0]\n"
    "v_mfma_f32_16x16x32_bf16 a[124:127], v[28:31], v[76:79], a[124:127]\n"
    "v_cvt_pk_bf16_f32 v94, v244, v245\n"
    "v_cvt_pk_bf16_f32 v95, v246, v247\n"
    "s_waitcnt lgkmcnt(0)\n"
    "s_waitcnt vmcnt(16)\n"
    "s_barrier\n"
    "s_add_u32 s68, s60, 16384\n"
    "v_mfma_f32_16x16x32_bf16 a[0:3], v[32:35], v[80:83], a[0:3]\n"
    "ds_read_b128 v[208:211], v239 offset:2816\n"
    "v_fma_mix_f32 v240, v96, v192, v200 op_sel:[0,0,0] op_sel_hi:[1,0,0]\n"
    "v_fma_mix_f32 v241, v96, v193, v201 op_sel:[1,0,0] op_sel_hi:[1,0,0]\n"
    "v_mfma_f32_16x16x32_bf16 a[4:7], v[32:35], v[84:87], a[4:7]\n"
    "ds_read_b128 v[212:215], v239 offset:2832\n"
    "v_fma_mix_f32 v242, v97, v194, v202 op_sel:[0,0,0] op_sel_hi:[1,0,0]\n"
    "v_fma_mix_f32 v243, v97, v195, v203 op_sel:[1,0,0] op_sel_hi:[1,0,0]\n"
    "v_mfma_f32_16x16x32_bf16 a[8:11], v[32:35], v[88:91], a[8:11]\n"
    "s_add_u32 m0, s61, 16384\n"
    "ds_read_b128 v[216:219], v239 offset:2848\n"
    "v_cvt_pk_bf16_f32 v96, v240, v241\n"
    "v_cvt_pk_bf16_f32 v97, v242, v243\n"
    "v_mfma_f32_16x16x32_bf16 a[12:15], v[32:35], v[92:95], a[12:15]\n"
    "buffer_load_dwordx4 v232, s[40:43], s68 offen lds\n"
    "v_fma_mix_f32 v244, v98, v196, v204 op_sel:[0,0,0] op_sel_hi:[1,0,0]\n"
    "v_fma_mix_f32 v245, v98, v197, v205 op_sel:[1,0,0] op_sel_hi:[1,0,0]\n"
    "v_mfma_f32_16x16x32_bf16 a[16:19], v[36:39], v[80:83], a[16:19]\n"
    "ds_read_b128 v[220:223], v239 offset:2864\n"
    "v_fma_mix_f32 v246, v99, v198, v206 op_sel:[0,0,0] op_sel_hi:[1,0,0]\n"
    "v_fma_mix_f32 v247, v99, v199, v207 op_sel:[1,0,0] op_sel_hi:[1,0,0]\n"
    "v_mfma_f32_16x16x32_bf16 a[20:23], v[36:39], v[84:87], a[20:23]\n"
    "buffer_load_dwordx4 v233, s[40:43], s68 offen offset:1024 lds\n"
    "v_cvt_pk_bf16_f32 v98, v244, v245\n"
    "v_cvt_pk_bf16_f32 v99, v246, v247\n"
    "v_mfma_f32_16x16x32_bf16 a[24:27], v[36:39], v[88:91], a[24:27]\n"
    "ds_read_b64_tr_b16 v[0:1], v231 offset:32768\n"
    "v_fma_mix_f32 v240, v100, v192, v200 op_sel:[0,0,0] op_sel_hi:[1,0,0]\n"
    "v_fma_mix_f32 v241, v100, v193, v201 op_sel:[1,0,0] op_sel_hi:[1,0,0]\n"
    "v_mfma_f32_16x16x32_bf16 a[28:31], v[36:39], v[92:95], a[28:31]\n"
    "ds_read_b64_tr_b16 v[2:3], v231 offset:36864\n"
    "v_fma_mix_f32 v242, v101, v194, v202 op_sel:[0,0,0] op_sel_hi:[1,0,0]\n"
    "v_fma_mix_f32 v243, v101, v195, v203 op_sel:[1,0,0] op_sel_hi:[1,0,0]\n"
    "v_mfma_f32_16x16x32_bf16 a[32:35], v[40:43], v[80:83], a[32:35]\n"
    "buffer_load_dwordx4 v234, s[40:43], s68 offen offset:2048 lds\n"
    "v_cvt_pk_bf16_f32 v100, v240, v241\n"
    "v_cvt_pk_bf16_f32 v101, v242, v243\n"
    "v_mfma_f32_16x16x32_bf16 a[36:39], v[40:43], v[84:87], a[36:39]\n"
    "ds_read_b64_tr_b16 v[4:5], v231 offset:32776\n"
    "v_fma_mix_f32 v244, v102, v196, v204 op_sel:[0,0,0] op_sel_hi:[1,0,0]\n"
    "v_fma_mix_f32 v245, v102, v197, v205 op_sel:[1,0,0] op_sel_hi:[1,0,0]\n"
    "v_mfma_f32_16x16x32_bf16 a[40:43], v[40:43], v[88:91], a[40:43]\n"
    "ds_read_b64_tr_b16 v[6:7], v231 offset:36872\n"
    "v_fma_mix_f32 v246, v103, v198, v206 op_sel:[0,0,0] op_sel_hi:[1,0,0]\n"
    "v_fma_mix_f32 v247, v103, v199, v207 op_sel:[1,0,0] op_sel_hi:[1,0,0]\n"
    "v_mfma_f32_16x16x32_bf16 a[44:47], v[40:43], v[92:95], a[44:47]\n"
    "buffer_load_dwordx4 v235, s[40:43], s68 offen offset:3072 lds\n"
    "v_cvt_pk_bf16_f32 v102, v244, v245\n"
    "v_cvt_pk_bf16_f32 v103, v246, v247\n"
    "v_mfma_f32_16x16x32_bf16 a[48:51], v[44:47], v[80:83], a[48:51]\n"
    "ds_read_b64_tr_b16 v[8:9], v231 offset:33280\n"
    "v_fma_mix_f32 v240, v104, v192, v200 op_sel:[0,0,0] op_sel_hi:[1,0,0]\n"
    "v_mfma_f32_16x16x32_bf16 a[52:55], v[44:47], v[84:87], a[52:55]\n"
    "ds_read_b64_tr_b16 v[10:11], v231 offset:37376\n"
    "v_fma_mix_f32 v241, v104, v193, v201 op_sel:[1,0,0] op_sel_hi:[1,0,0]\n"
    "v_mfma_f32_16x16x32_bf16 a[56:59], v[44:47], v[88:91], a[56:59]\n"
    "s_mov_b32 s67, 12288\n"
    "ds_read_b64_tr_b16 v[12:13], v231 offset:33288\n"
    "v_fma_mix_f32 v242, v105, v194, v202 op_sel:[0,0,0] op_sel_hi:[1,0,0]\n"
    "v_mfma_f32_16x16x32_bf16 a[60:63], v[44:47], v[92:95], a[60:63]\n"
    "buffer_load_dwordx4 v[144:147], v238, s[52:55], s67 offen offset:1024\n"
    "v_fma_mix_f32 v243, v105, v195, v203 op_sel:[1,0,0] op_sel_hi:[1,0,0]\n"
    "v_mfma_f32_16x16x32_bf16 a[64:67], v[48:51], v[80:83], a[64:67]\n"
    "ds_read_b64_tr_b16 v[14:15], v231 offset:37384\n"
    "v_cvt_pk_bf16_f32 v104, v240, v241\n"
    "v_mfma_f32_16x16x32_bf16 a[68:71], v[48:51], v[84:87], a[68:71]\n"
    "s_mov_b32 s67, 28672\n"
    "ds_read_b64_tr_b16 v[16:17], v231 offset:33792\n"
    "v_cvt_pk_bf16_f32 v105, v242, v243\n"
    "v_mfma_f32_16x16x32_bf16 a[72:75], v[48:51], v[88:91], a[72:75]\n"
    "buffer_load_dwordx4 v[148:151], v238, s[52:55], s67 offen offset:1024\n"
    "v_fma_mix_f32 v244, v106, v196, v204 op_sel:[0,0,0] op_sel_hi:[1,0,0]\n"
    "v_mfma_f32_16x16x32_bf16 a[76:79], v[48:51], v[92:95], a[76:79]\n"
    "ds_read_b64_tr_b16 v[18:19], v231 offset:37888\n"
    "v_fma_mix_f32 v245, v106, v197, v205 op_sel:[1,0,0] op_sel_hi:[1,0,0]\n"
    "v_mfma_f32_16x16x32_bf16 a[80:83], v[52:55], v[80:83], a[80:83]\n"
    "s_mov_b32 s67, 45056\n"
    "ds_read_b64_tr_b16 v[20:21], v231 offset:33800\n"
    "v_fma_mix_f32 v246, v107, v198, v206 op_sel:[0,0,0] op_sel_hi:[1,0,0]\n"
    "v_mfma_f32_16x16x32_bf16 a[84:87], v[52:55], v[84:87], a[84:87]\n"
    "buffer_load_dwordx4 v[152:155], v238, s[52:55], s67 offen offset:1024\n"
    "v_fma_mix_f32 v247, v107, v199, v207 op_sel:[1,0,0] op_sel_hi:[1,0,0]\n"
    "v_mfma_f32_16x16x32_bf16 a[88:91], v[52:55], v[88:91], a[88:91]\n"
    "ds_read_b64_tr_b16 v[22:23], v231 offset:37896\n"
    "v_cvt_pk_bf16_f32 v106, v244, v245\n"
    "v_mfma_f32_16x16x32_bf16 a[92:95], v[52:55], v[92:95], a[92:95]\n"
    "s_mov_b32 s67, 61440\n"
    "ds_read_b64_tr_b16 v[24:25], v231 offset:34304\n"
    "v_cvt_pk_bf16_f32 v107, v246, v247\n"
    "v_mfma_f32_16x16x32_bf16 a[96:99], v[56:59], v[80:83], a[96:99]\n"
    "buffer_load_dwordx4 v[156:159], v238, s[52:55], s67 offen offset:1024\n"
    "v_fma_mix_f32 v240, v108, v192, v200 op_sel:[0,0,0] op_sel_hi:[1,0,0]\n"
    "v_mfma_f32_16x16x32_bf16 a[100:103], v[56:59], v[84:87], a[100:103]\n"
    "ds_read_b64_tr_b16 v[26:27], v231 offset:38400\n"
    "v_fma_mix_f32 v241, v108, v193, v201 op_sel:[1,0,0] op_sel_hi:[1,0,0]\n"
    "v_mfma_f32_16x16x32_bf16 a[104:107], v[56:59], v[88:91], a[104:107]\n"
    "ds_read_b64_tr_b16 v[28:29], v231 offset:34312\n"
    "v_fma_mix_f32 v242, v109, v194, v202 op_sel:[0,0,0] op_sel_hi:[1,0,0]\n"
    "v_mfma_f32_16x16x32_bf16 a[108:111], v[56:59], v[92:95], a[108:111]\n"
    "ds_read_b64_tr_b16 v[30:31], v231 offset:38408\n"
    "v_fma_mix_f32 v243, v109, v195, v203 op_sel:[1,0,0] op_sel_hi:[1,0,0]\n"
    "v_mfma_f32_16x16x32_bf16 a[112:115], v[60:63], v[80:83], a[112:115]\n"
    "v_cvt_pk_bf16_f32 v108, v240, v241\n"
    "v_cvt_pk_bf16_f32 v109, v242, v243\n"
    "v_mfma_f32_16x16x32_bf16 a[116:119], v[60:63], v[84:87], a[116:119]\n"
    "v_fma_mix_f32 v244, v110, v196, v204 op_sel:[0,0,0] op_sel_hi:[1,0,0]\n"
    "v_fma_mix_f32 v245, v110, v197, v205 op_sel:[1,0,0] op_sel_hi:[1,0,0]\n"
    "v_mfma_f32_16x16x32_bf16 a[120:123], v[60:63], v[88:91], a[120:123]\n"
    "v_fma_mix_f32 v246, v111, v198, v206 op_sel:[0,0,0] op_sel_hi:[1,0,0]\n"
    "v_fma_mix_f32 v247, v111, v199, v207 op_sel:[1,0,0] op_sel_hi:[1,0,0]\n"
    "v_mfma_f32_16x16x32_bf16 a[124:127], v[60:63], v[92:95], a[124:127]\n"
    "v_cvt_pk_bf16_f32 v110, v244, v245\n"
    "v_cvt_pk_bf16_f32 v111, v246, v247\n"
    "s_waitcnt lgkmcnt(0)\n"
    "s_waitcnt vmcnt(16)\n"
    "v_mfma_f32_16x16x32_bf16 a[0:3], v[0:3], v[96:99], a[0:3]\n"
    "ds_read_b128 v[192:195], v239 offset:3072\n"
    "v_fma_mix_f32 v240, v112, v208, v216 op_sel:[0,0,0] op_sel_hi:[1,0,0]\n"
    "v_fma_mix_f32 v241, v112, v209, v217 op_sel:[1,0,0] op_sel_hi:[1,0,0]\n"
    "v_mfma_f32_16x16x32_bf16 a[4:7], v[0:3], v[100:103], a[4:7]\n"
    "ds_read_b128 v[196:199], v239 offset:3088\n"
    "v_fma_mix_f32 v242, v113, v210, v218 op_sel:[0,0,0] op_sel_hi:[1,0,0]\n"
    "v_fma_mix_f32 v243, v113, v211, v219 op_sel:[1,0,0] op_sel_hi:[1,0,0]\n"
    "v_mfma_f32_16x16x32_bf16 a[8:11], v[0:3], v[104:107], a[8:11]\n"
    "s_add_u32 s65, s66, 3670016\n"
    "s_add_u32 m0, s61, 114688\n"
    "ds_read_b128 v[200:203], v239 offset:3104\n"
    "v_cvt_pk_bf16_f32 v112, v240, v241\n"
    "v_cvt_pk_bf16_f32 v113, v242, v243\n"
    "v_mfma_f32_16x16x32_bf16 a[12:15], v[0:3], v[108:111], a[12:15]\n"
    "buffer_load_dwordx4 v237, s[48:51], s65 offen lds\n"
    "v_fma_mix_f32 v244, v114, v212, v220 op_sel:[0,0,0] op_sel_hi:[1,0,0]\n"
    "v_fma_mix_f32 v245, v114, v213, v221 op_sel:[1,0,0] op_sel_hi:[1,0,0]\n"
    "v_mfma_f32_16x16x32_bf16 a[16:19], v[4:7], v[96:99], a[16:19]\n"
    "ds_read_b128 v[204:207], v239 offset:3120\n"
    "v_fma_mix_f32 v246, v115, v214, v222 op_sel:[0,0,0] op_sel_hi:[1,0,0]\n"
    "v_fma_mix_f32 v247, v115, v215, v223 op_sel:[1,0,0] op_sel_hi:[1,0,0]\n"
    "v_mfma_f32_16x16x32_bf16 a[20:23], v[4:7], v[100:103], a[20:23]\n"
    "s_add_u32 m0, s61, 115584\n"
    "ds_read_b64_tr_b16 v[32:33], v231 offset:40960\n"
    "v_cvt_pk_bf16_f32 v114, v244, v245\n"
    "v_cvt_pk_bf16_f32 v115, v246, v247\n"
    "v_mfma_f32_16x16x32_bf16 a[24:27], v[4:7], v[104:107], a[24:27]\n"
    "buffer_load_dwordx4 v237, s[48:51], s65 offen offset:128 lds\n"
    "v_fma_mix_f32 v240, v116, v208, v216 op_sel:[0,0,0] op_sel_hi:[1,0,0]\n"
    "v_fma_mix_f32 v241, v116, v209, v217 op_sel:[1,0,0] op_sel_hi:[1,0,0]\n"
    "v_mfma_f32_16x16x32_bf16 a[28:31], v[4:7], v[108:111], a[28:31]\n"
    "ds_read_b64_tr_b16 v[34:35], v231 offset:45056\n"
    "v_fma_mix_f32 v242, v117, v210, v218 op_sel:[0,0,0] op_sel_hi:[1,0,0]\n"
    "v_fma_mix_f32 v243, v117, v211, v219 op_sel:[1,0,0] op_sel_hi:[1,0,0]\n"
    "v_mfma_f32_16x16x32_bf16 a[32:35], v[8:11], v[96:99], a[32:35]\n"
    "s_add_u32 s65, s66, 3735552\n"
    "s_add_u32 m0, s61, 116736\n"
    "ds_read_b64_tr_b16 v[36:37], v231 offset:40968\n"
    "v_cvt_pk_bf16_f32 v116, v240, v241\n"
    "v_cvt_pk_bf16_f32 v117, v242, v243\n"
    "v_mfma_f32_16x16x32_bf16 a[36:39], v[8:11], v[100:103], a[36:39]\n"
    "buffer_load_dwordx4 v237, s[48:51], s65 offen lds\n"
    "v_fma_mix_f32 v244, v118, v212, v220 op_sel:[0,0,0] op_sel_hi:[1,0,0]\n"
    "v_fma_mix_f32 v245, v118, v213, v221 op_sel:[1,0,0] op_sel_hi:[1,0,0]\n"
    "v_mfma_f32_16x16x32_bf16 a[40:43], v[8:11], v[104:107], a[40:43]\n"
    "ds_read_b64_tr_b16 v[38:39], v231 offset:45064\n"
    "v_fma_mix_f32 v246, v119, v214, v222 op_sel:[0,0,0] op_sel_hi:[1,0,0]\n"
    "v_fma_mix_f32 v247, v119, v215, v223 op_sel:[1,0,0] op_sel_hi:[1,0,0]\n"
    "v_mfma_f32_16x16x32_bf16 a[44:47], v[8:11], v[108:111], a[44:47]\n"
    "s_add_u32 m0, s61, 117632\n"
    "ds_read_b64_tr_b16 v[40:41], v231 offset:41472\n"
    "v_cvt_pk_bf16_f32 v118, v244, v245\n"
    "v_cvt_pk_bf16_f32 v119, v246, v247\n"
    "v_mfma_f32_16x16x32_bf16 a[48:51], v[12:15], v[96:99], a[48:51]\n"
    "buffer_load_dwordx4 v237, s[48:51], s65 offen offset:128 lds\n"
    "v_fma_mix_f32 v240, v120, v208, v216 op_sel:[0,0,0] op_sel_hi:[1,0,0]\n"
    "v_mfma_f32_16x16x32_bf16 a[52:55], v[12:15], v[100:103], a[52:55]\n"
    "ds_read_b64_tr_b16 v[42:43], v231 offset:45568\n"
    "v_fma_mix_f32 v241, v120, v209, v217 op_sel:[1,0,0] op_sel_hi:[1,0,0]\n"
    "v_mfma_f32_16x16x32_bf16 a[56:59], v[12:15], v[104:107], a[56:59]\n"
    "s_mov_b32 s67, 12288\n"
    "ds_read_b64_tr_b16 v[44:45], v231 offset:41480\n"
    "v_fma_mix_f32 v242, v121, v210, v218 op_sel:[0,0,0] op_sel_hi:[1,0,0]\n"
    "v_mfma_f32_16x16x32_bf16 a[60:63], v[12:15], v[108:111], a[60:63]\n"
    "buffer_load_dwordx4 v[160:163], v238, s[52:55], s67 offen offset:2048\n"
    "v_fma_mix_f32 v243, v121, v211, v219 op_sel:[1,0,0] op_sel_hi:[1,0,0]\n"
    "v_mfma_f32_16x16x32_bf16 a[64:67], v[16:19], v[96:99], a[64:67]\n"
    "ds_read_b64_tr_b16 v[46:47], v231 offset:45576\n"
    "v_cvt_pk_bf16_f32 v120, v240, v241\n"
    "v_mfma_f32_16x16x32_bf16 a[68:71], v[16:19], v[100:103], a[68:71]\n"
    "s_mov_b32 s67, 28672\n"
    "ds_read_b64_tr_b16 v[48:49], v231 offset:41984\n"
    "v_cvt_pk_bf16_f32 v121, v242, v243\n"
    "v_mfma_f32_16x16x32_bf16 a[72:75], v[16:19], v[104:107], a[72:75]\n"
    "buffer_load_dwordx4 v[164:167], v238, s[52:55], s67 offen offset:2048\n"
    "v_fma_mix_f32 v244, v122, v212, v220 op_sel:[0,0,0] op_sel_hi:[1,0,0]\n"
    "v_mfma_f32_16x16x32_bf16 a[76:79], v[16:19], v[108:111], a[76:79]\n"
    "ds_read_b64_tr_b16 v[50:51], v231 offset:46080\n"
    "v_fma_mix_f32 v245, v122, v213, v221 op_sel:[1,0,0] op_sel_hi:[1,0,0]\n"
    "v_mfma_f32_16x16x32_bf16 a[80:83], v[20:23], v[96:99], a[80:83]\n"
    "s_mov_b32 s67, 45056\n"
    "ds_read_b64_tr_b16 v[52:53], v231 offset:41992\n"
    "v_fma_mix_f32 v246, v123, v214, v222 op_sel:[0,0,0] op_sel_hi:[1,0,0]\n"
    "v_mfma_f32_16x16x32_bf16 a[84:87], v[20:23], v[100:103], a[84:87]\n"
    "buffer_load_dwordx4 v[168:171], v238, s[52:55], s67 offen offset:2048\n"
    "v_fma_mix_f32 v247, v123, v215, v223 op_sel:[1,0,0] op_sel_hi:[1,0,0]\n"
    "v_mfma_f32_16x16x32_bf16 a[88:91], v[20:23], v[104:107], a[88:91]\n"
    "ds_read_b64_tr_b16 v[54:55], v231 offset:46088\n"
    "v_cvt_pk_bf16_f32 v122, v244, v245\n"
    "v_mfma_f32_16x16x32_bf16 a[92:95], v[20:23], v[108:111], a[92:95]\n"
    "s_mov_b32 s67, 61440\n"
    "ds_read_b64_tr_b16 v[56:57], v231 offset:42496\n"
    "v_cvt_pk_bf16_f32 v123, v246, v247\n"
    "v_mfma_f32_16x16x32_bf16 a[96:99], v[24:27], v[96:99], a[96:99]\n"
    "buffer_load_dwordx4 v[172:175], v238, s[52:55], s67 offen offset:2048\n"
    "v_fma_mix_f32 v240, v124, v208, v216 op_sel:[0,0,0] op_sel_hi:[1,0,0]\n"
    "v_mfma_f32_16x16x32_bf16 a[100:103], v[24:27], v[100:103], a[100:103]\n"
    "ds_read_b64_tr_b16 v[58:59], v231 offset:46592\n"
    "v_fma_mix_f32 v241, v124, v209, v217 op_sel:[1,0,0] op_sel_hi:[1,0,0]\n"
    "v_mfma_f32_16x16x32_bf16 a[104:107], v[24:27], v[104:107], a[104:107]\n"
    "ds_read_b64_tr_b16 v[60:61], v231 offset:42504\n"
    "v_fma_mix_f32 v242, v125, v210, v218 op_sel:[0,0,0] op_sel_hi:[1,0,0]\n"
    "v_mfma_f32_16x16x32_bf16 a[108:111], v[24:27], v[108:111], a[108:111]\n"
    "ds_read_b64_tr_b16 v[62:63], v231 offset:46600\n"
    "v_fma_mix_f32 v243, v125, v211, v219 op_sel:[1,0,0] op_sel_hi:[1,0,0]\n"
    "v_mfma_f32_16x16x32_bf16 a[112:115], v[28:31], v[96:99], a[112:115]\n"
    "v_cvt_pk_bf16_f32 v124, v240, v241\n"
    "v_cvt_pk_bf16_f32 v125, v242, v243\n"
    "v_mfma_f32_16x16x32_bf16 a[116:119], v[28:31], v[100:103], a[116:119]\n"
    "v_fma_mix_f32 v244, v126, v212, v220 op_sel:[0,0,0] op_sel_hi:[1,0,0]\n"
    "v_fma_mix_f32 v245, v126, v213, v221 op_sel:[1,0,0] op_sel_hi:[1,0,0]\n"
    "v_mfma_f32_16x16x32_bf16 a[120:123], v[28:31], v[104:107], a[120:123]\n"
    "v_fma_mix_f32 v246, v127, v214, v222 op_sel:[0,0,0] op_sel_hi:[1,0,0]\n"
    "v_fma_mix_f32 v247, v127, v215, v223 op_sel:[1,0,0] op_sel_hi:[1,0,0]\n"
    "v_mfma_f32_16x16x32_bf16 a[124:127], v[28:31], v[108:111], a[124:127]\n"
    "v_cvt_pk_bf16_f32 v126, v244, v245\n"
    "v_cvt_pk_bf16_f32 v127, v246, v247\n"
    "s_waitcnt lgkmcnt(0)\n"
    "s_waitcnt vmcnt(16)\n"
    "s_barrier\n"
    "s_add_u32 s68, s60, 16384\n"
    "v_mfma_f32_16x16x32_bf16 a[0:3], v[32:35], v[112:115], a[0:3]\n"
    "ds_read_b128 v[208:211], v239 offset:3328\n"
    "v_fma_mix_f32 v240, v128, v192, v200 op_sel:[0,0,0] op_sel_hi:[1,0,0]\n"
    "v_fma_mix_f32 v241, v128, v193, v201 op_sel:[1,0,0] op_sel_hi:[1,0,0]\n"
    "v_mfma_f32_16x16x32_bf16 a[4:7], v[32:35], v[116:119], a[4:7]\n"
    "ds_read_b128 v[212:215], v239 offset:3344\n"
    "v_fma_mix_f32 v242, v129, v194, v202 op_sel:[0,0,0] op_sel_hi:[1,0,0]\n"
    "v_fma_mix_f32 v243, v129, v195, v203 op_sel:[1,0,0] op_sel_hi:[1,0,0]\n"
    "v_mfma_f32_16x16x32_bf16 a[8:11], v[32:35], v[120:123], a[8:11]\n"
    "s_add_u32 m0, s62, 16384\n"
    "ds_read_b128 v[216:219], v239 offset:3360\n"
    "v_cvt_pk_bf16_f32 v128, v240, v241\n"
    "v_cvt_pk_bf16_f32 v129, v242, v243\n"
    "v_mfma_f32_16x16x32_bf16 a[12:15], v[32:35], v[124:127], a[12:15]\n"
    "buffer_load_dwordx4 v236, s[44:47], s68 offen lds\n"
    "v_fma_mix_f32 v244, v130, v196, v204 op_sel:[0,0,0] op_sel_hi:[1,0,0]\n"
    "v_fma_mix_f32 v245, v130, v197, v205 op_sel:[1,0,0] op_sel_hi:[1,0,0]\n"
    "v_mfma_f32_16x16x32_bf16 a[16:19], v[36:39], v[112:115], a[16:19]\n"
    "ds_read_b128 v[220:223], v239 offset:3376\n"
    "v_fma_mix_f32 v246, v131, v198, v206 op_sel:[0,0,0] op_sel_hi:[1,0,0]\n"
    "v_fma_mix_f32 v247, v131, v199, v207 op_sel:[1,0,0] op_sel_hi:[1,0,0]\n"
    "v_mfma_f32_16x16x32_bf16 a[20:23], v[36:39], v[116:119], a[20:23]\n"
    "s_add_u32 m0, s62, 17280\n"
    "ds_read_b64_tr_b16 v[0:1], v230 offset:49152\n"
    "v_cvt_pk_bf16_f32 v130, v244, v245\n"
    "v_cvt_pk_bf16_f32 v131, v246, v247\n"
    "v_mfma_f32_16x16x32_bf16 a[24:27], v[36:39], v[120:123], a[24:27]\n"
    "buffer_load_dwordx4 v236, s[44:47], s68 offen offset:128 lds\n"
    "v_fma_mix_f32 v240, v132, v192, v200 op_sel:[0,0,0] op_sel_hi:[1,0,0]\n"
    "v_fma_mix_f32 v241, v132, v193, v201 op_sel:[1,0,0] op_sel_hi:[1,0,0]\n"
    "v_mfma_f32_16x16x32_bf16 a[28:31], v[36:39], v[124:127], a[28:31]\n"
    "ds_read_b64_tr_b16 v[2:3], v230 offset:53248\n"
    "v_fma_mix_f32 v242, v133, v194, v202 op_sel:[0,0,0] op_sel_hi:[1,0,0]\n"
    "v_fma_mix_f32 v243, v133, v195, v203 op_sel:[1,0,0] op_sel_hi:[1,0,0]\n"
    "v_mfma_f32_16x16x32_bf16 a[32:35], v[40:43], v[112:115], a[32:35]\n"
    "s_add_u32 m0, s62, 16384\n"
    "ds_read_b64_tr_b16 v[4:5], v230 offset:49160\n"
    "v_cvt_pk_bf16_f32 v132, v240, v241\n"
    "v_cvt_pk_bf16_f32 v133, v242, v243\n"
    "v_mfma_f32_16x16x32_bf16 a[36:39], v[40:43], v[116:119], a[36:39]\n"
    "buffer_load_dwordx4 v236, s[44:47], s68 offen offset:2048 lds\n"
    "v_fma_mix_f32 v244, v134, v196, v204 op_sel:[0,0,0] op_sel_hi:[1,0,0]\n"
    "v_fma_mix_f32 v245, v134, v197, v205 op_sel:[1,0,0] op_sel_hi:[1,0,0]\n"
    "v_mfma_f32_16x16x32_bf16 a[40:43], v[40:43], v[120:123], a[40:43]\n"
    "ds_read_b64_tr_b16 v[6:7], v230 offset:53256\n"
    "v_fma_mix_f32 v246, v135, v198, v206 op_sel:[0,0,0] op_sel_hi:[1,0,0]\n"
    "v_fma_mix_f32 v247, v135, v199, v207 op_sel:[1,0,0] op_sel_hi:[1,0,0]\n"
    "v_mfma_f32_16x16x32_bf16 a[44:47], v[40:43], v[124:127], a[44:47]\n"
    "s_add_u32 m0, s62, 17280\n"
    "ds_read_b64_tr_b16 v[8:9], v230 offset:49664\n"
    "v_cvt_pk_bf16_f32 v134, v244, v245\n"
    "v_cvt_pk_bf16_f32 v135, v246, v247\n"
    "v_mfma_f32_16x16x32_bf16 a[48:51], v[44:47], v[112:115], a[48:51]\n"
    "buffer_load_dwordx4 v236, s[44:47], s68 offen offset:2176 lds\n"
    "v_fma_mix_f32 v240, v136, v192, v200 op_sel:[0,0,0] op_sel_hi:[1,0,0]\n"
    "v_mfma_f32_16x16x32_bf16 a[52:55], v[44:47], v[116:119], a[52:55]\n"
    "ds_read_b64_tr_b16 v[10:11], v230 offset:53760\n"
    "v_fma_mix_f32 v241, v136, v193, v201 op_sel:[1,0,0] op_sel_hi:[1,0,0]\n"
    "v_mfma_f32_16x16x32_bf16 a[56:59], v[44:47], v[120:123], a[56:59]\n"
    "s_mov_b32 s67, 12288\n"
    "ds_read_b64_tr_b16 v[12:13], v230 offset:49672\n"
    "v_fma_mix_f32 v242, v137, v194, v202 op_sel:[0,0,0] op_sel_hi:[1,0,0]\n"
    "v_mfma_f32_16x16x32_bf16 a[60:63], v[44:47], v[124:127], a[60:63]\n"
    "buffer_load_dwordx4 v[176:179], v238, s[52:55], s67 offen offset:3072\n"
    "v_fma_mix_f32 v243, v137, v195, v203 op_sel:[1,0,0] op_sel_hi:[1,0,0]\n"
    "v_mfma_f32_16x16x32_bf16 a[64:67], v[48:51], v[112:115], a[64:67]\n"
    "ds_read_b64_tr_b16 v[14:15], v230 offset:53768\n"
    "v_cvt_pk_bf16_f32 v136, v240, v241\n"
    "v_mfma_f32_16x16x32_bf16 a[68:71], v[48:51], v[116:119], a[68:71]\n"
    "s_mov_b32 s67, 28672\n"
    "ds_read_b64_tr_b16 v[16:17], v230 offset:50176\n"
    "v_cvt_pk_bf16_f32 v137, v242, v243\n"
    "v_mfma_f32_16x16x32_bf16 a[72:75], v[48:51], v[120:123], a[72:75]\n"
    "buffer_load_dwordx4 v[180:183], v238, s[52:55], s67 offen offset:3072\n"
    "v_fma_mix_f32 v244, v138, v196, v204 op_sel:[0,0,0] op_sel_hi:[1,0,0]\n"
    "v_mfma_f32_16x16x32_bf16 a[76:79], v[48:51], v[124:127], a[76:79]\n"
    "ds_read_b64_tr_b16 v[18:19], v230 offset:54272\n"
    "v_fma_mix_f32 v245, v138, v197, v205 op_sel:[1,0,0] op_sel_hi:[1,0,0]\n"
    "v_mfma_f32_16x16x32_bf16 a[80:83], v[52:55], v[112:115], a[80:83]\n"
    "s_mov_b32 s67, 45056\n"
    "ds_read_b64_tr_b16 v[20:21], v230 offset:50184\n"
    "v_fma_mix_f32 v246, v139, v198, v206 op_sel:[0,0,0] op_sel_hi:[1,0,0]\n"
    "v_mfma_f32_16x16x32_bf16 a[84:87], v[52:55], v[116:119], a[84:87]\n"
    "buffer_load_dwordx4 v[184:187], v238, s[52:55], s67 offen offset:3072\n"
    "v_fma_mix_f32 v247, v139, v199, v207 op_sel:[1,0,0] op_sel_hi:[1,0,0]\n"
    "v_mfma_f32_16x16x32_bf16 a[88:91], v[52:55], v[120:123], a[88:91]\n"
    "ds_read_b64_tr_b16 v[22:23], v230 offset:54280\n"
    "v_cvt_pk_bf16_f32 v138, v244, v245\n"
    "v_mfma_f32_16x16x32_bf16 a[92:95], v[52:55], v[124:127], a[92:95]\n"
    "s_mov_b32 s67, 61440\n"
    "ds_read_b64_tr_b16 v[24:25], v230 offset:50688\n"
    "v_cvt_pk_bf16_f32 v139, v246, v247\n"
    "v_mfma_f32_16x16x32_bf16 a[96:99], v[56:59], v[112:115], a[96:99]\n"
    "buffer_load_dwordx4 v[188:191], v238, s[52:55], s67 offen offset:3072\n"
    "v_fma_mix_f32 v240, v140, v192, v200 op_sel:[0,0,0] op_sel_hi:[1,0,0]\n"
    "v_mfma_f32_16x16x32_bf16 a[100:103], v[56:59], v[116:119], a[100:103]\n"
    "ds_read_b64_tr_b16 v[26:27], v230 offset:54784\n"
    "v_fma_mix_f32 v241, v140, v193, v201 op_sel:[1,0,0] op_sel_hi:[1,0,0]\n"
    "v_mfma_f32_16x16x32_bf16 a[104:107], v[56:59], v[120:123], a[104:107]\n"
    "ds_read_b64_tr_b16 v[28:29], v230 offset:50696\n"
    "v_fma_mix_f32 v242, v141, v194, v202 op_sel:[0,0,0] op_sel_hi:[1,0,0]\n"
    "v_mfma_f32_16x16x32_bf16 a[108:111], v[56:59], v[124:127], a[108:111]\n"
    "ds_read_b64_tr_b16 v[30:31], v230 offset:54792\n"
    "v_fma_mix_f32 v243, v141, v195, v203 op_sel:[1,0,0] op_sel_hi:[1,0,0]\n"
    "v_mfma_f32_16x16x32_bf16 a[112:115], v[60:63], v[112:115], a[112:115]\n"
    "v_cvt_pk_bf16_f32 v140, v240, v241\n"
    "v_cvt_pk_bf16_f32 v141, v242, v243\n"
    "v_mfma_f32_16x16x32_bf16 a[116:119], v[60:63], v[116:119], a[116:119]\n"
    "v_fma_mix_f32 v244, v142, v196, v204 op_sel:[0,0,0] op_sel_hi:[1,0,0]\n"
    "v_fma_mix_f32 v245, v142, v197, v205 op_sel:[1,0,0] op_sel_hi:[1,0,0]\n"
    "v_mfma_f32_16x16x32_bf16 a[120:123], v[60:63], v[120:123], a[120:123]\n"
    "v_fma_mix_f32 v246, v143, v198, v206 op_sel:[0,0,0] op_sel_hi:[1,0,0]\n"
    "v_fma_mix_f32 v247, v143, v199, v207 op_sel:[1,0,0] op_sel_hi:[1,0,0]\n"
    "v_mfma_f32_16x16x32_bf16 a[124:127], v[60:63], v[124:127], a[124:127]\n"
    "v_cvt_pk_bf16_f32 v142, v244, v245\n"
    "v_cvt_pk_bf16_f32 v143, v246, v247\n"
    "s_waitcnt lgkmcnt(0)\n"
    "s_waitcnt vmcnt(16)\n"
    "v_mfma_f32_16x16x32_bf16 a[0:3], v[0:3], v[128:131], a[0:3]\n"
    "ds_read_b128 v[192:195], v239 offset:3584\n"
    "v_fma_mix_f32 v240, v144, v208, v216 op_sel:[0,0,0] op_sel_hi:[1,0,0]\n"
    "v_fma_mix_f32 v241, v144, v209, v217 op_sel:[1,0,0] op_sel_hi:[1,0,0]\n"
    "v_mfma_f32_16x16x32_bf16 a[4:7], v[0:3], v[132:135], a[4:7]\n"
    "ds_read_b128 v[196:199], v239 offset:3600\n"
    "v_fma_mix_f32 v242, v145, v210, v218 op_sel:[0,0,0] op_sel_hi:[1,0,0]\n"
    "v_fma_mix_f32 v243, v145, v211, v219 op_sel:[1,0,0] op_sel_hi:[1,0,0]\n"
    "v_mfma_f32_16x16x32_bf16 a[8:11], v[0:3], v[136:139], a[8:11]\n"
    "ds_read_b128 v[200:203], v239 offset:3616\n"
    "v_cvt_pk_bf16_f32 v144, v240, v241\n"
    "v_cvt_pk_bf16_f32 v145, v242, v243\n"
    "v_mfma_f32_16x16x32_bf16 a[12:15], v[0:3], v[140:143], a[12:15]\n"
    "ds_read_b128 v[204:207], v239 offset:3632\n"
    "v_fma_mix_f32 v244, v146, v212, v220 op_sel:[0,0,0] op_sel_hi:[1,0,0]\n"
    "v_fma_mix_f32 v245, v146, v213, v221 op_sel:[1,0,0] op_sel_hi:[1,0,0]\n"
    "v_mfma_f32_16x16x32_bf16 a[16:19], v[4:7], v[128:131], a[16:19]\n"
    "ds_read_b64_tr_b16 v[32:33], v230 offset:57344\n"
    "v_fma_mix_f32 v246, v147, v214, v222 op_sel:[0,0,0] op_sel_hi:[1,0,0]\n"
    "v_mfma_f32_16x16x32_bf16 a[20:23], v[4:7], v[132:135], a[20:23]\n"
    "ds_read_b64_tr_b16 v[34:35], v230 offset:61440\n"
    "v_fma_mix_f32 v247, v147, v215, v223 op_sel:[1,0,0] op_sel_hi:[1,0,0]\n"
    "v_mfma_f32_16x16x32_bf16 a[24:27], v[4:7], v[136:139], a[24:27]\n"
    "ds_read_b64_tr_b16 v[36:37], v230 offset:57352\n"
    "v_cvt_pk_bf16_f32 v146, v244, v245\n"
    "v_mfma_f32_16x16x32_bf16 a[28:31], v[4:7], v[140:143], a[28:31]\n"
    "ds_read_b64_tr_b16 v[38:39], v230 offset:61448\n"
    "v_cvt_pk_bf16_f32 v147, v246, v247\n"
    "v_mfma_f32_16x16x32_bf16 a[32:35], v[8:11], v[128:131], a[32:35]\n"
    "ds_read_b64_tr_b16 v[40:41], v230 offset:57856\n"
    "v_fma_mix_f32 v240, v148, v208, v216 op_sel:[0,0,0] op_sel_hi:[1,0,0]\n"
    "v_mfma_f32_16x16x32_bf16 a[36:39], v[8:11], v[132:135], a[36:39]\n"
    "ds_read_b64_tr_b16 v[42:43], v230 offset:61952\n"
    "v_fma_mix_f32 v241, v148, v209, v217 op_sel:[1,0,0] op_sel_hi:[1,0,0]\n"
    "v_mfma_f32_16x16x32_bf16 a[40:43], v[8:11], v[136:139], a[40:43]\n"
    "ds_read_b64_tr_b16 v[44:45], v230 offset:57864\n"
    "v_fma_mix_f32 v242, v149, v210, v218 op_sel:[0,0,0] op_sel_hi:[1,0,0]\n"
    "v_mfma_f32_16x16x32_bf16 a[44:47], v[8:11], v[140:143], a[44:47]\n"
    "ds_read_b64_tr_b16 v[46:47], v230 offset:61960\n"
    "v_fma_mix_f32 v243, v149, v211, v219 op_sel:[1,0,0] op_sel_hi:[1,0,0]\n"
    "v_mfma_f32_16x16x32_bf16 a[48:51], v[12:15], v[128:131], a[48:51]\n"
    "ds_read_b64_tr_b16 v[48:49], v230 offset:58368\n"
    "v_cvt_pk_bf16_f32 v148, v240, v241\n"
    "v_mfma_f32_16x16x32_bf16 a[52:55], v[12:15], v[132:135], a[52:55]\n"
    "ds_read_b64_tr_b16 v[50:51], v230 offset:62464\n"
    "v_cvt_pk_bf16_f32 v149, v242, v243\n"
    "v_mfma_f32_16x16x32_bf16 a[56:59], v[12:15], v[136:139], a[56:59]\n"
    "ds_read_b64_tr_b16 v[52:53], v230 offset:58376\n"
    "v_fma_mix_f32 v244, v150, v212, v220 op_sel:[0,0,0] op_sel_hi:[1,0,0]\n"
    "v_mfma_f32_16x16x32_bf16 a[60:63], v[12:15], v[140:143], a[60:63]\n"
    "ds_read_b64_tr_b16 v[54:55], v230 offset:62472\n"
    "v_fma_mix_f32 v245, v150, v213, v221 op_sel:[1,0,0] op_sel_hi:[1,0,0]\n"
    "v_mfma_f32_16x16x32_bf16 a[64:67], v[16:19], v[128:131], a[64:67]\n"
    "ds_read_b64_tr_b16 v[56:57], v230 offset:58880\n"
    "v_fma_mix_f32 v246, v151, v214, v222 op_sel:[0,0,0] op_sel_hi:[1,0,0]\n"
    "v_mfma_f32_16x16x32_bf16 a[68:71], v[16:19], v[132:135], a[68:71]\n"
    "ds_read_b64_tr_b16 v[58:59], v230 offset:62976\n"
    "v_fma_mix_f32 v247, v151, v215, v223 op_sel:[1,0,0] op_sel_hi:[1,0,0]\n"
    "v_mfma_f32_16x16x32_bf16 a[72:75], v[16:19], v[136:139], a[72:75]\n"
    "ds_read_b64_tr_b16 v[60:61], v230 offset:58888\n"
    "v_cvt_pk_bf16_f32 v150, v244, v245\n"
    "v_mfma_f32_16x16x32_bf16 a[76:79], v[16:19], v[140:143], a[76:79]\n"
    "ds_read_b64_tr_b16 v[62:63], v230 offset:62984\n"
    "v_cvt_pk_bf16_f32 v151, v246, v247\n"
    "v_mfma_f32_16x16x32_bf16 a[80:83], v[20:23], v[128:131], a[80:83]\n"
    "v_fma_mix_f32 v240, v152, v208, v216 op_sel:[0,0,0] op_sel_hi:[1,0,0]\n"
    "v_fma_mix_f32 v241, v152, v209, v217 op_sel:[1,0,0] op_sel_hi:[1,0,0]\n"
    "v_mfma_f32_16x16x32_bf16 a[84:87], v[20:23], v[132:135], a[84:87]\n"
    "v_fma_mix_f32 v242, v153, v210, v218 op_sel:[0,0,0] op_sel_hi:[1,0,0]\n"
    "v_fma_mix_f32 v243, v153, v211, v219 op_sel:[1,0,0] op_sel_hi:[1,0,0]\n"
    "v_mfma_f32_16x16x32_bf16 a[88:91], v[20:23], v[136:139], a[88:91]\n"
    "v_cvt_pk_bf16_f32 v152, v240, v241\n"
    "v_cvt_pk_bf16_f32 v153, v242, v243\n"
    "v_mfma_f32_16x16x32_bf16 a[92:95], v[20:23], v[140:143], a[92:95]\n"
    "v_fma_mix_f32 v244, v154, v212, v220 op_sel:[0,0,0] op_sel_hi:[1,0,0]\n"
    "v_fma_mix_f32 v245, v154, v213, v221 op_sel:[1,0,0] op_sel_hi:[1,0,0]\n"
    "v_mfma_f32_16x16x32_bf16 a[96:99], v[24:27], v[128:131], a[96:99]\n"
    "v_fma_mix_f32 v246, v155, v214, v222 op_sel:[0,0,0] op_sel_hi:[1,0,0]\n"
    "v_fma_mix_f32 v247, v155, v215, v223 op_sel:[1,0,0] op_sel_hi:[1,0,0]\n"
    "v_mfma_f32_16x16x32_bf16 a[100:103], v[24:27], v[132:135], a[100:103]\n"
    "v_cvt_pk_bf16_f32 v154, v244, v245\n"
    "v_cvt_pk_bf16_f32 v155, v246, v247\n"
    "v_mfma_f32_16x16x32_bf16 a[104:107], v[24:27], v[136:139], a[104:107]\n"
    "v_fma_mix_f32 v240, v156, v208, v216 op_sel:[0,0,0] op_sel_hi:[1,0,0]\n"
    "v_fma_mix_f32 v241, v156, v209, v217 op_sel:[1,0,0] op_sel_hi:[1,0,0]\n"
    "v_mfma_f32_16x16x32_bf16 a[108:111], v[24:27], v[140:143], a[108:111]\n"
    "v_fma_mix_f32 v242, v157, v210, v218 op_sel:[0,0,0] op_sel_hi:[1,0,0]\n"
    "v_fma_mix_f32 v243, v157, v211, v219 op_sel:[1,0,0] op_sel_hi:[1,0,0]\n"
    "v_mfma_f32_16x16x32_bf16 a[112:115], v[28:31], v[128:131], a[112:115]\n"
    "v_cvt_pk_bf16_f32 v156, v240, v241\n"
    "v_cvt_pk_bf16_f32 v157, v242, v243\n"
    "v_mfma_f32_16x16x32_bf16 a[116:119], v[28:31], v[132:135], a[116:119]\n"
    "v_fma_mix_f32 v244, v158, v212, v220 op_sel:[0,0,0] op_sel_hi:[1,0,0]\n"
    "v_fma_mix_f32 v245, v158, v213, v221 op_sel:[1,0,0] op_sel_hi:[1,0,0]\n"
    "v_mfma_f32_16x16x32_bf16 a[120:123], v[28:31], v[136:139], a[120:123]\n"
    "v_fma_mix_f32 v246, v159, v214, v222 op_sel:[0,0,0] op_sel_hi:[1,0,0]\n"
    "v_fma_mix_f32 v247, v159, v215, v223 op_sel:[1,0,0] op_sel_hi:[1,0,0]\n"
    "v_mfma_f32_16x16x32_bf16 a[124:127], v[28:31], v[140:143], a[124:127]\n"
    "v_cvt_pk_bf16_f32 v158, v244, v245\n"
    "v_cvt_pk_bf16_f32 v159, v246, v247\n"
    "s_waitcnt lgkmcnt(0)\n"
    "s_waitcnt vmcnt(8)\n"
    "s_barrier\n"
    "s_add_u32 s68, s60, 32768\n"
    "v_mfma_f32_16x16x32_bf16 a[0:3], v[32:35], v[144:147], a[0:3]\n"
    "ds_read_b128 v[208:211], v239 offset:3840\n"
    "v_fma_mix_f32 v240, v160, v192, v200 op_sel:[0,0,0] op_sel_hi:[1,0,0]\n"
    "v_fma_mix_f32 v241, v160, v193, v201 op_sel:[1,0,0] op_sel_hi:[1,0,0]\n"
    "v_mfma_f32_16x16x32_bf16 a[4:7], v[32:35], v[148:151], a[4:7]\n"
    "ds_read_b128 v[212:215], v239 offset:3856\n"
    "v_fma_mix_f32 v242, v161, v194, v202 op_sel:[0,0,0] op_sel_hi:[1,0,0]\n"
    "v_fma_mix_f32 v243, v161, v195, v203 op_sel:[1,0,0] op_sel_hi:[1,0,0]\n"
    "v_mfma_f32_16x16x32_bf16 a[8:11], v[32:35], v[152:155], a[8:11]\n"
    "s_add_u32 m0, s61, 32768\n"
    "ds_read_b128 v[216:219], v239 offset:3872\n"
    "v_cvt_pk_bf16_f32 v160, v240, v241\n"
    "v_cvt_pk_bf16_f32 v161, v242, v243\n"
    "v_mfma_f32_16x16x32_bf16 a[12:15], v[32:35], v[156:159], a[12:15]\n"
    "buffer_load_dwordx4 v232, s[40:43], s68 offen lds\n"
    "v_fma_mix_f32 v244, v162, v196, v204 op_sel:[0,0,0] op_sel_hi:[1,0,0]\n"
    "v_fma_mix_f32 v245, v162, v197, v205 op_sel:[1,0,0] op_sel_hi:[1,0,0]\n"
    "v_mfma_f32_16x16x32_bf16 a[16:19], v[36:39], v[144:147], a[16:19]\n"
    "ds_read_b128 v[220:223], v239 offset:3888\n"
    "v_fma_mix_f32 v246, v163, v198, v206 op_sel:[0,0,0] op_sel_hi:[1,0,0]\n"
    "v_fma_mix_f32 v247, v163, v199, v207 op_sel:[1,0,0] op_sel_hi:[1,0,0]\n"
    "v_mfma_f32_16x16x32_bf16 a[20:23], v[36:39], v[148:151], a[20:23]\n"
    "buffer_load_dwordx4 v233, s[40:43], s68 offen offset:1024 lds\n"
    "v_cvt_pk_bf16_f32 v162, v244, v245\n"
    "v_cvt_pk_bf16_f32 v163, v246, v247\n"
    "v_mfma_f32_16x16x32_bf16 a[24:27], v[36:39], v[152:155], a[24:27]\n"
    "ds_read_b64_tr_b16 v[0:1], v231 offset:49152\n"
    "v_fma_mix_f32 v240, v164, v192, v200 op_sel:[0,0,0] op_sel_hi:[1,0,0]\n"
    "v_fma_mix_f32 v241, v164, v193, v201 op_sel:[1,0,0] op_sel_hi:[1,0,0]\n"
    "v_mfma_f32_16x16x32_bf16 a[28:31], v[36:39], v[156:159], a[28:31]\n"
    "ds_read_b64_tr_b16 v[2:3], v231 offset:53248\n"
    "v_fma_mix_f32 v242, v165, v194, v202 op_sel:[0,0,0] op_sel_hi:[1,0,0]\n"
    "v_fma_mix_f32 v243, v165, v195, v203 op_sel:[1,0,0] op_sel_hi:[1,0,0]\n"
    "v_mfma_f32_16x16x32_bf16 a[32:35], v[40:43], v[144:147], a[32:35]\n"
    "buffer_load_dwordx4 v234, s[40:43], s68 offen offset:2048 lds\n"
    "v_cvt_pk_bf16_f32 v164, v240, v241\n"
    "v_mfma_f32_16x16x32_bf16 a[36:39], v[40:43], v[148:151], a[36:39]\n"
    "ds_read_b64_tr_b16 v[4:5], v231 offset:49160\n"
    "v_cvt_pk_bf16_f32 v165, v242, v243\n"
    "v_mfma_f32_16x16x32_bf16 a[40:43], v[40:43], v[152:155], a[40:43]\n"
    "ds_read_b64_tr_b16 v[6:7], v231 offset:53256\n"
    "v_fma_mix_f32 v244, v166, v196, v204 op_sel:[0,0,0] op_sel_hi:[1,0,0]\n"
    "v_mfma_f32_16x16x32_bf16 a[44:47], v[40:43], v[156:159], a[44:47]\n"
    "buffer_load_dwordx4 v235, s[40:43], s68 offen offset:3072 lds\n"
    "v_fma_mix_f32 v245, v166, v197, v205 op_sel:[1,0,0] op_sel_hi:[1,0,0]\n"
    "v_mfma_f32_16x16x32_bf16 a[48:51], v[44:47], v[144:147], a[48:51]\n"
    "ds_read_b64_tr_b16 v[8:9], v231 offset:49664\n"
    "v_fma_mix_f32 v246, v167, v198, v206 op_sel:[0,0,0] op_sel_hi:[1,0,0]\n"
    "v_mfma_f32_16x16x32_bf16 a[52:55], v[44:47], v[148:151], a[52:55]\n"
    "ds_read_b64_tr_b16 v[10:11], v231 offset:53760\n"
    "v_fma_mix_f32 v247, v167, v199, v207 op_sel:[1,0,0] op_sel_hi:[1,0,0]\n"
    "v_mfma_f32_16x16x32_bf16 a[56:59], v[44:47], v[152:155], a[56:59]\n"
    "ds_read_b64_tr_b16 v[12:13], v231 offset:49672\n"
    "v_cvt_pk_bf16_f32 v166, v244, v245\n"
    "v_mfma_f32_16x16x32_bf16 a[60:63], v[44:47], v[156:159], a[60:63]\n"
    "ds_read_b64_tr_b16 v[14:15], v231 offset:53768\n"
    "v_cvt_pk_bf16_f32 v167, v246, v247\n"
    "v_mfma_f32_16x16x32_bf16 a[64:67], v[48:51], v[144:147], a[64:67]\n"
    "ds_read_b64_tr_b16 v[16:17], v231 offset:50176\n"
    "v_fma_mix_f32 v240, v168, v192, v200 op_sel:[0,0,0] op_sel_hi:[1,0,0]\n"
    "v_mfma_f32_16x16x32_bf16 a[68:71], v[48:51], v[148:151], a[68:71]\n"
    "ds_read_b64_tr_b16 v[18:19], v231 offset:54272\n"
    "v_fma_mix_f32 v241, v168, v193, v201 op_sel:[1,0,0] op_sel_hi:[1,0,0]\n"
    "v_mfma_f32_16x16x32_bf16 a[72:75], v[48:51], v[152:155], a[72:75]\n"
    "ds_read_b64_tr_b16 v[20:21], v231 offset:50184\n"
    "v_fma_mix_f32 v242, v169, v194, v202 op_sel:[0,0,0] op_sel_hi:[1,0,0]\n"
    "v_mfma_f32_16x16x32_bf16 a[76:79], v[48:51], v[156:159], a[76:79]\n"
    "ds_read_b64_tr_b16 v[22:23], v231 offset:54280\n"
    "v_fma_mix_f32 v243, v169, v195, v203 op_sel:[1,0,0] op_sel_hi:[1,0,0]\n"
    "v_mfma_f32_16x16x32_bf16 a[80:83], v[52:55], v[144:147], a[80:83]\n"
    "ds_read_b64_tr_b16 v[24:25], v231 offset:50688\n"
    "v_cvt_pk_bf16_f32 v168, v240, v241\n"
    "v_mfma_f32_16x16x32_bf16 a[84:87], v[52:55], v[148:151], a[84:87]\n"
    "ds_read_b64_tr_b16 v[26:27], v231 offset:54784\n"
    "v_cvt_pk_bf16_f32 v169, v242, v243\n"
    "v_mfma_f32_16x16x32_bf16 a[88:91], v[52:55], v[152:155], a[88:91]\n"
    "ds_read_b64_tr_b16 v[28:29], v231 offset:50696\n"
    "v_fma_mix_f32 v244, v170, v196, v204 op_sel:[0,0,0] op_sel_hi:[1,0,0]\n"
    "v_mfma_f32_16x16x32_bf16 a[92:95], v[52:55], v[156:159], a[92:95]\n"
    "ds_read_b64_tr_b16 v[30:31], v231 offset:54792\n"
    "v_fma_mix_f32 v245, v170, v197, v205 op_sel:[1,0,0] op_sel_hi:[1,0,0]\n"
    "v_mfma_f32_16x16x32_bf16 a[96:99], v[56:59], v[144:147], a[96:99]\n"
    "v_fma_mix_f32 v246, v171, v198, v206 op_sel:[0,0,0] op_sel_hi:[1,0,0]\n"
    "v_fma_mix_f32 v247, v171, v199, v207 op_sel:[1,0,0] op_sel_hi:[1,0,0]\n"
    "v_mfma_f32_16x16x32_bf16 a[100:103], v[56:59], v[148:151], a[100:103]\n"
    "v_cvt_pk_bf16_f32 v170, v244, v245\n"
    "v_cvt_pk_bf16_f32 v171, v246, v247\n"
    "v_mfma_f32_16x16x32_bf16 a[104:107], v[56:59], v[152:155], a[104:107]\n"
    "v_fma_mix_f32 v240, v172, v192, v200 op_sel:[0,0,0] op_sel_hi:[1,0,0]\n"
    "v_fma_mix_f32 v241, v172, v193, v201 op_sel:[1,0,0] op_sel_hi:[1,0,0]\n"
    "v_mfma_f32_16x16x32_bf16 a[108:111], v[56:59], v[156:159], a[108:111]\n"
    "v_fma_mix_f32 v242, v173, v194, v202 op_sel:[0,0,0] op_sel_hi:[1,0,0]\n"
    "v_fma_mix_f32 v243, v173, v195, v203 op_sel:[1,0,0] op_sel_hi:[1,0,0]\n"
    "v_mfma_f32_16x16x32_bf16 a[112:115], v[60:63], v[144:147], a[112:115]\n"
    "v_cvt_pk_bf16_f32 v172, v240, v241\n"
    "v_cvt_pk_bf16_f32 v173, v242, v243\n"
    "v_mfma_f32_16x16x32_bf16 a[116:119], v[60:63], v[148:151], a[116:119]\n"
    "v_fma_mix_f32 v244, v174, v196, v204 op_sel:[0,0,0] op_sel_hi:[1,0,0]\n"
    "v_fma_mix_f32 v245, v174, v197, v205 op_sel:[1,0,0] op_sel_hi:[1,0,0]\n"
    "v_mfma_f32_16x16x32_bf16 a[120:123], v[60:63], v[152:155], a[120:123]\n"
    "v_fma_mix_f32 v246, v175, v198, v206 op_sel:[0,0,0] op_sel_hi:[1,0,0]\n"
    "v_fma_mix_f32 v247, v175, v199, v207 op_sel:[1,0,0] op_sel_hi:[1,0,0]\n"
    "v_mfma_f32_16x16x32_bf16 a[124:127], v[60:63], v[156:159], a[124:127]\n"
    "v_cvt_pk_bf16_f32 v174, v244, v245\n"
    "v_cvt_pk_bf16_f32 v175, v246, v247\n"
    "s_waitcnt lgkmcnt(0)\n"
    "s_waitcnt vmcnt(4)\n"
    "v_mfma_f32_16x16x32_bf16 a[0:3], v[0:3], v[160:163], a[0:3]\n"
    "ds_read_b64_tr_b16 v[32:33], v231 offset:57344\n"
    "v_fma_mix_f32 v240, v176, v208, v216 op_sel:[0,0,0] op_sel_hi:[1,0,0]\n"
    "v_mfma_f32_16x16x32_bf16 a[4:7], v[0:3], v[164:167], a[4:7]\n"
    "ds_read_b64_tr_b16 v[34:35], v231 offset:61440\n"
    "v_fma_mix_f32 v241, v176, v209, v217 op_sel:[1,0,0] op_sel_hi:[1,0,0]\n"
    "v_mfma_f32_16x16x32_bf16 a[8:11], v[0:3], v[168:171], a[8:11]\n"
    "ds_read_b64_tr_b16 v[36:37], v231 offset:57352\n"
    "v_fma_mix_f32 v242, v177, v210, v218 op_sel:[0,0,0] op_sel_hi:[1,0,0]\n"
    "v_mfma_f32_16x16x32_bf16 a[12:15], v[0:3], v[172:175], a[12:15]\n"
    "ds_read_b64_tr_b16 v[38:39], v231 offset:61448\n"
    "v_fma_mix_f32 v243, v177, v211, v219 op_sel:[1,0,0] op_sel_hi:[1,0,0]\n"
    "v_mfma_f32_16x16x32_bf16 a[16:19], v[4:7], v[160:163], a[16:19]\n"
    "ds_read_b64_tr_b16 v[40:41], v231 offset:57856\n"
    "v_cvt_pk_bf16_f32 v176, v240, v241\n"
    "v_mfma_f32_16x16x32_bf16 a[20:23], v[4:7], v[164:167], a[20:23]\n"
    "ds_read_b64_tr_b16 v[42:43], v231 offset:61952\n"
    "v_cvt_pk_bf16_f32 v177, v242, v243\n"
    "v_mfma_f32_16x16x32_bf16 a[24:27], v[4:7], v[168:171], a[24:27]\n"
    "ds_read_b64_tr_b16 v[44:45], v231 offset:57864\n"
    "v_fma_mix_f32 v244, v178, v212, v220 op_sel:[0,0,0] op_sel_hi:[1,0,0]\n"
    "v_mfma_f32_16x16x32_bf16 a[28:31], v[4:7], v[172:175], a[28:31]\n"
    "ds_read_b64_tr_b16 v[46:47], v231 offset:61960\n"
    "v_fma_mix_f32 v245, v178, v213, v221 op_sel:[1,0,0] op_sel_hi:[1,0,0]\n"
    "v_mfma_f32_16x16x32_bf16 a[32:35], v[8:11], v[160:163], a[32:35]\n"
    "ds_read_b64_tr_b16 v[48:49], v231 offset:58368\n"
    "v_fma_mix_f32 v246, v179, v214, v222 op_sel:[0,0,0] op_sel_hi:[1,0,0]\n"
    "v_mfma_f32_16x16x32_bf16 a[36:39], v[8:11], v[164:167], a[36:39]\n"
    "ds_read_b64_tr_b16 v[50:51], v231 offset:62464\n"
    "v_fma_mix_f32 v247, v179, v215, v223 op_sel:[1,0,0] op_sel_hi:[1,0,0]\n"
    "v_mfma_f32_16x16x32_bf16 a[40:43], v[8:11], v[168:171], a[40:43]\n"
    "ds_read_b64_tr_b16 v[52:53], v231 offset:58376\n"
    "v_cvt_pk_bf16_f32 v178, v244, v245\n"
    "v_mfma_f32_16x16x32_bf16 a[44:47], v[8:11], v[172:175], a[44:47]\n"
    "ds_read_b64_tr_b16 v[54:55], v231 offset:62472\n"
    "v_cvt_pk_bf16_f32 v179, v246, v247\n"
    "v_mfma_f32_16x16x32_bf16 a[48:51], v[12:15], v[160:163], a[48:51]\n"
    "ds_read_b64_tr_b16 v[56:57], v231 offset:58880\n"
    "v_fma_mix_f32 v240, v180, v208, v216 op_sel:[0,0,0] op_sel_hi:[1,0,0]\n"
    "v_mfma_f32_16x16x32_bf16 a[52:55], v[12:15], v[164:167], a[52:55]\n"
    "ds_read_b64_tr_b16 v[58:59], v231 offset:62976\n"
    "v_fma_mix_f32 v241, v180, v209, v217 op_sel:[1,0,0] op_sel_hi:[1,0,0]\n"
    "v_mfma_f32_16x16x32_bf16 a[56:59], v[12:15], v[168:171], a[56:59]\n"
    "ds_read_b64_tr_b16 v[60:61], v231 offset:58888\n"
    "v_fma_mix_f32 v242, v181, v210, v218 op_sel:[0,0,0] op_sel_hi:[1,0,0]\n"
    "v_mfma_f32_16x16x32_bf16 a[60:63], v[12:15], v[172:175], a[60:63]\n"
    "ds_read_b64_tr_b16 v[62:63], v231 offset:62984\n"
    "v_fma_mix_f32 v243, v181, v211, v219 op_sel:[1,0,0] op_sel_hi:[1,0,0]\n"
    "v_mfma_f32_16x16x32_bf16 a[64:67], v[16:19], v[160:163], a[64:67]\n"
    "v_cvt_pk_bf16_f32 v180, v240, v241\n"
    "v_cvt_pk_bf16_f32 v181, v242, v243\n"
    "v_mfma_f32_16x16x32_bf16 a[68:71], v[16:19], v[164:167], a[68:71]\n"
    "v_fma_mix_f32 v244, v182, v212, v220 op_sel:[0,0,0] op_sel_hi:[1,0,0]\n"
    "v_fma_mix_f32 v245, v182, v213, v221 op_sel:[1,0,0] op_sel_hi:[1,0,0]\n"
    "v_mfma_f32_16x16x32_bf16 a[72:75], v[16:19], v[168:171], a[72:75]\n"
    "v_fma_mix_f32 v246, v183, v214, v222 op_sel:[0,0,0] op_sel_hi:[1,0,0]\n"
    "v_fma_mix_f32 v247, v183, v215, v223 op_sel:[1,0,0] op_sel_hi:[1,0,0]\n"
    "v_mfma_f32_16x16x32_bf16 a[76:79], v[16:19], v[172:175], a[76:79]\n"
    "v_cvt_pk_bf16_f32 v182, v244, v245\n"
    "v_cvt_pk_bf16_f32 v183, v246, v247\n"
    "v_mfma_f32_16x16x32_bf16 a[80:83], v[20:23], v[160:163], a[80:83]\n"
    "v_fma_mix_f32 v240, v184, v208, v216 op_sel:[0,0,0] op_sel_hi:[1,0,0]\n"
    "v_fma_mix_f32 v241, v184, v209, v217 op_sel:[1,0,0] op_sel_hi:[1,0,0]\n"
    "v_mfma_f32_16x16x32_bf16 a[84:87], v[20:23], v[164:167], a[84:87]\n"
    "v_fma_mix_f32 v242, v185, v210, v218 op_sel:[0,0,0] op_sel_hi:[1,0,0]\n"
    "v_fma_mix_f32 v243, v185, v211, v219 op_sel:[1,0,0] op_sel_hi:[1,0,0]\n"
    "v_mfma_f32_16x16x32_bf16 a[88:91], v[20:23], v[168:171], a[88:91]\n"
    "v_cvt_pk_bf16_f32 v184, v240, v241\n"
    "v_cvt_pk_bf16_f32 v185, v242, v243\n"
    "v_mfma_f32_16x16x32_bf16 a[92:95], v[20:23], v[172:175], a[92:95]\n"
    "v_fma_mix_f32 v244, v186, v212, v220 op_sel:[0,0,0] op_sel_hi:[1,0,0]\n"
    "v_fma_mix_f32 v245, v186, v213, v221 op_sel:[1,0,0] op_sel_hi:[1,0,0]\n"
    "v_mfma_f32_16x16x32_bf16 a[96:99], v[24:27], v[160:163], a[96:99]\n"
    "v_fma_mix_f32 v246, v187, v214, v222 op_sel:[0,0,0] op_sel_hi:[1,0,0]\n"
    "v_fma_mix_f32 v247, v187, v215, v223 op_sel:[1,0,0] op_sel_hi:[1,0,0]\n"
    "v_mfma_f32_16x16x32_bf16 a[100:103], v[24:27], v[164:167], a[100:103]\n"
    "v_cvt_pk_bf16_f32 v186, v244, v245\n"
    "v_cvt_pk_bf16_f32 v187, v246, v247\n"
    "v_mfma_f32_16x16x32_bf16 a[104:107], v[24:27], v[168:171], a[104:107]\n"
    "v_fma_mix_f32 v240, v188, v208, v216 op_sel:[0,0,0] op_sel_hi:[1,0,0]\n"
    "v_fma_mix_f32 v241, v188, v209, v217 op_sel:[1,0,0] op_sel_hi:[1,0,0]\n"
    "v_mfma_f32_16x16x32_bf16 a[108:111], v[24:27], v[172:175], a[108:111]\n"
    "v_fma_mix_f32 v242, v189, v210, v218 op_sel:[0,0,0] op_sel_hi:[1,0,0]\n"
    "v_fma_mix_f32 v243, v189, v211, v219 op_sel:[1,0,0] op_sel_hi:[1,0,0]\n"
    "v_mfma_f32_16x16x32_bf16 a[112:115], v[28:31], v[160:163], a[112:115]\n"
    "v_cvt_pk_bf16_f32 v188, v240, v241\n"
    "v_cvt_pk_bf16_f32 v189, v242, v243\n"
    "v_mfma_f32_16x16x32_bf16 a[116:119], v[28:31], v[164:167], a[116:119]\n"
    "v_fma_mix_f32 v244, v190, v212, v220 op_sel:[0,0,0] op_sel_hi:[1,0,0]\n"
    "v_fma_mix_f32 v245, v190, v213, v221 op_sel:[1,0,0] op_sel_hi:[1,0,0]\n"
    "v_mfma_f32_16x16x32_bf16 a[120:123], v[28:31], v[168:171], a[120:123]\n"
    "v_fma_mix_f32 v246, v191, v214, v222 op_sel:[0,0,0] op_sel_hi:[1,0,0]\n"
    "v_fma_mix_f32 v247, v191, v215, v223 op_sel:[1,0,0] op_sel_hi:[1,0,0]\n"
    "v_mfma_f32_16x16x32_bf16 a[124:127], v[28:31], v[172:175], a[124:127]\n"
    "v_cvt_pk_bf16_f32 v190, v244, v245\n"
    "v_cvt_pk_bf16_f32 v191, v246, v247\n"
    "s_waitcnt lgkmcnt(0)\n"
    "s_waitcnt vmcnt(24)\n"
    "s_barrier\n"
    "s_add_u32 s68, s60, 32768\n"
    "ds_read_b128 a[128:131], v224 offset:0\n"
    "ds_read_b128 a[132:135], v225 offset:0\n"
    "s_add_u32 m0, s62, 32768\n"
    "s_nop 0\n"
    "buffer_load_dwordx4 v236, s[44:47], s68 offen lds\n"
    "ds_read_b128 a[136:139], v226 offset:0\n"
    "ds_read_b128 a[140:143], v227 offset:0\n"
    "s_add_u32 m0, s62, 33664\n"
    "s_nop 0\n"
    "buffer_load_dwordx4 v236, s[44:47], s68 offen offset:128 lds\n"
    "ds_read_b128 a[144:147], v224 offset:4096\n"
    "ds_read_b128 a[148:151], v225 offset:4096\n"
    "s_add_u32 m0, s62, 32768\n"
    "s_nop 0\n"
    "buffer_load_dwordx4 v236, s[44:47], s68 offen offset:2048 lds\n"
    "ds_read_b128 a[152:155], v226 offset:4096\n"
    "ds_read_b128 a[156:159], v227 offset:4096\n"
    "s_add_u32 m0, s62, 33664\n"
    "s_nop 0\n"
    "buffer_load_dwordx4 v236, s[44:47], s68 offen offset:2176 lds\n"
    "s_waitcnt lgkmcnt(0)\n"
    "s_add_u32 s68, s60, 49152\n"
    "s_add_u32 m0, s61, 49152\n"
    "s_nop 0\n"
    "buffer_load_dwordx4 v232, s[40:43], s68 offen lds\n"
    "buffer_load_dwordx4 v233, s[40:43], s68 offen offset:1024 lds\n"
    "buffer_load_dwordx4 v234, s[40:43], s68 offen offset:2048 lds\n"
    "buffer_load_dwordx4 v235, s[40:43], s68 offen offset:3072 lds\n"
    "v_mfma_f32_16x16x32_bf16 a[0:3], v[32:35], v[176:179], a[0:3]\n"
    "ds_read_b128 a[160:163], v224 offset:8192\n"
    "ds_read_b128 a[164:167], v225 offset:8192\n"
    "v_mfma_f32_16x16x32_bf16 a[16:19], v[36:39], v[176:179], a[16:19]\n"
    "ds_read_b128 a[168:171], v226 offset:8192\n"
    "ds_read_b128 a[172:175], v227 offset:8192\n"
    "v_mfma_f32_16x16x32_bf16 a[32:35], v[40:43], v[176:179], a[32:35]\n"
    "ds_read_b128 a[176:179], v224 offset:12288\n"
    "ds_read_b128 a[180:183], v225 offset:12288\n"
    "v_mfma_f32_16x16x32_bf16 a[48:51], v[44:47], v[176:179], a[48:51]\n"
    "ds_read_b128 a[184:187], v226 offset:12288\n"
    "ds_read_b128 a[188:191], v227 offset:12288\n"
    "v_mfma_f32_16x16x32_bf16 a[64:67], v[48:51], v[176:179], a[64:67]\n"
    "v_accvgpr_read_b32 v96, a0\n"
    "v_accvgpr_read_b32 v97, a1\n"
    "v_mfma_f32_16x16x32_bf16 a[80:83], v[52:55], v[176:179], a[80:83]\n"
    "v_accvgpr_read_b32 v98, a2\n"
    "v_accvgpr_read_b32 v99, a3\n"
    "v_mfma_f32_16x16x32_bf16 a[96:99], v[56:59], v[176:179], a[96:99]\n"
    "v_cvt_pk_bf16_f32 v0, v96, v97\n"
    "v_cvt_pk_bf16_f32 v1, v98, v99\n"
    "v_mfma_f32_16x16x32_bf16 a[112:115], v[60:63], v[176:179], a[112:115]\n"
    "v_accvgpr_read_b32 v100, a16\n"
    "v_accvgpr_read_b32 v101, a17\n"
    "v_mfma_f32_16x16x32_bf16 a[4:7], v[32:35], v[180:183], a[4:7]\n"
    "v_accvgpr_read_b32 v102, a18\n"
    "v_accvgpr_read_b32 v103, a19\n"
    "v_mfma_f32_16x16x32_bf16 a[20:23], v[36:39], v[180:183], a[20:23]\n"
    "v_cvt_pk_bf16_f32 v2, v100, v101\n"
    "v_cvt_pk_bf16_f32 v3, v102, v103\n"
    "v_mfma_f32_16x16x32_bf16 a[36:39], v[40:43], v[180:183], a[36:39]\n"
    "v_accvgpr_read_b32 v104, a32\n"
    "v_accvgpr_read_b32 v105, a33\n"
    "v_mfma_f32_16x16x32_bf16 a[52:55], v[44:47], v[180:183], a[52:55]\n"
    "v_accvgpr_read_b32 v106, a34\n"
    "v_accvgpr_read_b32 v107, a35\n"
    "v_mfma_f32_16x16x32_bf16 a[68:71], v[48:51], v[180:183], a[68:71]\n"
    "v_cvt_pk_bf16_f32 v4, v104, v105\n"
    "v_cvt_pk_bf16_f32 v5, v106, v107\n"
    "v_mfma_f32_16x16x32_bf16 a[84:87], v[52:55], v[180:183], a[84:87]\n"
    "v_accvgpr_read_b32 v108, a48\n"
    "v_accvgpr_read_b32 v109, a49\n"
    "v_mfma_f32_16x16x32_bf16 a[100:103], v[56:59], v[180:183], a[100:103]\n"
    "v_accvgpr_read_b32 v110, a50\n"
    "v_accvgpr_read_b32 v111, a51\n"
    "v_mfma_f32_16x16x32_bf16 a[116:119], v[60:63], v[180:183], a[116:119]\n"
    "v_cvt_pk_bf16_f32 v6, v108, v109\n"
    "v_cvt_pk_bf16_f32 v7, v110, v111\n"
    "v_mfma_f32_16x16x32_bf16 a[8:11], v[32:35], v[184:187], a[8:11]\n"
    "v_accvgpr_read_b32 v112, a64\n"
    "v_accvgpr_read_b32 v113, a65\n"
    "v_mfma_f32_16x16x32_bf16 a[24:27], v[36:39], v[184:187], a[24:27]\n"
    "v_accvgpr_read_b32 v114, a66\n"
    "v_accvgpr_read_b32 v115, a67\n"
    "v_mfma_f32_16x16x32_bf16 a[40:43], v[40:43], v[184:187], a[40:43]\n"
    "v_cvt_pk_bf16_f32 v8, v112, v113\n"
    "v_cvt_pk_bf16_f32 v9, v114, v115\n"
    "v_mfma_f32_16x16x32_bf16 a[56:59], v[44:47], v[184:187], a[56:59]\n"
    "v_accvgpr_read_b32 v116, a80\n"
    "v_accvgpr_read_b32 v117, a81\n"
    "v_mfma_f32_16x16x32_bf16 a[72:75], v[48:51], v[184:187], a[72:75]\n"
    "v_accvgpr_read_b32 v118, a82\n"
    "v_accvgpr_read_b32 v119, a83\n"
    "v_mfma_f32_16x16x32_bf16 a[88:91], v[52:55], v[184:187], a[88:91]\n"
    "v_cvt_pk_bf16_f32 v10, v116, v117\n"
    "v_cvt_pk_bf16_f32 v11, v118, v119\n"
    "v_mfma_f32_16x16x32_bf16 a[104:107], v[56:59], v[184:187], a[104:107]\n"
    "v_accvgpr_read_b32 v120, a96\n"
    "v_accvgpr_read_b32 v121, a97\n"
    "v_mfma_f32_16x16x32_bf16 a[120:123], v[60:63], v[184:187], a[120:123]\n"
    "v_accvgpr_read_b32 v122, a98\n"
    "v_accvgpr_read_b32 v123, a99\n"
    "v_mfma_f32_16x16x32_bf16 a[12:15], v[32:35], v[188:191], a[12:15]\n"
    "v_cvt_pk_bf16_f32 v12, v120, v121\n"
    "v_cvt_pk_bf16_f32 v13, v122, v123\n"
    "v_mfma_f32_16x16x32_bf16 a[28:31], v[36:39], v[188:191], a[28:31]\n"
    "v_accvgpr_read_b32 v124, a112\n"
    "v_accvgpr_read_b32 v125, a113\n"
    "v_mfma_f32_16x16x32_bf16 a[44:47], v[40:43], v[188:191], a[44:47]\n"
    "v_accvgpr_read_b32 v126, a114\n"
    "v_accvgpr_read_b32 v127, a115\n"
    "v_mfma_f32_16x16x32_bf16 a[60:63], v[44:47], v[188:191], a[60:63]\n"
    "v_cvt_pk_bf16_f32 v14, v124, v125\n"
    "v_cvt_pk_bf16_f32 v15, v126, v127\n"
    "v_mfma_f32_16x16x32_bf16 a[76:79], v[48:51], v[188:191], a[76:79]\n"
    "v_accvgpr_read_b32 v96, a4\n"
    "v_accvgpr_read_b32 v97, a5\n"
    "v_mfma_f32_16x16x32_bf16 a[92:95], v[52:55], v[188:191], a[92:95]\n"
    "v_accvgpr_read_b32 v98, a6\n"
    "v_accvgpr_read_b32 v99, a7\n"
    "v_mfma_f32_16x16x32_bf16 a[108:111], v[56:59], v[188:191], a[108:111]\n"
    "v_cvt_pk_bf16_f32 v16, v96, v97\n"
    "v_cvt_pk_bf16_f32 v17, v98, v99\n"
    "v_mfma_f32_16x16x32_bf16 a[124:127], v[60:63], v[188:191], a[124:127]\n"
    "v_accvgpr_read_b32 v100, a20\n"
    "v_accvgpr_read_b32 v101, a21\n"
    "v_mfma_f32_16x16x32_bf16 v[64:67], a[128:131], v[0:3], 0\n"
    "v_accvgpr_read_b32 v102, a22\n"
    "v_accvgpr_read_b32 v103, a23\n"
    "v_mfma_f32_16x16x32_bf16 v[64:67], a[132:135], v[4:7], v[64:67]\n"
    "v_cvt_pk_bf16_f32 v18, v100, v101\n"
    "v_cvt_pk_bf16_f32 v19, v102, v103\n"
    "v_mfma_f32_16x16x32_bf16 v[64:67], a[136:139], v[8:11], v[64:67]\n"
    "v_accvgpr_read_b32 v104, a36\n"
    "v_accvgpr_read_b32 v105, a37\n"
    "v_mfma_f32_16x16x32_bf16 v[64:67], a[140:143], v[12:15], v[64:67]\n"
    "v_accvgpr_read_b32 v106, a38\n"
    "v_accvgpr_read_b32 v107, a39\n"
    "v_mfma_f32_16x16x32_bf16 v[80:83], a[144:147], v[0:3], 0\n"
    "v_cvt_pk_bf16_f32 v20, v104, v105\n"
    "v_cvt_pk_bf16_f32 v21, v106, v107\n"
    "v_mfma_f32_16x16x32_bf16 v[80:83], a[148:151], v[4:7], v[80:83]\n"
    "v_accvgpr_read_b32 v108, a52\n"
    "v_accvgpr_read_b32 v109, a53\n"
    "v_mfma_f32_16x16x32_bf16 v[80:83], a[152:155], v[8:11], v[80:83]\n"
    "v_accvgpr_read_b32 v110, a54\n"
    "v_accvgpr_read_b32 v111, a55\n"
    "v_mfma_f32_16x16x32_bf16 v[80:83], a[156:159], v[12:15], v[80:83]\n"
    "v_cvt_pk_bf16_f32 v22, v108, v109\n"
    "v_cvt_pk_bf16_f32 v23, v110, v111\n"
    "v_mfma_f32_16x16x32_bf16 v[68:71], a[128:131], v[16:19], 0\n"
    "v_accvgpr_read_b32 v112, a68\n"
    "v_accvgpr_read_b32 v113, a69\n"
    "v_mfma_f32_16x16x32_bf16 v[68:71], a[132:135], v[20:23], v[68:71]\n"
    "v_accvgpr_read_b32 v114, a70\n"
    "v_accvgpr_read_b32 v115, a71\n"
    "v_cvt_pk_bf16_f32 v24, v112, v113\n"
    "v_cvt_pk_bf16_f32 v25, v114, v115\n"
    "v_accvgpr_read_b32 v116, a84\n"
    "v_accvgpr_read_b32 v117, a85\n"
    "v_accvgpr_read_b32 v118, a86\n"
    "v_accvgpr_read_b32 v119, a87\n"
    "v_cvt_pk_bf16_f32 v26, v116, v117\n"
    "v_cvt_pk_bf16_f32 v27, v118, v119\n"
    "v_accvgpr_read_b32 v120, a100\n"
    "v_accvgpr_read_b32 v121, a101\n"
    "v_mfma_f32_16x16x32_bf16 v[68:71], a[136:139], v[24:27], v[68:71]\n"
    "v_accvgpr_read_b32 v122, a102\n"
    "v_accvgpr_read_b32 v123, a103\n"
    "v_cvt_pk_bf16_f32 v28, v120, v121\n"
    "v_cvt_pk_bf16_f32 v29, v122, v123\n"
    "v_accvgpr_read_b32 v124, a116\n"
    "v_accvgpr_read_b32 v125, a117\n"
    "v_accvgpr_read_b32 v126, a118\n"
    "v_accvgpr_read_b32 v127, a119\n"
    "v_cvt_pk_bf16_f32 v30, v124, v125\n"
    "v_cvt_pk_bf16_f32 v31, v126, v127\n"
    "v_accvgpr_read_b32 v96, a8\n"
    "v_accvgpr_read_b32 v97, a9\n"
    "v_mfma_f32_16x16x32_bf16 v[68:71], a[140:143], v[28:31], v[68:71]\n"
    "v_accvgpr_read_b32 v98, a10\n"
    "v_accvgpr_read_b32 v99, a11\n"
    "v_mfma_f32_16x16x32_bf16 v[84:87], a[144:147], v[16:19], 0\n"
    "v_cvt_pk_bf16_f32 v32, v96, v97\n"
    "v_cvt_pk_bf16_f32 v33, v98, v99\n"
    "v_mfma_f32_16x16x32_bf16 v[84:87], a[148:151], v[20:23], v[84:87]\n"
    "v_accvgpr_read_b32 v100, a24\n"
    "v_accvgpr_read_b32 v101, a25\n"
    "v_mfma_f32_16x16x32_bf16 v[84:87], a[152:155], v[24:27], v[84:87]\n"
    "v_accvgpr_read_b32 v102, a26\n"
    "v_accvgpr_read_b32 v103, a27\n"
    "v_mfma_f32_16x16x32_bf16 v[84:87], a[156:159], v[28:31], v[84:87]\n"
    "v_cvt_pk_bf16_f32 v34, v100, v101\n"
    "v_cvt_pk_bf16_f32 v35, v102, v103\n"
    "v_accvgpr_read_b32 v104, a40\n"
    "v_accvgpr_read_b32 v105, a41\n"
    "v_mfma_f32_16x16x32_bf16 v[72:75], a[128:131], v[32:35], 0\n"
    "v_accvgpr_read_b32 v106, a42\n"
    "v_accvgpr_read_b32 v107, a43\n"
    "v_cvt_pk_bf16_f32 v36, v104, v105\n"
    "v_cvt_pk_bf16_f32 v37, v106, v107\n"
    "v_accvgpr_read_b32 v108, a56\n"
    "v_accvgpr_read_b32 v109, a57\n"
    "v_accvgpr_read_b32 v110, a58\n"
    "v_accvgpr_read_b32 v111, a59\n"
    "v_cvt_pk_bf16_f32 v38, v108, v109\n"
    "v_cvt_pk_bf16_f32 v39, v110, v111\n"
    "v_accvgpr_read_b32 v112, a72\n"
    "v_accvgpr_read_b32 v113, a73\n"
    "v_mfma_f32_16x16x32_bf16 v[72:75], a[132:135], v[36:39], v[72:75]\n"
    "v_accvgpr_read_b32 v114, a74\n"
    "v_accvgpr_read_b32 v115, a75\n"
    "v_cvt_pk_bf16_f32 v40, v112, v113\n"
    "v_cvt_pk_bf16_f32 v41, v114, v115\n"
    "v_accvgpr_read_b32 v116, a88\n"
    "v_accvgpr_read_b32 v117, a89\n"
    "v_accvgpr_read_b32 v118, a90\n"
    "v_accvgpr_read_b32 v119, a91\n"
    "v_cvt_pk_bf16_f32 v42, v116, v117\n"
    "v_cvt_pk_bf16_f32 v43, v118, v119\n"
    "v_accvgpr_read_b32 v120, a104\n"
    "v_accvgpr_read_b32 v121, a105\n"
    "v_mfma_f32_16x16x32_bf16 v[72:75], a[136:139], v[40:43], v[72:75]\n"
    "v_accvgpr_read_b32 v122, a106\n"
    "v_accvgpr_read_b32 v123, a107\n"
    "v_cvt_pk_bf16_f32 v44, v120, v121\n"
    "v_cvt_pk_bf16_f32 v45, v122, v123\n"
    "v_accvgpr_read_b32 v124, a120\n"
    "v_accvgpr_read_b32 v125, a121\n"
    "v_accvgpr_read_b32 v126, a122\n"
    "v_accvgpr_read_b32 v127, a123\n"
    "v_cvt_pk_bf16_f32 v46, v124, v125\n"
    "v_cvt_pk_bf16_f32 v47, v126, v127\n"
    "v_accvgpr_read_b32 v96, a12\n"
    "v_accvgpr_read_b32 v97, a13\n"
    "v_mfma_f32_16x16x32_bf16 v[72:75], a[140:143], v[44:47], v[72:75]\n"
    "v_accvgpr_read_b32 v98, a14\n"
    "v_accvgpr_read_b32 v99, a15\n"
    "v_mfma_f32_16x16x32_bf16 v[88:91], a[144:147], v[32:35], 0\n"
    "v_cvt_pk_bf16_f32 v48, v96, v97\n"
    "v_cvt_pk_bf16_f32 v49, v98, v99\n"
    "v_mfma_f32_16x16x32_bf16 v[88:91], a[148:151], v[36:39], v[88:91]\n"
    "v_accvgpr_read_b32 v100, a28\n"
    "v_accvgpr_read_b32 v101, a29\n"
    "v_mfma_f32_16x16x32_bf16 v[88:91], a[152:155], v[40:43], v[88:91]\n"
    "v_accvgpr_read_b32 v102, a30\n"
    "v_accvgpr_read_b32 v103, a31\n"
    "v_mfma_f32_16x16x32_bf16 v[88:91], a[156:159], v[44:47], v[88:91]\n"
    "v_cvt_pk_bf16_f32 v50, v100, v101\n"
    "v_cvt_pk_bf16_f32 v51, v102, v103\n"
    "v_accvgpr_read_b32 v104, a44\n"
    "v_accvgpr_read_b32 v105, a45\n"
    "v_mfma_f32_16x16x32_bf16 v[76:79], a[128:131], v[48:51], 0\n"
    "v_accvgpr_read_b32 v106, a46\n"
    "v_accvgpr_read_b32 v107, a47\n"
    "v_cvt_pk_bf16_f32 v52, v104, v105\n"
    "v_cvt_pk_bf16_f32 v53, v106, v107\n"
    "v_accvgpr_read_b32 v108, a60\n"
    "v_accvgpr_read_b32 v109, a61\n"
    "v_accvgpr_read_b32 v110, a62\n"
    "v_accvgpr_read_b32 v111, a63\n"
    "v_cvt_pk_bf16_f32 v54, v108, v109\n"
    "v_cvt_pk_bf16_f32 v55, v110, v111\n"
    "v_accvgpr_read_b32 v112, a76\n"
    "v_accvgpr_read_b32 v113, a77\n"
    "v_mfma_f32_16x16x32_bf16 v[76:79], a[132:135], v[52:55], v[76:79]\n"
    "v_accvgpr_read_b32 v114, a78\n"
    "v_accvgpr_read_b32 v115, a79\n"
    "v_cvt_pk_bf16_f32 v56, v112, v113\n"
    "v_cvt_pk_bf16_f32 v57, v114, v115\n"
    "v_accvgpr_read_b32 v116, a92\n"
    "v_accvgpr_read_b32 v117, a93\n"
    "v_accvgpr_read_b32 v118, a94\n"
    "v_accvgpr_read_b32 v119, a95\n"
    "v_cvt_pk_bf16_f32 v58, v116, v117\n"
    "v_cvt_pk_bf16_f32 v59, v118, v119\n"
    "v_accvgpr_read_b32 v120, a108\n"
    "v_accvgpr_read_b32 v121, a109\n"
    "v_mfma_f32_16x16x32_bf16 v[76:79], a[136:139], v[56:59], v[76:79]\n"
    "v_accvgpr_read_b32 v122, a110\n"
    "v_accvgpr_read_b32 v123, a111\n"
    "v_cvt_pk_bf16_f32 v60, v120, v121\n"
    "v_cvt_pk_bf16_f32 v61, v122, v123\n"
    "v_accvgpr_read_b32 v124, a124\n"
    "v_accvgpr_read_b32 v125, a125\n"
    "v_accvgpr_read_b32 v126, a126\n"
    "v_accvgpr_read_b32 v127, a127\n"
    "v_cvt_pk_bf16_f32 v62, v124, v125\n"
    "v_cvt_pk_bf16_f32 v63, v126, v127\n"
    "v_max3_f32 v160, v64, v65, v66\n"
    "v_max3_f32 v160, v160, v67, v80\n"
    "v_mfma_f32_16x16x32_bf16 v[76:79], a[140:143], v[60:63], v[76:79]\n"
    "v_max3_f32 v160, v160, v81, v82\n"
    "v_max_f32 v160, v160, v83\n"
    "v_mfma_f32_16x16x32_bf16 v[92:95], a[144:147], v[48:51], 0\n"
    "v_mov_b32 v212, v160\n"
    "v_max3_f32 v164, v68, v69, v70\n"
    "v_mfma_f32_16x16x32_bf16 v[92:95], a[148:151], v[52:55], v[92:95]\n"
    "v_permlane16_swap_b32 v160, v212\n"
    "v_max_f32 v160, v160, v212\n"
    "v_mfma_f32_16x16x32_bf16 v[92:95], a[152:155], v[56:59], v[92:95]\n"
    "v_mov_b32 v212, v160\n"
    "v_max3_f32 v164, v164, v71, v84\n"
    "v_mfma_f32_16x16x32_bf16 v[92:95], a[156:159], v[60:63], v[92:95]\n"
    "v_permlane32_swap_b32 v160, v212\n"
    "v_max_f32 v160, v160, v212\n"
    "v_sub_f32 v64, v64, v160\n"
    "v_sub_f32 v65, v65, v160\n"
    "v_sub_f32 v66, v66, v160\n"
    "v_sub_f32 v67, v67, v160\n"
    "v_sub_f32 v80, v80, v160\n"
    "v_sub_f32 v81, v81, v160\n"
    "v_sub_f32 v82, v82, v160\n"
    "v_sub_f32 v83, v83, v160\n"
    "v_sub_f32 v160, 0, v160\n"
    "v_mov_b32 v161, v160\n"
    "v_mov_b32 v162, v160\n"
    "v_mov_b32 v163, v160\n"
    "v_max3_f32 v164, v164, v85, v86\n"
    "v_max_f32 v164, v164, v87\n"
    "v_mov_b32 v213, v164\n"
    "v_max3_f32 v168, v72, v73, v74\n"
    "v_max3_f32 v168, v168, v75, v88\n"
    "v_permlane16_swap_b32 v164, v213\n"
    "v_max_f32 v164, v164, v213\n"
    "v_mov_b32 v213, v164\n"
    "v_max3_f32 v168, v168, v89, v90\n"
    "v_max_f32 v168, v168, v91\n"
    "v_permlane32_swap_b32 v164, v213\n"
    "v_max_f32 v164, v164, v213\n"
    "v_sub_f32 v68, v68, v164\n"
    "v_sub_f32 v69, v69, v164\n"
    "v_sub_f32 v70, v70, v164\n"
    "v_sub_f32 v71, v71, v164\n"
    "v_sub_f32 v84, v84, v164\n"
    "v_sub_f32 v85, v85, v164\n"
    "v_sub_f32 v86, v86, v164\n"
    "v_sub_f32 v87, v87, v164\n"
    "v_sub_f32 v164, 0, v164\n"
    "v_mov_b32 v165, v164\n"
    "v_mov_b32 v166, v164\n"
    "v_mov_b32 v167, v164\n"
    "v_mov_b32 v214, v168\n"
    "v_max3_f32 v172, v76, v77, v78\n"
    "v_max3_f32 v172, v172, v79, v92\n"
    "v_permlane16_swap_b32 v168, v214\n"
    "v_max_f32 v168, v168, v214\n"
    "v_mov_b32 v214, v168\n"
    "v_max3_f32 v172, v172, v93, v94\n"
    "v_max_f32 v172, v172, v95\n"
    "v_permlane32_swap_b32 v168, v214\n"
    "v_max_f32 v168, v168, v214\n"
    "v_sub_f32 v72, v72, v168\n"
    "v_sub_f32 v73, v73, v168\n"
    "v_sub_f32 v74, v74, v168\n"
    "v_sub_f32 v75, v75, v168\n"
    "v_sub_f32 v88, v88, v168\n"
    "v_sub_f32 v89, v89, v168\n"
    "v_sub_f32 v90, v90, v168\n"
    "v_sub_f32 v91, v91, v168\n"
    "v_sub_f32 v168, 0, v168\n"
    "v_mov_b32 v169, v168\n"
    "v_mov_b32 v170, v168\n"
    "v_mov_b32 v171, v168\n"
    "v_mov_b32 v215, v172\n"
    "v_lshrrev_b32 v204, 4, v238\n"
    "v_and_b32 v205, 15, v204\n"
    "v_permlane16_swap_b32 v172, v215\n"
    "v_max_f32 v172, v172, v215\n"
    "v_mov_b32 v215, v172\n"
    "v_cmp_eq_u32 vcc, 0, v205\n"
    "v_mov_b32 v207, 0x3f803f80\n"
    "v_permlane32_swap_b32 v172, v215\n"
    "v_max_f32 v172, v172, v215\n"
    "v_sub_f32 v76, v76, v172\n"
    "v_sub_f32 v77, v77, v172\n"
    "v_sub_f32 v78, v78, v172\n"
    "v_sub_f32 v79, v79, v172\n"
    "v_sub_f32 v92, v92, v172\n"
    "v_sub_f32 v93, v93, v172\n"
    "v_sub_f32 v94, v94, v172\n"
    "v_sub_f32 v95, v95, v172\n"
    "v_sub_f32 v172, 0, v172\n"
    "v_mov_b32 v173, v172\n"
    "v_mov_b32 v174, v172\n"
    "v_mov_b32 v175, v172\n"
    "s_nop 1\n"
    "v_cndmask_b32 v200, 0, v207, vcc\n"
    "v_cndmask_b32 v201, 0, v207, vcc\n"
    "v_cndmask_b32 v202, 0, v207, vcc\n"
    "v_cndmask_b32 v203, 0, v207, vcc\n"
    "v_mov_b32 v192, 0\n"
    "v_mov_b32 v193, 0\n"
    "v_mov_b32 v194, 0\n"
    "v_mov_b32 v195, 0\n"
    "v_mov_b32 v196, 0\n"
    "v_mov_b32 v197, 0\n"
    "v_mov_b32 v198, 0\n"
    "v_mov_b32 v199, 0\n"
    "v_lshlrev_b32 v246, 6, v204\n"
    "s_mov_b64 s[56:57], %[of]\n"
    "s_and_b32 s57, s57, 0xffff\n"
    "s_mov_b32 s58, 397312\n"
    "s_waitcnt lgkmcnt(0)\n"
    "s_barrier\n"
    "s_add_u32 s60, s60, 49152\n"
    "s_add_u32 s68, s60, 16384\n"
    "s_min_u32 s68, s68, s69\n"
    "v_mfma_f32_16x16x32_bf16 v[96:99], a[160:163], v[0:3], v[160:163]\n"
    "s_add_u32 m0, s61, 0\n"
    "v_exp_f32 v64, v64\n"
    "v_mfma_f32_16x16x32_bf16 v[100:103], a[160:163], v[16:19], v[164:167]\n"
    "v_exp_f32 v65, v65\n"
    "v_mfma_f32_16x16x32_bf16 v[104:107], a[160:163], v[32:35], v[168:171]\n"
    "v_exp_f32 v66, v66\n"
    "v_mfma_f32_16x16x32_bf16 v[108:111], a[160:163], v[48:51], v[172:175]\n"
    "v_exp_f32 v67, v67\n"
    "v_mfma_f32_16x16x32_bf16 v[96:99], a[164:167], v[4:7], v[96:99]\n"
    "v_exp_f32 v68, v68\n"
    "v_mfma_f32_16x16x32_bf16 v[100:103], a[164:167], v[20:23], v[100:103]\n"
    "v_exp_f32 v69, v69\n"
    "v_mfma_f32_16x16x32_bf16 v[104:107], a[164:167], v[36:39], v[104:107]\n"
    "v_exp_f32 v70, v70\n"
    "v_mfma_f32_16x16x32_bf16 v[108:111], a[164:167], v[52:55], v[108:111]\n"
    "v_exp_f32 v71, v71\n"
    "v_exp_f32 v72, v72\n"
    "v_mfma_f32_16x16x32_bf16 v[96:99], a[168:171], v[8:11], v[96:99]\n"
    "v_exp_f32 v73, v73\n"
    "v_mfma_f32_16x16x32_bf16 v[100:103], a[168:171], v[24:27], v[100:103]\n"
    "v_exp_f32 v74, v74\n"
    "v_mfma_f32_16x16x32_bf16 v[104:107], a[168:171], v[40:43], v[104:107]\n"
    "v_exp_f32 v75, v75\n"
    "v_mfma_f32_16x16x32_bf16 v[108:111], a[168:171], v[56:59], v[108:111]\n"
    "v_exp_f32 v76, v76\n"
    "v_mfma_f32_16x16x32_bf16 v[96:99], a[172:175], v[12:15], v[96:99]\n"
    "v_exp_f32 v77, v77\n"
    "v_mfma_f32_16x16x32_bf16 v[100:103], a[172:175], v[28:31], v[100:103]\n"
    "v_exp_f32 v78, v78\n"
    "v_mfma_f32_16x16x32_bf16 v[104:107], a[172:175], v[44:47], v[104:107]\n"
    "v_exp_f32 v79, v79\n"
    "v_exp_f32 v80, v80\n"
    "v_mfma_f32_16x16x32_bf16 v[108:111], a[172:175], v[60:63], v[108:111]\n"
    "v_exp_f32 v81, v81\n"
    "v_mfma_f32_16x16x32_bf16 v[112:115], a[176:179], v[0:3], v[160:163]\n"
    "v_exp_f32 v82, v82\n"
    "v_mfma_f32_16x16x32_bf16 v[116:119], a[176:179], v[16:19], v[164:167]\n"
    "v_exp_f32 v83, v83\n"
    "v_mfma_f32_16x16x32_bf16 v[120:123], a[176:179], v[32:35], v[168:171]\n"
    "v_exp_f32 v84, v84\n"
    "v_mfma_f32_16x16x32_bf16 v[124:127], a[176:179], v[48:51], v[172:175]\n"
    "v_exp_f32 v85, v85\n"
    "v_mfma_f32_16x16x32_bf16 v[112:115], a[180:183], v[4:7], v[112:115]\n"
    "v_exp_f32 v86, v86\n"
    "v_mfma_f32_16x16x32_bf16 v[116:119], a[180:183], v[20:23], v[116:119]\n"
    "v_exp_f32 v87, v87\n"
    "v_exp_f32 v88, v88\n"
    "v_mfma_f32_16x16x32_bf16 v[120:123], a[180:183], v[36:39], v[120:123]\n"
    "v_exp_f32 v89, v89\n"
    "v_mfma_f32_16x16x32_bf16 v[124:127], a[180:183], v[52:55], v[124:127]\n"
    "v_exp_f32 v90, v90\n"
    "v_mfma_f32_16x16x32_bf16 v[112:115], a[184:187], v[8:11], v[112:115]\n"
    "v_exp_f32 v91, v91\n"
    "v_mfma_f32_16x16x32_bf16 v[116:119], a[184:187], v[24:27], v[116:119]\n"
    "v_exp_f32 v92, v92\n"
    "v_mfma_f32_16x16x32_bf16 v[120:123], a[184:187], v[40:43], v[120:123]\n"
    "v_exp_f32 v93, v93\n"
    "v_mfma_f32_16x16x32_bf16 v[124:127], a[184:187], v[56:59], v[124:127]\n"
    "v_exp_f32 v94, v94\n"
    "v_mfma_f32_16x16x32_bf16 v[112:115], a[188:191], v[12:15], v[112:115]\n"
    "v_exp_f32 v95, v95\n"
    "v_mfma_f32_16x16x32_bf16 v[116:119], a[188:191], v[28:31], v[116:119]\n"
    "buffer_load_dwordx4 v232, s[40:43], s68 offen lds\n"
    "v_cvt_pk_bf16_f32 v128, v64, v65\n"
    "v_mfma_f32_16x16x32_bf16 v[120:123], a[188:191], v[44:47], v[120:123]\n"
    "ds_read_b64_tr_b16 a[192:193], v228 offset:0\n"
    "v_cvt_pk_bf16_f32 v129, v66, v67\n"
    "v_mfma_f32_16x16x32_bf16 v[124:127], a[188:191], v[60:63], v[124:127]\n"
    "ds_read_b64_tr_b16 a[194:195], v228 offset:4096\n"
    "v_cvt_pk_bf16_f32 v132, v68, v69\n"
    "s_nop 0\n"
    "ds_read_b128 a[128:131], v224 offset:16384\n"
    "ds_read_b64_tr_b16 a[196:197], v229 offset:0\n"
    "ds_read_b64_tr_b16 a[198:199], v229 offset:4096\n"
    "ds_read_b128 a[132:135], v225 offset:16384\n"
    "buffer_load_dwordx4 v233, s[40:43], s68 offen offset:1024 lds\n"
    "ds_read_b64_tr_b16 a[200:201], v228 offset:512\n"
    "ds_read_b64_tr_b16 a[202:203], v228 offset:4608\n"
    "ds_read_b128 a[136:139], v226 offset:16384\n"
    "ds_read_b64_tr_b16 a[204:205], v229 offset:512\n"
    "ds_read_b64_tr_b16 a[206:207], v229 offset:4608\n"
    "ds_read_b128 a[140:143], v227 offset:16384\n"
    "buffer_load_dwordx4 v234, s[40:43], s68 offen offset:2048 lds\n"
    "ds_read_b64_tr_b16 a[208:209], v228 offset:1024\n"
    "ds_read_b64_tr_b16 a[210:211], v228 offset:5120\n"
    "ds_read_b128 a[144:147], v224 offset:20480\n"
    "ds_read_b64_tr_b16 a[212:213], v229 offset:1024\n"
    "ds_read_b64_tr_b16 a[214:215], v229 offset:5120\n"
    "ds_read_b128 a[148:151], v225 offset:20480\n"
    "buffer_load_dwordx4 v235, s[40:43], s68 offen offset:3072 lds\n"
    "ds_read_b64_tr_b16 a[216:217], v228 offset:1536\n"
    "ds_read_b64_tr_b16 a[218:219], v228 offset:5632\n"
    "ds_read_b128 a[152:155], v226 offset:20480\n"
    "ds_read_b64_tr_b16 a[220:221], v229 offset:1536\n"
    "ds_read_b64_tr_b16 a[222:223], v229 offset:5632\n"
    "ds_read_b128 a[156:159], v227 offset:20480\n"
    "v_cvt_pk_bf16_f32 v133, v70, v71\n"
    "v_add_f32 v196, v196, v72\n"
    "v_cvt_pk_bf16_f32 v136, v72, v73\n"
    "v_add_f32 v197, v197, v73\n"
    "v_add_f32 v196, v196, v74\n"
    "v_cvt_pk_bf16_f32 v137, v74, v75\n"
    "v_add_f32 v197, v197, v75\n"
    "v_add_f32 v198, v198, v76\n"
    "v_cvt_pk_bf16_f32 v140, v76, v77\n"
    "v_add_f32 v199, v199, v77\n"
    "v_add_f32 v198, v198, v78\n"
    "v_cvt_pk_bf16_f32 v141, v78, v79\n"
    "v_add_f32 v199, v199, v79\n"
    "v_cvt_pk_bf16_f32 v130, v80, v81\n"
    "v_cvt_pk_bf16_f32 v131, v82, v83\n"
    "v_cvt_pk_bf16_f32 v134, v84, v85\n"
    "v_cvt_pk_bf16_f32 v135, v86, v87\n"
    "v_add_f32 v196, v196, v88\n"
    "v_cvt_pk_bf16_f32 v138, v88, v89\n"
    "v_add_f32 v197, v197, v89\n"
    "v_add_f32 v196, v196, v90\n"
    "v_cvt_pk_bf16_f32 v139, v90, v91\n"
    "v_add_f32 v197, v197, v91\n"
    "v_add_f32 v198, v198, v92\n"
    "v_cvt_pk_bf16_f32 v142, v92, v93\n"
    "v_add_f32 v199, v199, v93\n"
    "v_add_f32 v198, v198, v94\n"
    "v_cvt_pk_bf16_f32 v143, v94, v95\n"
    "v_add_f32 v199, v199, v95\n"
    "s_waitcnt lgkmcnt(0)\n"
    "v_mfma_f32_16x16x32_bf16 v[64:67], a[128:131], v[0:3], v[160:163]\n"
    "s_add_u32 m0, s62, 49152\n"
    "v_exp_f32 v96, v96\n"
    "v_mfma_f32_16x16x32_bf16 v[68:71], a[128:131], v[16:19], v[164:167]\n"
    "v_exp_f32 v97, v97\n"
    "v_mfma_f32_16x16x32_bf16 v[72:75], a[128:131], v[32:35], v[168:171]\n"
    "buffer_load_dwordx4 v236, s[44:47], s60 offen lds\n"
    "v_cvt_pk_bf16_f32 v144, v96, v97\n"
    "v_mfma_f32_16x16x32_bf16 v[76:79], a[128:131], v[48:51], v[172:175]\n"
    "v_exp_f32 v98, v98\n"
    "v_mfma_f32_16x16x32_bf16 a[0:3], a[192:195], v[128:131], 0\n"
    "ds_read_b64_tr_b16 a[224:225], v228 offset:8192\n"
    "v_mfma_f32_16x16x32_bf16 a[4:7], a[192:195], v[132:135], 0\n"
    "v_exp_f32 v99, v99\n"
    "v_mfma_f32_16x16x32_bf16 a[8:11], a[192:195], v[136:139], 0\n"
    "ds_read_b64_tr_b16 a[226:227], v228 offset:12288\n"
    "v_cvt_pk_bf16_f32 v145, v98, v99\n"
    "v_mfma_f32_16x16x32_bf16 a[12:15], a[192:195], v[140:143], 0\n"
    "v_exp_f32 v100, v100\n"
    "v_mfma_f32_16x16x32_bf16 v[64:67], a[132:135], v[4:7], v[64:67]\n"
    "ds_read_b128 a[160:163], v224 offset:24576\n"
    "v_mfma_f32_16x16x32_bf16 v[68:71], a[132:135], v[20:23], v[68:71]\n"
    "v_exp_f32 v101, v101\n"
    "v_mfma_f32_16x16x32_bf16 v[72:75], a[132:135], v[36:39], v[72:75]\n"
    "ds_read_b64_tr_b16 a[228:229], v229 offset:8192\n"
    "v_cvt_pk_bf16_f32 v148, v100, v101\n"
    "v_mfma_f32_16x16x32_bf16 v[76:79], a[132:135], v[52:55], v[76:79]\n"
    "v_exp_f32 v102, v102\n"
    "v_mfma_f32_16x16x32_bf16 a[16:19], a[196:199], v[128:131], 0\n"
    "ds_read_b64_tr_b16 a[230:231], v229 offset:12288\n"
    "v_mfma_f32_16x16x32_bf16 a[20:23], a[196:199], v[132:135], 0\n"
    "v_exp_f32 v103, v103\n"
    "v_mfma_f32_16x16x32_bf16 a[24:27], a[196:199], v[136:139], 0\n"
    "ds_read_b128 a[164:167], v225 offset:24576\n"
    "v_cvt_pk_bf16_f32 v149, v102, v103\n"
    "v_mfma_f32_16x16x32_bf16 a[28:31], a[196:199], v[140:143], 0\n"
    "s_add_u32 m0, s62, 50048\n"
    "v_exp_f32 v104, v104\n"
    "v_mfma_f32_16x16x32_bf16 v[176:179], v[200:203], v[128:131], 0\n"
    "buffer_load_dwordx4 v236, s[44:47], s60 offen offset:128 lds\n"
    "v_add_f32 v196, v196, v104\n"
    "v_mfma_f32_16x16x32_bf16 v[64:67], a[136:139], v[8:11], v[64:67]\n"
    "v_exp_f32 v105, v105\n"
    "v_mfma_f32_16x16x32_bf16 v[68:71], a[136:139], v[24:27], v[68:71]\n"
    "ds_read_b64_tr_b16 a[232:233], v228 offset:8704\n"
    "v_cvt_pk_bf16_f32 v152, v104, v105\n"
    "v_mfma_f32_16x16x32_bf16 v[72:75], a[136:139], v[40:43], v[72:75]\n"
    "v_exp_f32 v106, v106\n"
    "v_mfma_f32_16x16x32_bf16 v[76:79], a[136:139], v[56:59], v[76:79]\n"
    "ds_read_b64_tr_b16 a[234:235], v228 offset:12800\n"
    "v_add_f32 v197, v197, v105\n"
    "v_mfma_f32_16x16x32_bf16 a[32:35], a[200:203], v[128:131], 0\n"
    "v_exp_f32 v107, v107\n"
    "v_mfma_f32_16x16x32_bf16 a[36:39], a[200:203], v[132:135], 0\n"
    "ds_read_b128 a[168:171], v226 offset:24576\n"
    "v_add_f32 v196, v196, v106\n"
    "v_mfma_f32_16x16x32_bf16 a[40:43], a[200:203], v[136:139], 0\n"
    "v_exp_f32 v108, v108\n"
    "v_mfma_f32_16x16x32_bf16 a[44:47], a[200:203], v[140:143], 0\n"
    "ds_read_b64_tr_b16 a[236:237], v229 offset:8704\n"
    "v_cvt_pk_bf16_f32 v153, v106, v107\n"
    "v_mfma_f32_16x16x32_bf16 v[64:67], a[140:143], v[12:15], v[64:67]\n"
    "v_exp_f32 v109, v109\n"
    "v_mfma_f32_16x16x32_bf16 v[68:71], a[140:143], v[28:31], v[68:71]\n"
    "ds_read_b64_tr_b16 a[238:239], v229 offset:12800\n"
    "v_add_f32 v197, v197, v107\n"
    "v_mfma_f32_16x16x32_bf16 v[72:75], a[140:143], v[44:47], v[72:75]\n"
    "v_exp_f32 v110, v110\n"
    "v_mfma_f32_16x16x32_bf16 v[76:79], a[140:143], v[60:63], v[76:79]\n"
    "ds_read_b128 a[172:175], v227 offset:24576\n"
    "v_add_f32 v198, v198, v108\n"
    "v_mfma_f32_16x16x32_bf16 a[48:51], a[204:207], v[128:131], 0\n"
    "s_add_u32 m0, s62, 49152\n"
    "v_exp_f32 v111, v111\n"
    "v_mfma_f32_16x16x32_bf16 a[52:55], a[204:207], v[132:135], 0\n"
    "buffer_load_dwordx4 v236, s[44:47], s60 offen offset:2048 lds\n"
    "v_cvt_pk_bf16_f32 v156, v108, v109\n"
    "v_mfma_f32_16x16x32_bf16 a[56:59], a[204:207], v[136:139], 0\n"
    "v_exp_f32 v112, v112\n"
    "v_mfma_f32_16x16x32_bf16 a[60:63], a[204:207], v[140:143], 0\n"
    "v_exp_f32 v113, v113\n"
    "v_mfma_f32_16x16x32_bf16 v[180:183], v[200:203], v[132:135], 0\n"
    "ds_read_b64_tr_b16 a[240:241], v228 offset:9216\n"
    "v_add_f32 v199, v199, v109\n"
    "v_mfma_f32_16x16x32_bf16 v[80:83], a[144:147], v[0:3], v[160:163]\n"
    "v_exp_f32 v114, v114\n"
    "v_mfma_f32_16x16x32_bf16 v[84:87], a[144:147], v[16:19], v[164:167]\n"
    "ds_read_b64_tr_b16 a[242:243], v228 offset:13312\n"
    "v_add_f32 v198, v198, v110\n"
    "v_mfma_f32_16x16x32_bf16 v[88:91], a[144:147], v[32:35], v[168:171]\n"
    "v_exp_f32 v115, v115\n"
    "v_mfma_f32_16x16x32_bf16 v[92:95], a[144:147], v[48:51], v[172:175]\n"
    "ds_read_b128 a[176:179], v224 offset:28672\n"
    "v_cvt_pk_bf16_f32 v157, v110, v111\n"
    "v_mfma_f32_16x16x32_bf16 a[64:67], a[208:211], v[128:131], 0\n"
    "v_exp_f32 v116, v116\n"
    "v_mfma_f32_16x16x32_bf16 a[68:71], a[208:211], v[132:135], 0\n"
    "ds_read_b64_tr_b16 a[244:245], v229 offset:9216\n"
    "v_add_f32 v199, v199, v111\n"
    "v_mfma_f32_16x16x32_bf16 a[72:75], a[208:211], v[136:139], 0\n"
    "v_exp_f32 v117, v117\n"
    "v_mfma_f32_16x16x32_bf16 a[76:79], a[208:211], v[140:143], 0\n"
    "ds_read_b64_tr_b16 a[246:247], v229 offset:13312\n"
    "v_cvt_pk_bf16_f32 v146, v112, v113\n"
    "v_mfma_f32_16x16x32_bf16 v[80:83], a[148:151], v[4:7], v[80:83]\n"
    "v_exp_f32 v118, v118\n"
    "v_mfma_f32_16x16x32_bf16 v[84:87], a[148:151], v[20:23], v[84:87]\n"
    "ds_read_b128 a[180:183], v225 offset:28672\n"
    "v_cvt_pk_bf16_f32 v147, v114, v115\n"
    "v_mfma_f32_16x16x32_bf16 v[88:91], a[148:151], v[36:39], v[88:91]\n"
    "s_add_u32 m0, s62, 50048\n"
    "v_exp_f32 v119, v119\n"
    "v_mfma_f32_16x16x32_bf16 v[92:95], a[148:151], v[52:55], v[92:95]\n"
    "buffer_load_dwordx4 v236, s[44:47], s60 offen offset:2176 lds\n"
    "v_cvt_pk_bf16_f32 v150, v116, v117\n"
    "v_mfma_f32_16x16x32_bf16 a[80:83], a[212:215], v[128:131], 0\n"
    "v_exp_f32 v120, v120\n"
    "v_mfma_f32_16x16x32_bf16 a[84:87], a[212:215], v[132:135], 0\n"
    "ds_read_b64_tr_b16 a[248:249], v228 offset:9728\n"
    "v_cvt_pk_bf16_f32 v151, v118, v119\n"
    "v_mfma_f32_16x16x32_bf16 a[88:91], a[212:215], v[136:139], 0\n"
    "v_exp_f32 v121, v121\n"
    "v_mfma_f32_16x16x32_bf16 a[92:95], a[212:215], v[140:143], 0\n"
    "ds_read_b64_tr_b16 a[250:251], v228 offset:13824\n"
    "v_add_f32 v196, v196, v120\n"
    "v_mfma_f32_16x16x32_bf16 v[80:83], a[152:155], v[8:11], v[80:83]\n"
    "v_exp_f32 v122, v122\n"
    "v_mfma_f32_16x16x32_bf16 v[84:87], a[152:155], v[24:27], v[84:87]\n"
    "ds_read_b128 a[184:187], v226 offset:28672\n"
    "v_cvt_pk_bf16_f32 v154, v120, v121\n"
    "v_mfma_f32_16x16x32_bf16 v[88:91], a[152:155], v[40:43], v[88:91]\n"
    "v_exp_f32 v123, v123\n"
    "v_mfma_f32_16x16x32_bf16 v[92:95], a[152:155], v[56:59], v[92:95]\n"
    "ds_read_b64_tr_b16 a[252:253], v229 offset:9728\n"
    "v_add_f32 v197, v197, v121\n"
    "v_mfma_f32_16x16x32_bf16 a[96:99], a[216:219], v[128:131], 0\n"
    "v_exp_f32 v124, v124\n"
    "v_mfma_f32_16x16x32_bf16 a[100:103], a[216:219], v[132:135], 0\n"
    "ds_read_b64_tr_b16 a[254:255], v229 offset:13824\n"
    "v_add_f32 v196, v196, v122\n"
    "v_mfma_f32_16x16x32_bf16 a[104:107], a[216:219], v[136:139], 0\n"
    "v_exp_f32 v125, v125\n"
    "v_mfma_f32_16x16x32_bf16 a[108:111], a[216:219], v[140:143], 0\n"
    "ds_read_b128 a[188:191], v227 offset:28672\n"
    "v_cvt_pk_bf16_f32 v155, v122, v123\n"
    "v_mfma_f32_16x16x32_bf16 v[80:83], a[156:159], v[12:15], v[80:83]\n"
    "v_exp_f32 v126, v126\n"
    "v_mfma_f32_16x16x32_bf16 v[84:87], a[156:159], v[28:31], v[84:87]\n"
    "v_add_f32 v197, v197, v123\n"
    "v_add_f32 v198, v198, v124\n"
    "v_mfma_f32_16x16x32_bf16 v[88:91], a[156:159], v[44:47], v[88:91]\n"
    "v_exp_f32 v127, v127\n"
    "v_mfma_f32_16x16x32_bf16 v[92:95], a[156:159], v[60:63], v[92:95]\n"
    "v_cvt_pk_bf16_f32 v158, v124, v125\n"
    "v_add_f32 v199, v199, v125\n"
    "v_mfma_f32_16x16x32_bf16 a[112:115], a[220:223], v[128:131], 0\n"
    "v_add_f32 v198, v198, v126\n"
    "v_cvt_pk_bf16_f32 v159, v126, v127\n"
    "v_mfma_f32_16x16x32_bf16 a[116:119], a[220:223], v[132:135], 0\n"
    "v_add_f32 v199, v199, v127\n"
    "v_mfma_f32_16x16x32_bf16 a[120:123], a[220:223], v[136:139], 0\n"
    "v_mfma_f32_16x16x32_bf16 a[124:127], a[220:223], v[140:143], 0\n"
    "s_add_u32 s60, s60, 16384\n"
    "s_waitcnt vmcnt(16) lgkmcnt(0)\n"
    "s_barrier\n"
    "s_mov_b32 s63, 7\n"
    "L_loop_%=:\n"
    "s_add_u32 s68, s60, 16384\n"
    "s_min_u32 s68, s68, s69\n"
    "v_mfma_f32_16x16x32_bf16 v[96:99], a[160:163], v[0:3], v[160:163]\n"
    "s_add_u32 m0, s61, 16384\n"
    "v_exp_f32 v64, v64\n"
    "v_mfma_f32_16x16x32_bf16 v[100:103], a[160:163], v[16:19], v[164:167]\n"
    "v_exp_f32 v65, v65\n"
    "v_mfma_f32_16x16x32_bf16 v[104:107], a[160:163], v[32:35], v[168:171]\n"
    "buffer_load_dwordx4 v232, s[40:43], s68 offen lds\n"
    "v_cvt_pk_bf16_f32 v128, v64, v65\n"
    "v_mfma_f32_16x16x32_bf16 v[108:111], a[160:163], v[48:51], v[172:175]\n"
    "v_exp_f32 v66, v66\n"
    "v_mfma_f32_16x16x32_bf16 a[0:3], a[224:227], v[144:147], a[0:3]\n"
    "ds_read_b64_tr_b16 a[192:193], v228 offset:16384\n"
    "v_mfma_f32_16x16x32_bf16 a[4:7], a[224:227], v[148:151], a[4:7]\n"
    "v_exp_f32 v67, v67\n"
    "v_mfma_f32_16x16x32_bf16 a[8:11], a[224:227], v[152:155], a[8:11]\n"
    "ds_read_b64_tr_b16 a[194:195], v228 offset:20480\n"
    "v_cvt_pk_bf16_f32 v129, v66, v67\n"
    "v_mfma_f32_16x16x32_bf16 a[12:15], a[224:227], v[156:159], a[12:15]\n"
    "v_exp_f32 v68, v68\n"
    "v_mfma_f32_16x16x32_bf16 v[96:99], a[164:167], v[4:7], v[96:99]\n"
    "ds_read_b128 a[128:131], v224 offset:32768\n"
    "v_mfma_f32_16x16x32_bf16 v[100:103], a[164:167], v[20:23], v[100:103]\n"
    "v_exp_f32 v69, v69\n"
    "v_mfma_f32_16x16x32_bf16 v[104:107], a[164:167], v[36:39], v[104:107]\n"
    "ds_read_b64_tr_b16 a[196:197], v229 offset:16384\n"
    "v_cvt_pk_bf16_f32 v132, v68, v69\n"
    "v_mfma_f32_16x16x32_bf16 v[108:111], a[164:167], v[52:55], v[108:111]\n"
    "v_exp_f32 v70, v70\n"
    "v_mfma_f32_16x16x32_bf16 a[16:19], a[228:231], v[144:147], a[16:19]\n"
    "ds_read_b64_tr_b16 a[198:199], v229 offset:20480\n"
    "v_mfma_f32_16x16x32_bf16 a[20:23], a[228:231], v[148:151], a[20:23]\n"
    "v_exp_f32 v71, v71\n"
    "v_mfma_f32_16x16x32_bf16 a[24:27], a[228:231], v[152:155], a[24:27]\n"
    "ds_read_b128 a[132:135], v225 offset:32768\n"
    "v_cvt_pk_bf16_f32 v133, v70, v71\n"
    "v_mfma_f32_16x16x32_bf16 a[28:31], a[228:231], v[156:159], a[28:31]\n"
    "v_exp_f32 v72, v72\n"
    "v_mfma_f32_16x16x32_bf16 v[176:179], v[200:203], v[144:147], v[176:179]\n"
    "buffer_load_dwordx4 v233, s[40:43], s68 offen offset:1024 lds\n"
    "v_add_f32 v196, v196, v72\n"
    "v_mfma_f32_16x16x32_bf16 v[96:99], a[168:171], v[8:11], v[96:99]\n"
    "v_exp_f32 v73, v73\n"
    "v_mfma_f32_16x16x32_bf16 v[100:103], a[168:171], v[24:27], v[100:103]\n"
    "ds_read_b64_tr_b16 a[200:201], v228 offset:16896\n"
    "v_cvt_pk_bf16_f32 v136, v72, v73\n"
    "v_mfma_f32_16x16x32_bf16 v[104:107], a[168:171], v[40:43], v[104:107]\n"
    "v_exp_f32 v74, v74\n"
    "v_mfma_f32_16x16x32_bf16 v[108:111], a[168:171], v[56:59], v[108:111]\n"
    "ds_read_b64_tr_b16 a[202:203], v228 offset:20992\n"
    "v_add_f32 v197, v197, v73\n"
    "v_mfma_f32_16x16x32_bf16 a[32:35], a[232:235], v[144:147], a[32:35]\n"
    "v_exp_f32 v75, v75\n"
    "v_mfma_f32_16x16x32_bf16 a[36:39], a[232:235], v[148:151], a[36:39]\n"
    "ds_read_b128 a[136:139], v226 offset:32768\n"
    "v_add_f32 v196, v196, v74\n"
    "v_mfma_f32_16x16x32_bf16 a[40:43], a[232:235], v[152:155], a[40:43]\n"
    "v_exp_f32 v76, v76\n"
    "v_mfma_f32_16x16x32_bf16 a[44:47], a[232:235], v[156:159], a[44:47]\n"
    "ds_read_b64_tr_b16 a[204:205], v229 offset:16896\n"
    "v_cvt_pk_bf16_f32 v137, v74, v75\n"
    "v_mfma_f32_16x16x32_bf16 v[96:99], a[172:175], v[12:15], v[96:99]\n"
    "v_exp_f32 v77, v77\n"
    "v_mfma_f32_16x16x32_bf16 v[100:103], a[172:175], v[28:31], v[100:103]\n"
    "ds_read_b64_tr_b16 a[206:207], v229 offset:20992\n"
    "v_add_f32 v197, v197, v75\n"
    "v_mfma_f32_16x16x32_bf16 v[104:107], a[172:175], v[44:47], v[104:107]\n"
    "v_exp_f32 v78, v78\n"
    "v_mfma_f32_16x16x32_bf16 v[108:111], a[172:175], v[60:63], v[108:111]\n"
    "ds_read_b128 a[140:143], v227 offset:32768\n"
    "v_add_f32 v198, v198, v76\n"
    "v_mfma_f32_16x16x32_bf16 a[48:51], a[236:239], v[144:147], a[48:51]\n"
    "v_exp_f32 v79, v79\n"
    "v_mfma_f32_16x16x32_bf16 a[52:55], a[236:239], v[148:151], a[52:55]\n"
    "buffer_load_dwordx4 v234, s[40:43], s68 offen offset:2048 lds\n"
    "v_cvt_pk_bf16_f32 v140, v76, v77\n"
    "v_mfma_f32_16x16x32_bf16 a[56:59], a[236:239], v[152:155], a[56:59]\n"
    "v_exp_f32 v80, v80\n"
    "v_mfma_f32_16x16x32_bf16 a[60:63], a[236:239], v[156:159], a[60:63]\n"
    "v_exp_f32 v81, v81\n"
    "v_mfma_f32_16x16x32_bf16 v[180:183], v[200:203], v[148:151], v[180:183]\n"
    "ds_read_b64_tr_b16 a[208:209], v228 offset:17408\n"
    "v_add_f32 v199, v199, v77\n"
    "v_mfma_f32_16x16x32_bf16 v[112:115], a[176:179], v[0:3], v[160:163]\n"
    "v_exp_f32 v82, v82\n"
    "v_mfma_f32_16x16x32_bf16 v[116:119], a[176:179], v[16:19], v[164:167]\n"
    "ds_read_b64_tr_b16 a[210:211], v228 offset:21504\n"
    "v_add_f32 v198, v198, v78\n"
    "v_mfma_f32_16x16x32_bf16 v[120:123], a[176:179], v[32:35], v[168:171]\n"
    "v_exp_f32 v83, v83\n"
    "v_mfma_f32_16x16x32_bf16 v[124:127], a[176:179], v[48:51], v[172:175]\n"
    "ds_read_b128 a[144:147], v224 offset:36864\n"
    "v_cvt_pk_bf16_f32 v141, v78, v79\n"
    "v_mfma_f32_16x16x32_bf16 a[64:67], a[240:243], v[144:147], a[64:67]\n"
    "v_exp_f32 v84, v84\n"
    "v_mfma_f32_16x16x32_bf16 a[68:71], a[240:243], v[148:151], a[68:71]\n"
    "ds_read_b64_tr_b16 a[212:213], v229 offset:17408\n"
    "v_add_f32 v199, v199, v79\n"
    "v_mfma_f32_16x16x32_bf16 a[72:75], a[240:243], v[152:155], a[72:75]\n"
    "v_exp_f32 v85, v85\n"
    "v_mfma_f32_16x16x32_bf16 a[76:79], a[240:243], v[156:159], a[76:79]\n"
    "ds_read_b64_tr_b16 a[214:215], v229 offset:21504\n"
    "v_cvt_pk_bf16_f32 v130, v80, v81\n"
    "v_mfma_f32_16x16x32_bf16 v[112:115], a[180:183], v[4:7], v[112:115]\n"
    "v_exp_f32 v86, v86\n"
    "v_mfma_f32_16x16x32_bf16 v[116:119], a[180:183], v[20:23], v[116:119]\n"
    "ds_read_b128 a[148:151], v225 offset:36864\n"
    "v_cvt_pk_bf16_f32 v131, v82, v83\n"
    "v_mfma_f32_16x16x32_bf16 v[120:123], a[180:183], v[36:39], v[120:123]\n"
    "v_exp_f32 v87, v87\n"
    "v_mfma_f32_16x16x32_bf16 v[124:127], a[180:183], v[52:55], v[124:127]\n"
    "buffer_load_dwordx4 v235, s[40:43], s68 offen offset:3072 lds\n"
    "v_cvt_pk_bf16_f32 v134, v84, v85\n"
    "v_mfma_f32_16x16x32_bf16 a[80:83], a[244:247], v[144:147], a[80:83]\n"
    "v_exp_f32 v88, v88\n"
    "v_mfma_f32_16x16x32_bf16 a[84:87], a[244:247], v[148:151], a[84:87]\n"
    "ds_read_b64_tr_b16 a[216:217], v228 offset:17920\n"
    "v_cvt_pk_bf16_f32 v135, v86, v87\n"
    "v_mfma_f32_16x16x32_bf16 a[88:91], a[244:247], v[152:155], a[88:91]\n"
    "v_exp_f32 v89, v89\n"
    "v_mfma_f32_16x16x32_bf16 a[92:95], a[244:247], v[156:159], a[92:95]\n"
    "ds_read_b64_tr_b16 a[218:219], v228 offset:22016\n"
    "v_add_f32 v196, v196, v88\n"
    "v_mfma_f32_16x16x32_bf16 v[112:115], a[184:187], v[8:11], v[112:115]\n"
    "v_exp_f32 v90, v90\n"
    "v_mfma_f32_16x16x32_bf16 v[116:119], a[184:187], v[24:27], v[116:119]\n"
    "ds_read_b128 a[152:155], v226 offset:36864\n"
    "v_cvt_pk_bf16_f32 v138, v88, v89\n"
    "v_mfma_f32_16x16x32_bf16 v[120:123], a[184:187], v[40:43], v[120:123]\n"
    "v_exp_f32 v91, v91\n"
    "v_mfma_f32_16x16x32_bf16 v[124:127], a[184:187], v[56:59], v[124:127]\n"
    "ds_read_b64_tr_b16 a[220:221], v229 offset:17920\n"
    "v_add_f32 v197, v197, v89\n"
    "v_mfma_f32_16x16x32_bf16 a[96:99], a[248:251], v[144:147], a[96:99]\n"
    "v_exp_f32 v92, v92\n"
    "v_mfma_f32_16x16x32_bf16 a[100:103], a[248:251], v[148:151], a[100:103]\n"
    "ds_read_b64_tr_b16 a[222:223], v229 offset:22016\n"
    "v_add_f32 v196, v196, v90\n"
    "v_mfma_f32_16x16x32_bf16 a[104:107], a[248:251], v[152:155], a[104:107]\n"
    "v_exp_f32 v93, v93\n"
    "v_mfma_f32_16x16x32_bf16 a[108:111], a[248:251], v[156:159], a[108:111]\n"
    "ds_read_b128 a[156:159], v227 offset:36864\n"
    "v_cvt_pk_bf16_f32 v139, v90, v91\n"
    "v_mfma_f32_16x16x32_bf16 v[112:115], a[188:191], v[12:15], v[112:115]\n"
    "v_exp_f32 v94, v94\n"
    "v_mfma_f32_16x16x32_bf16 v[116:119], a[188:191], v[28:31], v[116:119]\n"
    "v_add_f32 v197, v197, v91\n"
    "v_add_f32 v198, v198, v92\n"
    "v_mfma_f32_16x16x32_bf16 v[120:123], a[188:191], v[44:47], v[120:123]\n"
    "v_exp_f32 v95, v95\n"
    "v_mfma_f32_16x16x32_bf16 v[124:127], a[188:191], v[60:63], v[124:127]\n"
    "v_cvt_pk_bf16_f32 v142, v92, v93\n"
    "v_add_f32 v199, v199, v93\n"
    "v_mfma_f32_16x16x32_bf16 a[112:115], a[252:255], v[144:147], a[112:115]\n"
    "v_add_f32 v198, v198, v94\n"
    "v_cvt_pk_bf16_f32 v143, v94, v95\n"
    "v_mfma_f32_16x16x32_bf16 a[116:119], a[252:255], v[148:151], a[116:119]\n"
    "v_add_f32 v199, v199, v95\n"
    "v_mfma_f32_16x16x32_bf16 a[120:123], a[252:255], v[152:155], a[120:123]\n"
    "v_mfma_f32_16x16x32_bf16 a[124:127], a[252:255], v[156:159], a[124:127]\n"
    "s_waitcnt lgkmcnt(0)\n"
    "v_mfma_f32_16x16x32_bf16 v[64:67], a[128:131], v[0:3], v[160:163]\n"
    "s_add_u32 m0, s62, 0\n"
    "v_exp_f32 v96, v96\n"
    "v_mfma_f32_16x16x32_bf16 v[68:71], a[128:131], v[16:19], v[164:167]\n"
    "v_exp_f32 v97, v97\n"
    "v_mfma_f32_16x16x32_bf16 v[72:75], a[128:131], v[32:35], v[168:171]\n"
    "buffer_load_dwordx4 v236, s[44:47], s60 offen lds\n"
    "v_cvt_pk_bf16_f32 v144, v96, v97\n"
    "v_mfma_f32_16x16x32_bf16 v[76:79], a[128:131], v[48:51], v[172:175]\n"
    "v_exp_f32 v98, v98\n"
    "v_mfma_f32_16x16x32_bf16 a[0:3], a[192:195], v[128:131], a[0:3]\n"
    "ds_read_b64_tr_b16 a[224:225], v228 offset:24576\n"
    "v_mfma_f32_16x16x32_bf16 a[4:7], a[192:195], v[132:135], a[4:7]\n"
    "v_exp_f32 v99, v99\n"
    "v_mfma_f32_16x16x32_bf16 a[8:11], a[192:195], v[136:139], a[8:11]\n"
    "ds_read_b64_tr_b16 a[226:227], v228 offset:28672\n"
    "v_cvt_pk_bf16_f32 v145, v98, v99\n"
    "v_mfma_f32_16x16x32_bf16 a[12:15], a[192:195], v[140:143], a[12:15]\n"
    "v_exp_f32 v100, v100\n"
    "v_mfma_f32_16x16x32_bf16 v[64:67], a[132:135], v[4:7], v[64:67]\n"
    "ds_read_b128 a[160:163], v224 offset:40960\n"
    "v_mfma_f32_16x16x32_bf16 v[68:71], a[132:135], v[20:23], v[68:71]\n"
    "v_exp_f32 v101, v101\n"
    "v_mfma_f32_16x16x32_bf16 v[72:75], a[132:135], v[36:39], v[72:75]\n"
    "ds_read_b64_tr_b16 a[228:229], v229 offset:24576\n"
    "v_cvt_pk_bf16_f32 v148, v100, v101\n"
    "v_mfma_f32_16x16x32_bf16 v[76:79], a[132:135], v[52:55], v[76:79]\n"
    "v_exp_f32 v102, v102\n"
    "v_mfma_f32_16x16x32_bf16 a[16:19], a[196:199], v[128:131], a[16:19]\n"
    "ds_read_b64_tr_b16 a[230:231], v229 offset:28672\n"
    "v_mfma_f32_16x16x32_bf16 a[20:23], a[196:199], v[132:135], a[20:23]\n"
    "v_exp_f32 v103, v103\n"
    "v_mfma_f32_16x16x32_bf16 a[24:27], a[196:199], v[136:139], a[24:27]\n"
    "ds_read_b128 a[164:167], v225 offset:40960\n"
    "v_cvt_pk_bf16_f32 v149, v102, v103\n"
    "v_mfma_f32_16x16x32_bf16 a[28:31], a[196:199], v[140:143], a[28:31]\n"
    "s_add_u32 m0, s62, 896\n"
    "v_exp_f32 v104, v104\n"
    "v_mfma_f32_16x16x32_bf16 v[176:179], v[200:203], v[128:131], v[176:179]\n"
    "buffer_load_dwordx4 v236, s[44:47], s60 offen offset:128 lds\n"
    "v_add_f32 v196, v196, v104\n"
    "v_mfma_f32_16x16x32_bf16 v[64:67], a[136:139], v[8:11], v[64:67]\n"
    "v_exp_f32 v105, v105\n"
    "v_mfma_f32_16x16x32_bf16 v[68:71], a[136:139], v[24:27], v[68:71]\n"
    "ds_read_b64_tr_b16 a[232:233], v228 offset:25088\n"
    "v_cvt_pk_bf16_f32 v152, v104, v105\n"
    "v_mfma_f32_16x16x32_bf16 v[72:75], a[136:139], v[40:43], v[72:75]\n"
    "v_exp_f32 v106, v106\n"
    "v_mfma_f32_16x16x32_bf16 v[76:79], a[136:139], v[56:59], v[76:79]\n"
    "ds_read_b64_tr_b16 a[234:235], v228 offset:29184\n"
    "v_add_f32 v197, v197, v105\n"
    "v_mfma_f32_16x16x32_bf16 a[32:35], a[200:203], v[128:131], a[32:35]\n"
    "v_exp_f32 v107, v107\n"
    "v_mfma_f32_16x16x32_bf16 a[36:39], a[200:203], v[132:135], a[36:39]\n"
    "ds_read_b128 a[168:171], v226 offset:40960\n"
    "v_add_f32 v196, v196, v106\n"
    "v_mfma_f32_16x16x32_bf16 a[40:43], a[200:203], v[136:139], a[40:43]\n"
    "v_exp_f32 v108, v108\n"
    "v_mfma_f32_16x16x32_bf16 a[44:47], a[200:203], v[140:143], a[44:47]\n"
    "ds_read_b64_tr_b16 a[236:237], v229 offset:25088\n"
    "v_cvt_pk_bf16_f32 v153, v106, v107\n"
    "v_mfma_f32_16x16x32_bf16 v[64:67], a[140:143], v[12:15], v[64:67]\n"
    "v_exp_f32 v109, v109\n"
    "v_mfma_f32_16x16x32_bf16 v[68:71], a[140:143], v[28:31], v[68:71]\n"
    "ds_read_b64_tr_b16 a[238:239], v229 offset:29184\n"
    "v_add_f32 v197, v197, v107\n"
    "v_mfma_f32_16x16x32_bf16 v[72:75], a[140:143], v[44:47], v[72:75]\n"
    "v_exp_f32 v110, v110\n"
    "v_mfma_f32_16x16x32_bf16 v[76:79], a[140:143], v[60:63], v[76:79]\n"
    "ds_read_b128 a[172:175], v227 offset:40960\n"
    "v_add_f32 v198, v198, v108\n"
    "v_mfma_f32_16x16x32_bf16 a[48:51], a[204:207], v[128:131], a[48:51]\n"
    "s_add_u32 m0, s62, 0\n"
    "v_exp_f32 v111, v111\n"
    "v_mfma_f32_16x16x32_bf16 a[52:55], a[204:207], v[132:135], a[52:55]\n"
    "buffer_load_dwordx4 v236, s[44:47], s60 offen offset:2048 lds\n"
    "v_cvt_pk_bf16_f32 v156, v108, v109\n"
    "v_mfma_f32_16x16x32_bf16 a[56:59], a[204:207], v[136:139], a[56:59]\n"
    "v_exp_f32 v112, v112\n"
    "v_mfma_f32_16x16x32_bf16 a[60:63], a[204:207], v[140:143], a[60:63]\n"
    "v_exp_f32 v113, v113\n"
    "v_mfma_f32_16x16x32_bf16 v[180:183], v[200:203], v[132:135], v[180:183]\n"
    "ds_read_b64_tr_b16 a[240:241], v228 offset:25600\n"
    "v_add_f32 v199, v199, v109\n"
    "v_mfma_f32_16x16x32_bf16 v[80:83], a[144:147], v[0:3], v[160:163]\n"
    "v_exp_f32 v114, v114\n"
    "v_mfma_f32_16x16x32_bf16 v[84:87], a[144:147], v[16:19], v[164:167]\n"
    "ds_read_b64_tr_b16 a[242:243], v228 offset:29696\n"
    "v_add_f32 v198, v198, v110\n"
    "v_mfma_f32_16x16x32_bf16 v[88:91], a[144:147], v[32:35], v[168:171]\n"
    "v_exp_f32 v115, v115\n"
    "v_mfma_f32_16x16x32_bf16 v[92:95], a[144:147], v[48:51], v[172:175]\n"
    "ds_read_b128 a[176:179], v224 offset:45056\n"
    "v_cvt_pk_bf16_f32 v157, v110, v111\n"
    "v_mfma_f32_16x16x32_bf16 a[64:67], a[208:211], v[128:131], a[64:67]\n"
    "v_exp_f32 v116, v116\n"
    "v_mfma_f32_16x16x32_bf16 a[68:71], a[208:211], v[132:135], a[68:71]\n"
    "ds_read_b64_tr_b16 a[244:245], v229 offset:25600\n"
    "v_add_f32 v199, v199, v111\n"
    "v_mfma_f32_16x16x32_bf16 a[72:75], a[208:211], v[136:139], a[72:75]\n"
    "v_exp_f32 v117, v117\n"
    "v_mfma_f32_16x16x32_bf16 a[76:79], a[208:211], v[140:143], a[76:79]\n"
    "ds_read_b64_tr_b16 a[246:247], v229 offset:29696\n"
    "v_cvt_pk_bf16_f32 v146, v112, v113\n"
    "v_mfma_f32_16x16x32_bf16 v[80:83], a[148:151], v[4:7], v[80:83]\n"
    "v_exp_f32 v118, v118\n"
    "v_mfma_f32_16x16x32_bf16 v[84:87], a[148:151], v[20:23], v[84:87]\n"
    "ds_read_b128 a[180:183], v225 offset:45056\n"
    "v_cvt_pk_bf16_f32 v147, v114, v115\n"
    "v_mfma_f32_16x16x32_bf16 v[88:91], a[148:151], v[36:39], v[88:91]\n"
    "s_add_u32 m0, s62, 896\n"
    "v_exp_f32 v119, v119\n"
    "v_mfma_f32_16x16x32_bf16 v[92:95], a[148:151], v[52:55], v[92:95]\n"
    "buffer_load_dwordx4 v236, s[44:47], s60 offen offset:2176 lds\n"
    "v_cvt_pk_bf16_f32 v150, v116, v117\n"
    "v_mfma_f32_16x16x32_bf16 a[80:83], a[212:215], v[128:131], a[80:83]\n"
    "v_exp_f32 v120, v120\n"
    "v_mfma_f32_16x16x32_bf16 a[84:87], a[212:215], v[132:135], a[84:87]\n"
    "ds_read_b64_tr_b16 a[248:249], v228 offset:26112\n"
    "v_cvt_pk_bf16_f32 v151, v118, v119\n"
    "v_mfma_f32_16x16x32_bf16 a[88:91], a[212:215], v[136:139], a[88:91]\n"
    "v_exp_f32 v121, v121\n"
    "v_mfma_f32_16x16x32_bf16 a[92:95], a[212:215], v[140:143], a[92:95]\n"
    "ds_read_b64_tr_b16 a[250:251], v228 offset:30208\n"
    "v_add_f32 v196, v196, v120\n"
    "v_mfma_f32_16x16x32_bf16 v[80:83], a[152:155], v[8:11], v[80:83]\n"
    "v_exp_f32 v122, v122\n"
    "v_mfma_f32_16x16x32_bf16 v[84:87], a[152:155], v[24:27], v[84:87]\n"
    "ds_read_b128 a[184:187], v226 offset:45056\n"
    "v_cvt_pk_bf16_f32 v154, v120, v121\n"
    "v_mfma_f32_16x16x32_bf16 v[88:91], a[152:155], v[40:43], v[88:91]\n"
    "v_exp_f32 v123, v123\n"
    "v_mfma_f32_16x16x32_bf16 v[92:95], a[152:155], v[56:59], v[92:95]\n"
    "ds_read_b64_tr_b16 a[252:253], v229 offset:26112\n"
    "v_add_f32 v197, v197, v121\n"
    "v_mfma_f32_16x16x32_bf16 a[96:99], a[216:219], v[128:131], a[96:99]\n"
    "v_exp_f32 v124, v124\n"
    "v_mfma_f32_16x16x32_bf16 a[100:103], a[216:219], v[132:135], a[100:103]\n"
    "ds_read_b64_tr_b16 a[254:255], v229 offset:30208\n"
    "v_add_f32 v196, v196, v122\n"
    "v_mfma_f32_16x16x32_bf16 a[104:107], a[216:219], v[136:139], a[104:107]\n"
    "v_exp_f32 v125, v125\n"
    "v_mfma_f32_16x16x32_bf16 a[108:111], a[216:219], v[140:143], a[108:111]\n"
    "ds_read_b128 a[188:191], v227 offset:45056\n"
    "v_cvt_pk_bf16_f32 v155, v122, v123\n"
    "v_mfma_f32_16x16x32_bf16 v[80:83], a[156:159], v[12:15], v[80:83]\n"
    "v_exp_f32 v126, v126\n"
    "v_mfma_f32_16x16x32_bf16 v[84:87], a[156:159], v[28:31], v[84:87]\n"
    "v_add_f32 v197, v197, v123\n"
    "v_add_f32 v198, v198, v124\n"
    "v_mfma_f32_16x16x32_bf16 v[88:91], a[156:159], v[44:47], v[88:91]\n"
    "v_exp_f32 v127, v127\n"
    "v_mfma_f32_16x16x32_bf16 v[92:95], a[156:159], v[60:63], v[92:95]\n"
    "v_cvt_pk_bf16_f32 v158, v124, v125\n"
    "v_add_f32 v199, v199, v125\n"
    "v_mfma_f32_16x16x32_bf16 a[112:115], a[220:223], v[128:131], a[112:115]\n"
    "v_add_f32 v198, v198, v126\n"
    "v_cvt_pk_bf16_f32 v159, v126, v127\n"
    "v_mfma_f32_16x16x32_bf16 a[116:119], a[220:223], v[132:135], a[116:119]\n"
    "v_add_f32 v199, v199, v127\n"
    "v_mfma_f32_16x16x32_bf16 a[120:123], a[220:223], v[136:139], a[120:123]\n"
    "v_mfma_f32_16x16x32_bf16 a[124:127], a[220:223], v[140:143], a[124:127]\n"
    "s_add_u32 s60, s60, 16384\n"
    "s_waitcnt vmcnt(16) lgkmcnt(0)\n"
    "s_barrier\n"
    "s_add_u32 s68, s60, 16384\n"
    "s_min_u32 s68, s68, s69\n"
    "v_mfma_f32_16x16x32_bf16 v[96:99], a[160:163], v[0:3], v[160:163]\n"
    "s_add_u32 m0, s61, 32768\n"
    "v_exp_f32 v64, v64\n"
    "v_mfma_f32_16x16x32_bf16 v[100:103], a[160:163], v[16:19], v[164:167]\n"
    "v_exp_f32 v65, v65\n"
    "v_mfma_f32_16x16x32_bf16 v[104:107], a[160:163], v[32:35], v[168:171]\n"
    "buffer_load_dwordx4 v232, s[40:43], s68 offen lds\n"
    "v_cvt_pk_bf16_f32 v128, v64, v65\n"
    "v_mfma_f32_16x16x32_bf16 v[108:111], a[160:163], v[48:51], v[172:175]\n"
    "v_exp_f32 v66, v66\n"
    "v_mfma_f32_16x16x32_bf16 a[0:3], a[224:227], v[144:147], a[0:3]\n"
    "ds_read_b64_tr_b16 a[192:193], v228 offset:32768\n"
    "v_mfma_f32_16x16x32_bf16 a[4:7], a[224:227], v[148:151], a[4:7]\n"
    "v_exp_f32 v67, v67\n"
    "v_mfma_f32_16x16x32_bf16 a[8:11], a[224:227], v[152:155], a[8:11]\n"
    "ds_read_b64_tr_b16 a[194:195], v228 offset:36864\n"
    "v_cvt_pk_bf16_f32 v129, v66, v67\n"
    "v_mfma_f32_16x16x32_bf16 a[12:15], a[224:227], v[156:159], a[12:15]\n"
    "v_exp_f32 v68, v68\n"
    "v_mfma_f32_16x16x32_bf16 v[96:99], a[164:167], v[4:7], v[96:99]\n"
    "ds_read_b128 a[128:131], v224 offset:49152\n"
    "v_mfma_f32_16x16x32_bf16 v[100:103], a[164:167], v[20:23], v[100:103]\n"
    "v_exp_f32 v69, v69\n"
    "v_mfma_f32_16x16x32_bf16 v[104:107], a[164:167], v[36:39], v[104:107]\n"
    "ds_read_b64_tr_b16 a[196:197], v229 offset:32768\n"
    "v_cvt_pk_bf16_f32 v132, v68, v69\n"
    "v_mfma_f32_16x16x32_bf16 v[108:111], a[164:167], v[52:55], v[108:111]\n"
    "v_exp_f32 v70, v70\n"
    "v_mfma_f32_16x16x32_bf16 a[16:19], a[228:231], v[144:147], a[16:19]\n"
    "ds_read_b64_tr_b16 a[198:199], v229 offset:36864\n"
    "v_mfma_f32_16x16x32_bf16 a[20:23], a[228:231], v[148:151], a[20:23]\n"
    "v_exp_f32 v71, v71\n"
    "v_mfma_f32_16x16x32_bf16 a[24:27], a[228:231], v[152:155], a[24:27]\n"
    "ds_read_b128 a[132:135], v225 offset:49152\n"
    "v_cvt_pk_bf16_f32 v133, v70, v71\n"
    "v_mfma_f32_16x16x32_bf16 a[28:31], a[228:231], v[156:159], a[28:31]\n"
    "v_exp_f32 v72, v72\n"
    "v_mfma_f32_16x16x32_bf16 v[176:179], v[200:203], v[144:147], v[176:179]\n"
    "buffer_load_dwordx4 v233, s[40:43], s68 offen offset:1024 lds\n"
    "v_add_f32 v196, v196, v72\n"
    "v_mfma_f32_16x16x32_bf16 v[96:99], a[168:171], v[8:11], v[96:99]\n"
    "v_exp_f32 v73, v73\n"
    "v_mfma_f32_16x16x32_bf16 v[100:103], a[168:171], v[24:27], v[100:103]\n"
    "ds_read_b64_tr_b16 a[200:201], v228 offset:33280\n"
    "v_cvt_pk_bf16_f32 v136, v72, v73\n"
    "v_mfma_f32_16x16x32_bf16 v[104:107], a[168:171], v[40:43], v[104:107]\n"
    "v_exp_f32 v74, v74\n"
    "v_mfma_f32_16x16x32_bf16 v[108:111], a[168:171], v[56:59], v[108:111]\n"
    "ds_read_b64_tr_b16 a[202:203], v228 offset:37376\n"
    "v_add_f32 v197, v197, v73\n"
    "v_mfma_f32_16x16x32_bf16 a[32:35], a[232:235], v[144:147], a[32:35]\n"
    "v_exp_f32 v75, v75\n"
    "v_mfma_f32_16x16x32_bf16 a[36:39], a[232:235], v[148:151], a[36:39]\n"
    "ds_read_b128 a[136:139], v226 offset:49152\n"
    "v_add_f32 v196, v196, v74\n"
    "v_mfma_f32_16x16x32_bf16 a[40:43], a[232:235], v[152:155], a[40:43]\n"
    "v_exp_f32 v76, v76\n"
    "v_mfma_f32_16x16x32_bf16 a[44:47], a[232:235], v[156:159], a[44:47]\n"
    "ds_read_b64_tr_b16 a[204:205], v229 offset:33280\n"
    "v_cvt_pk_bf16_f32 v137, v74, v75\n"
    "v_mfma_f32_16x16x32_bf16 v[96:99], a[172:175], v[12:15], v[96:99]\n"
    "v_exp_f32 v77, v77\n"
    "v_mfma_f32_16x16x32_bf16 v[100:103], a[172:175], v[28:31], v[100:103]\n"
    "ds_read_b64_tr_b16 a[206:207], v229 offset:37376\n"
    "v_add_f32 v197, v197, v75\n"
    "v_mfma_f32_16x16x32_bf16 v[104:107], a[172:175], v[44:47], v[104:107]\n"
    "v_exp_f32 v78, v78\n"
    "v_mfma_f32_16x16x32_bf16 v[108:111], a[172:175], v[60:63], v[108:111]\n"
    "ds_read_b128 a[140:143], v227 offset:49152\n"
    "v_add_f32 v198, v198, v76\n"
    "v_mfma_f32_16x16x32_bf16 a[48:51], a[236:239], v[144:147], a[48:51]\n"
    "v_exp_f32 v79, v79\n"
    "v_mfma_f32_16x16x32_bf16 a[52:55], a[236:239], v[148:151], a[52:55]\n"
    "buffer_load_dwordx4 v234, s[40:43], s68 offen offset:2048 lds\n"
    "v_cvt_pk_bf16_f32 v140, v76, v77\n"
    "v_mfma_f32_16x16x32_bf16 a[56:59], a[236:239], v[152:155], a[56:59]\n"
    "v_exp_f32 v80, v80\n"
    "v_mfma_f32_16x16x32_bf16 a[60:63], a[236:239], v[156:159], a[60:63]\n"
    "v_exp_f32 v81, v81\n"
    "v_mfma_f32_16x16x32_bf16 v[180:183], v[200:203], v[148:151], v[180:183]\n"
    "ds_read_b64_tr_b16 a[208:209], v228 offset:33792\n"
    "v_add_f32 v199, v199, v77\n"
    "v_mfma_f32_16x16x32_bf16 v[112:115], a[176:179], v[0:3], v[160:163]\n"
    "v_exp_f32 v82, v82\n"
    "v_mfma_f32_16x16x32_bf16 v[116:119], a[176:179], v[16:19], v[164:167]\n"
    "ds_read_b64_tr_b16 a[210:211], v228 offset:37888\n"
    "v_add_f32 v198, v198, v78\n"
    "v_mfma_f32_16x16x32_bf16 v[120:123], a[176:179], v[32:35], v[168:171]\n"
    "v_exp_f32 v83, v83\n"
    "v_mfma_f32_16x16x32_bf16 v[124:127], a[176:179], v[48:51], v[172:175]\n"
    "ds_read_b128 a[144:147], v224 offset:53248\n"
    "v_cvt_pk_bf16_f32 v141, v78, v79\n"
    "v_mfma_f32_16x16x32_bf16 a[64:67], a[240:243], v[144:147], a[64:67]\n"
    "v_exp_f32 v84, v84\n"
    "v_mfma_f32_16x16x32_bf16 a[68:71], a[240:243], v[148:151], a[68:71]\n"
    "ds_read_b64_tr_b16 a[212:213], v229 offset:33792\n"
    "v_add_f32 v199, v199, v79\n"
    "v_mfma_f32_16x16x32_bf16 a[72:75], a[240:243], v[152:155], a[72:75]\n"
    "v_exp_f32 v85, v85\n"
    "v_mfma_f32_16x16x32_bf16 a[76:79], a[240:243], v[156:159], a[76:79]\n"
    "ds_read_b64_tr_b16 a[214:215], v229 offset:37888\n"
    "v_cvt_pk_bf16_f32 v130, v80, v81\n"
    "v_mfma_f32_16x16x32_bf16 v[112:115], a[180:183], v[4:7], v[112:115]\n"
    "v_exp_f32 v86, v86\n"
    "v_mfma_f32_16x16x32_bf16 v[116:119], a[180:183], v[20:23], v[116:119]\n"
    "ds_read_b128 a[148:151], v225 offset:53248\n"
    "v_cvt_pk_bf16_f32 v131, v82, v83\n"
    "v_mfma_f32_16x16x32_bf16 v[120:123], a[180:183], v[36:39], v[120:123]\n"
    "v_exp_f32 v87, v87\n"
    "v_mfma_f32_16x16x32_bf16 v[124:127], a[180:183], v[52:55], v[124:127]\n"
    "buffer_load_dwordx4 v235, s[40:43], s68 offen offset:3072 lds\n"
    "v_cvt_pk_bf16_f32 v134, v84, v85\n"
    "v_mfma_f32_16x16x32_bf16 a[80:83], a[244:247], v[144:147], a[80:83]\n"
    "v_exp_f32 v88, v88\n"
    "v_mfma_f32_16x16x32_bf16 a[84:87], a[244:247], v[148:151], a[84:87]\n"
    "ds_read_b64_tr_b16 a[216:217], v228 offset:34304\n"
    "v_cvt_pk_bf16_f32 v135, v86, v87\n"
    "v_mfma_f32_16x16x32_bf16 a[88:91], a[244:247], v[152:155], a[88:91]\n"
    "v_exp_f32 v89, v89\n"
    "v_mfma_f32_16x16x32_bf16 a[92:95], a[244:247], v[156:159], a[92:95]\n"
    "ds_read_b64_tr_b16 a[218:219], v228 offset:38400\n"
    "v_add_f32 v196, v196, v88\n"
    "v_mfma_f32_16x16x32_bf16 v[112:115], a[184:187], v[8:11], v[112:115]\n"
    "v_exp_f32 v90, v90\n"
    "v_mfma_f32_16x16x32_bf16 v[116:119], a[184:187], v[24:27], v[116:119]\n"
    "ds_read_b128 a[152:155], v226 offset:53248\n"
    "v_cvt_pk_bf16_f32 v138, v88, v89\n"
    "v_mfma_f32_16x16x32_bf16 v[120:123], a[184:187], v[40:43], v[120:123]\n"
    "v_exp_f32 v91, v91\n"
    "v_mfma_f32_16x16x32_bf16 v[124:127], a[184:187], v[56:59], v[124:127]\n"
    "ds_read_b64_tr_b16 a[220:221], v229 offset:34304\n"
    "v_add_f32 v197, v197, v89\n"
    "v_mfma_f32_16x16x32_bf16 a[96:99], a[248:251], v[144:147], a[96:99]\n"
    "v_exp_f32 v92, v92\n"
    "v_mfma_f32_16x16x32_bf16 a[100:103], a[248:251], v[148:151], a[100:103]\n"
    "ds_read_b64_tr_b16 a[222:223], v229 offset:38400\n"
    "v_add_f32 v196, v196, v90\n"
    "v_mfma_f32_16x16x32_bf16 a[104:107], a[248:251], v[152:155], a[104:107]\n"
    "v_exp_f32 v93, v93\n"
    "v_mfma_f32_16x16x32_bf16 a[108:111], a[248:251], v[156:159], a[108:111]\n"
    "ds_read_b128 a[156:159], v227 offset:53248\n"
    "v_cvt_pk_bf16_f32 v139, v90, v91\n"
    "v_mfma_f32_16x16x32_bf16 v[112:115], a[188:191], v[12:15], v[112:115]\n"
    "v_exp_f32 v94, v94\n"
    "v_mfma_f32_16x16x32_bf16 v[116:119], a[188:191], v[28:31], v[116:119]\n"
    "v_add_f32 v197, v197, v91\n"
    "v_add_f32 v198, v198, v92\n"
    "v_mfma_f32_16x16x32_bf16 v[120:123], a[188:191], v[44:47], v[120:123]\n"
    "v_exp_f32 v95, v95\n"
    "v_mfma_f32_16x16x32_bf16 v[124:127], a[188:191], v[60:63], v[124:127]\n"
    "v_cvt_pk_bf16_f32 v142, v92, v93\n"
    "v_add_f32 v199, v199, v93\n"
    "v_mfma_f32_16x16x32_bf16 a[112:115], a[252:255], v[144:147], a[112:115]\n"
    "v_add_f32 v198, v198, v94\n"
    "v_cvt_pk_bf16_f32 v143, v94, v95\n"
    "v_mfma_f32_16x16x32_bf16 a[116:119], a[252:255], v[148:151], a[116:119]\n"
    "v_add_f32 v199, v199, v95\n"
    "v_mfma_f32_16x16x32_bf16 a[120:123], a[252:255], v[152:155], a[120:123]\n"
    "v_mfma_f32_16x16x32_bf16 a[124:127], a[252:255], v[156:159], a[124:127]\n"
    "s_waitcnt lgkmcnt(0)\n"
    "v_mfma_f32_16x16x32_bf16 v[64:67], a[128:131], v[0:3], v[160:163]\n"
    "s_add_u32 m0, s62, 16384\n"
    "v_exp_f32 v96, v96\n"
    "v_mfma_f32_16x16x32_bf16 v[68:71], a[128:131], v[16:19], v[164:167]\n"
    "v_exp_f32 v97, v97\n"
    "v_mfma_f32_16x16x32_bf16 v[72:75], a[128:131], v[32:35], v[168:171]\n"
    "buffer_load_dwordx4 v236, s[44:47], s60 offen lds\n"
    "v_cvt_pk_bf16_f32 v144, v96, v97\n"
    "v_mfma_f32_16x16x32_bf16 v[76:79], a[128:131], v[48:51], v[172:175]\n"
    "v_exp_f32 v98, v98\n"
    "v_mfma_f32_16x16x32_bf16 a[0:3], a[192:195], v[128:131], a[0:3]\n"
    "ds_read_b64_tr_b16 a[224:225], v228 offset:40960\n"
    "v_mfma_f32_16x16x32_bf16 a[4:7], a[192:195], v[132:135], a[4:7]\n"
    "v_exp_f32 v99, v99\n"
    "v_mfma_f32_16x16x32_bf16 a[8:11], a[192:195], v[136:139], a[8:11]\n"
    "ds_read_b64_tr_b16 a[226:227], v228 offset:45056\n"
    "v_cvt_pk_bf16_f32 v145, v98, v99\n"
    "v_mfma_f32_16x16x32_bf16 a[12:15], a[192:195], v[140:143], a[12:15]\n"
    "v_exp_f32 v100, v100\n"
    "v_mfma_f32_16x16x32_bf16 v[64:67], a[132:135], v[4:7], v[64:67]\n"
    "ds_read_b128 a[160:163], v224 offset:57344\n"
    "v_mfma_f32_16x16x32_bf16 v[68:71], a[132:135], v[20:23], v[68:71]\n"
    "v_exp_f32 v101, v101\n"
    "v_mfma_f32_16x16x32_bf16 v[72:75], a[132:135], v[36:39], v[72:75]\n"
    "ds_read_b64_tr_b16 a[228:229], v229 offset:40960\n"
    "v_cvt_pk_bf16_f32 v148, v100, v101\n"
    "v_mfma_f32_16x16x32_bf16 v[76:79], a[132:135], v[52:55], v[76:79]\n"
    "v_exp_f32 v102, v102\n"
    "v_mfma_f32_16x16x32_bf16 a[16:19], a[196:199], v[128:131], a[16:19]\n"
    "ds_read_b64_tr_b16 a[230:231], v229 offset:45056\n"
    "v_mfma_f32_16x16x32_bf16 a[20:23], a[196:199], v[132:135], a[20:23]\n"
    "v_exp_f32 v103, v103\n"
    "v_mfma_f32_16x16x32_bf16 a[24:27], a[196:199], v[136:139], a[24:27]\n"
    "ds_read_b128 a[164:167], v225 offset:57344\n"
    "v_cvt_pk_bf16_f32 v149, v102, v103\n"
    "v_mfma_f32_16x16x32_bf16 a[28:31], a[196:199], v[140:143], a[28:31]\n"
    "s_add_u32 m0, s62, 17280\n"
    "v_exp_f32 v104, v104\n"
    "v_mfma_f32_16x16x32_bf16 v[176:179], v[200:203], v[128:131], v[176:179]\n"
    "buffer_load_dwordx4 v236, s[44:47], s60 offen offset:128 lds\n"
    "v_add_f32 v196, v196, v104\n"
    "v_mfma_f32_16x16x32_bf16 v[64:67], a[136:139], v[8:11], v[64:67]\n"
    "v_exp_f32 v105, v105\n"
    "v_mfma_f32_16x16x32_bf16 v[68:71], a[136:139], v[24:27], v[68:71]\n"
    "ds_read_b64_tr_b16 a[232:233], v228 offset:41472\n"
    "v_cvt_pk_bf16_f32 v152, v104, v105\n"
    "v_mfma_f32_16x16x32_bf16 v[72:75], a[136:139], v[40:43], v[72:75]\n"
    "v_exp_f32 v106, v106\n"
    "v_mfma_f32_16x16x32_bf16 v[76:79], a[136:139], v[56:59], v[76:79]\n"
    "ds_read_b64_tr_b16 a[234:235], v228 offset:45568\n"
    "v_add_f32 v197, v197, v105\n"
    "v_mfma_f32_16x16x32_bf16 a[32:35], a[200:203], v[128:131], a[32:35]\n"
    "v_exp_f32 v107, v107\n"
    "v_mfma_f32_16x16x32_bf16 a[36:39], a[200:203], v[132:135], a[36:39]\n"
    "ds_read_b128 a[168:171], v226 offset:57344\n"
    "v_add_f32 v196, v196, v106\n"
    "v_mfma_f32_16x16x32_bf16 a[40:43], a[200:203], v[136:139], a[40:43]\n"
    "v_exp_f32 v108, v108\n"
    "v_mfma_f32_16x16x32_bf16 a[44:47], a[200:203], v[140:143], a[44:47]\n"
    "ds_read_b64_tr_b16 a[236:237], v229 offset:41472\n"
    "v_cvt_pk_bf16_f32 v153, v106, v107\n"
    "v_mfma_f32_16x16x32_bf16 v[64:67], a[140:143], v[12:15], v[64:67]\n"
    "v_exp_f32 v109, v109\n"
    "v_mfma_f32_16x16x32_bf16 v[68:71], a[140:143], v[28:31], v[68:71]\n"
    "ds_read_b64_tr_b16 a[238:239], v229 offset:45568\n"
    "v_add_f32 v197, v197, v107\n"
    "v_mfma_f32_16x16x32_bf16 v[72:75], a[140:143], v[44:47], v[72:75]\n"
    "v_exp_f32 v110, v110\n"
    "v_mfma_f32_16x16x32_bf16 v[76:79], a[140:143], v[60:63], v[76:79]\n"
    "ds_read_b128 a[172:175], v227 offset:57344\n"
    "v_add_f32 v198, v198, v108\n"
    "v_mfma_f32_16x16x32_bf16 a[48:51], a[204:207], v[128:131], a[48:51]\n"
    "s_add_u32 m0, s62, 16384\n"
    "v_exp_f32 v111, v111\n"
    "v_mfma_f32_16x16x32_bf16 a[52:55], a[204:207], v[132:135], a[52:55]\n"
    "buffer_load_dwordx4 v236, s[44:47], s60 offen offset:2048 lds\n"
    "v_cvt_pk_bf16_f32 v156, v108, v109\n"
    "v_mfma_f32_16x16x32_bf16 a[56:59], a[204:207], v[136:139], a[56:59]\n"
    "v_exp_f32 v112, v112\n"
    "v_mfma_f32_16x16x32_bf16 a[60:63], a[204:207], v[140:143], a[60:63]\n"
    "v_exp_f32 v113, v113\n"
    "v_mfma_f32_16x16x32_bf16 v[180:183], v[200:203], v[132:135], v[180:183]\n"
    "ds_read_b64_tr_b16 a[240:241], v228 offset:41984\n"
    "v_add_f32 v199, v199, v109\n"
    "v_mfma_f32_16x16x32_bf16 v[80:83], a[144:147], v[0:3], v[160:163]\n"
    "v_exp_f32 v114, v114\n"
    "v_mfma_f32_16x16x32_bf16 v[84:87], a[144:147], v[16:19], v[164:167]\n"
    "ds_read_b64_tr_b16 a[242:243], v228 offset:46080\n"
    "v_add_f32 v198, v198, v110\n"
    "v_mfma_f32_16x16x32_bf16 v[88:91], a[144:147], v[32:35], v[168:171]\n"
    "v_exp_f32 v115, v115\n"
    "v_mfma_f32_16x16x32_bf16 v[92:95], a[144:147], v[48:51], v[172:175]\n"
    "ds_read_b128 a[176:179], v224 offset:61440\n"
    "v_cvt_pk_bf16_f32 v157, v110, v111\n"
    "v_mfma_f32_16x16x32_bf16 a[64:67], a[208:211], v[128:131], a[64:67]\n"
    "v_exp_f32 v116, v116\n"
    "v_mfma_f32_16x16x32_bf16 a[68:71], a[208:211], v[132:135], a[68:71]\n"
    "ds_read_b64_tr_b16 a[244:245], v229 offset:41984\n"
    "v_add_f32 v199, v199, v111\n"
    "v_mfma_f32_16x16x32_bf16 a[72:75], a[208:211], v[136:139], a[72:75]\n"
    "v_exp_f32 v117, v117\n"
    "v_mfma_f32_16x16x32_bf16 a[76:79], a[208:211], v[140:143], a[76:79]\n"
    "ds_read_b64_tr_b16 a[246:247], v229 offset:46080\n"
    "v_cvt_pk_bf16_f32 v146, v112, v113\n"
    "v_mfma_f32_16x16x32_bf16 v[80:83], a[148:151], v[4:7], v[80:83]\n"
    "v_exp_f32 v118, v118\n"
    "v_mfma_f32_16x16x32_bf16 v[84:87], a[148:151], v[20:23], v[84:87]\n"
    "ds_read_b128 a[180:183], v225 offset:61440\n"
    "v_cvt_pk_bf16_f32 v147, v114, v115\n"
    "v_mfma_f32_16x16x32_bf16 v[88:91], a[148:151], v[36:39], v[88:91]\n"
    "s_add_u32 m0, s62, 17280\n"
    "v_exp_f32 v119, v119\n"
    "v_mfma_f32_16x16x32_bf16 v[92:95], a[148:151], v[52:55], v[92:95]\n"
    "buffer_load_dwordx4 v236, s[44:47], s60 offen offset:2176 lds\n"
    "v_cvt_pk_bf16_f32 v150, v116, v117\n"
    "v_mfma_f32_16x16x32_bf16 a[80:83], a[212:215], v[128:131], a[80:83]\n"
    "v_exp_f32 v120, v120\n"
    "v_mfma_f32_16x16x32_bf16 a[84:87], a[212:215], v[132:135], a[84:87]\n"
    "ds_read_b64_tr_b16 a[248:249], v228 offset:42496\n"
    "v_cvt_pk_bf16_f32 v151, v118, v119\n"
    "v_mfma_f32_16x16x32_bf16 a[88:91], a[212:215], v[136:139], a[88:91]\n"
    "v_exp_f32 v121, v121\n"
    "v_mfma_f32_16x16x32_bf16 a[92:95], a[212:215], v[140:143], a[92:95]\n"
    "ds_read_b64_tr_b16 a[250:251], v228 offset:46592\n"
    "v_add_f32 v196, v196, v120\n"
    "v_mfma_f32_16x16x32_bf16 v[80:83], a[152:155], v[8:11], v[80:83]\n"
    "v_exp_f32 v122, v122\n"
    "v_mfma_f32_16x16x32_bf16 v[84:87], a[152:155], v[24:27], v[84:87]\n"
    "ds_read_b128 a[184:187], v226 offset:61440\n"
    "v_cvt_pk_bf16_f32 v154, v120, v121\n"
    "v_mfma_f32_16x16x32_bf16 v[88:91], a[152:155], v[40:43], v[88:91]\n"
    "v_exp_f32 v123, v123\n"
    "v_mfma_f32_16x16x32_bf16 v[92:95], a[152:155], v[56:59], v[92:95]\n"
    "ds_read_b64_tr_b16 a[252:253], v229 offset:42496\n"
    "v_add_f32 v197, v197, v121\n"
    "v_mfma_f32_16x16x32_bf16 a[96:99], a[216:219], v[128:131], a[96:99]\n"
    "v_exp_f32 v124, v124\n"
    "v_mfma_f32_16x16x32_bf16 a[100:103], a[216:219], v[132:135], a[100:103]\n"
    "ds_read_b64_tr_b16 a[254:255], v229 offset:46592\n"
    "v_add_f32 v196, v196, v122\n"
    "v_mfma_f32_16x16x32_bf16 a[104:107], a[216:219], v[136:139], a[104:107]\n"
    "v_exp_f32 v125, v125\n"
    "v_mfma_f32_16x16x32_bf16 a[108:111], a[216:219], v[140:143], a[108:111]\n"
    "ds_read_b128 a[188:191], v227 offset:61440\n"
    "v_cvt_pk_bf16_f32 v155, v122, v123\n"
    "v_mfma_f32_16x16x32_bf16 v[80:83], a[156:159], v[12:15], v[80:83]\n"
    "v_exp_f32 v126, v126\n"
    "v_mfma_f32_16x16x32_bf16 v[84:87], a[156:159], v[28:31], v[84:87]\n"
    "v_add_f32 v197, v197, v123\n"
    "v_add_f32 v198, v198, v124\n"
    "v_mfma_f32_16x16x32_bf16 v[88:91], a[156:159], v[44:47], v[88:91]\n"
    "v_exp_f32 v127, v127\n"
    "v_mfma_f32_16x16x32_bf16 v[92:95], a[156:159], v[60:63], v[92:95]\n"
    "v_cvt_pk_bf16_f32 v158, v124, v125\n"
    "v_add_f32 v199, v199, v125\n"
    "v_mfma_f32_16x16x32_bf16 a[112:115], a[220:223], v[128:131], a[112:115]\n"
    "v_add_f32 v198, v198, v126\n"
    "v_cvt_pk_bf16_f32 v159, v126, v127\n"
    "v_mfma_f32_16x16x32_bf16 a[116:119], a[220:223], v[132:135], a[116:119]\n"
    "v_add_f32 v199, v199, v127\n"
    "v_mfma_f32_16x16x32_bf16 a[120:123], a[220:223], v[136:139], a[120:123]\n"
    "v_mfma_f32_16x16x32_bf16 a[124:127], a[220:223], v[140:143], a[124:127]\n"
    "s_add_u32 s60, s60, 16384\n"
    "s_waitcnt vmcnt(16) lgkmcnt(0)\n"
    "s_barrier\n"
    "s_add_u32 s68, s60, 16384\n"
    "s_min_u32 s68, s68, s69\n"
    "v_mfma_f32_16x16x32_bf16 v[96:99], a[160:163], v[0:3], v[160:163]\n"
    "s_add_u32 m0, s61, 49152\n"
    "v_exp_f32 v64, v64\n"
    "v_mfma_f32_16x16x32_bf16 v[100:103], a[160:163], v[16:19], v[164:167]\n"
    "v_exp_f32 v65, v65\n"
    "v_mfma_f32_16x16x32_bf16 v[104:107], a[160:163], v[32:35], v[168:171]\n"
    "buffer_load_dwordx4 v232, s[40:43], s68 offen lds\n"
    "v_cvt_pk_bf16_f32 v128, v64, v65\n"
    "v_mfma_f32_16x16x32_bf16 v[108:111], a[160:163], v[48:51], v[172:175]\n"
    "v_exp_f32 v66, v66\n"
    "v_mfma_f32_16x16x32_bf16 a[0:3], a[224:227], v[144:147], a[0:3]\n"
    "ds_read_b64_tr_b16 a[192:193], v228 offset:49152\n"
    "v_mfma_f32_16x16x32_bf16 a[4:7], a[224:227], v[148:151], a[4:7]\n"
    "v_exp_f32 v67, v67\n"
    "v_mfma_f32_16x16x32_bf16 a[8:11], a[224:227], v[152:155], a[8:11]\n"
    "ds_read_b64_tr_b16 a[194:195], v228 offset:53248\n"
    "v_cvt_pk_bf16_f32 v129, v66, v67\n"
    "v_mfma_f32_16x16x32_bf16 a[12:15], a[224:227], v[156:159], a[12:15]\n"
    "v_exp_f32 v68, v68\n"
    "v_mfma_f32_16x16x32_bf16 v[96:99], a[164:167], v[4:7], v[96:99]\n"
    "ds_read_b128 a[128:131], v224 offset:0\n"
    "v_mfma_f32_16x16x32_bf16 v[100:103], a[164:167], v[20:23], v[100:103]\n"
    "v_exp_f32 v69, v69\n"
    "v_mfma_f32_16x16x32_bf16 v[104:107], a[164:167], v[36:39], v[104:107]\n"
    "ds_read_b64_tr_b16 a[196:197], v229 offset:49152\n"
    "v_cvt_pk_bf16_f32 v132, v68, v69\n"
    "v_mfma_f32_16x16x32_bf16 v[108:111], a[164:167], v[52:55], v[108:111]\n"
    "v_exp_f32 v70, v70\n"
    "v_mfma_f32_16x16x32_bf16 a[16:19], a[228:231], v[144:147], a[16:19]\n"
    "ds_read_b64_tr_b16 a[198:199], v229 offset:53248\n"
    "v_mfma_f32_16x16x32_bf16 a[20:23], a[228:231], v[148:151], a[20:23]\n"
    "v_exp_f32 v71, v71\n"
    "v_mfma_f32_16x16x32_bf16 a[24:27], a[228:231], v[152:155], a[24:27]\n"
    "ds_read_b128 a[132:135], v225 offset:0\n"
    "v_cvt_pk_bf16_f32 v133, v70, v71\n"
    "v_mfma_f32_16x16x32_bf16 a[28:31], a[228:231], v[156:159], a[28:31]\n"
    "v_exp_f32 v72, v72\n"
    "v_mfma_f32_16x16x32_bf16 v[176:179], v[200:203], v[144:147], v[176:179]\n"
    "buffer_load_dwordx4 v233, s[40:43], s68 offen offset:1024 lds\n"
    "v_add_f32 v196, v196, v72\n"
    "v_mfma_f32_16x16x32_bf16 v[96:99], a[168:171], v[8:11], v[96:99]\n"
    "v_exp_f32 v73, v73\n"
    "v_mfma_f32_16x16x32_bf16 v[100:103], a[168:171], v[24:27], v[100:103]\n"
    "ds_read_b64_tr_b16 a[200:201], v228 offset:49664\n"
    "v_cvt_pk_bf16_f32 v136, v72, v73\n"
    "v_mfma_f32_16x16x32_bf16 v[104:107], a[168:171], v[40:43], v[104:107]\n"
    "v_exp_f32 v74, v74\n"
    "v_mfma_f32_16x16x32_bf16 v[108:111], a[168:171], v[56:59], v[108:111]\n"
    "ds_read_b64_tr_b16 a[202:203], v228 offset:53760\n"
    "v_add_f32 v197, v197, v73\n"
    "v_mfma_f32_16x16x32_bf16 a[32:35], a[232:235], v[144:147], a[32:35]\n"
    "v_exp_f32 v75, v75\n"
    "v_mfma_f32_16x16x32_bf16 a[36:39], a[232:235], v[148:151], a[36:39]\n"
    "ds_read_b128 a[136:139], v226 offset:0\n"
    "v_add_f32 v196, v196, v74\n"
    "v_mfma_f32_16x16x32_bf16 a[40:43], a[232:235], v[152:155], a[40:43]\n"
    "v_exp_f32 v76, v76\n"
    "v_mfma_f32_16x16x32_bf16 a[44:47], a[232:235], v[156:159], a[44:47]\n"
    "ds_read_b64_tr_b16 a[204:205], v229 offset:49664\n"
    "v_cvt_pk_bf16_f32 v137, v74, v75\n"
    "v_mfma_f32_16x16x32_bf16 v[96:99], a[172:175], v[12:15], v[96:99]\n"
    "v_exp_f32 v77, v77\n"
    "v_mfma_f32_16x16x32_bf16 v[100:103], a[172:175], v[28:31], v[100:103]\n"
    "ds_read_b64_tr_b16 a[206:207], v229 offset:53760\n"
    "v_add_f32 v197, v197, v75\n"
    "v_mfma_f32_16x16x32_bf16 v[104:107], a[172:175], v[44:47], v[104:107]\n"
    "v_exp_f32 v78, v78\n"
    "v_mfma_f32_16x16x32_bf16 v[108:111], a[172:175], v[60:63], v[108:111]\n"
    "ds_read_b128 a[140:143], v227 offset:0\n"
    "v_add_f32 v198, v198, v76\n"
    "v_mfma_f32_16x16x32_bf16 a[48:51], a[236:239], v[144:147], a[48:51]\n"
    "v_exp_f32 v79, v79\n"
    "v_mfma_f32_16x16x32_bf16 a[52:55], a[236:239], v[148:151], a[52:55]\n"
    "buffer_load_dwordx4 v234, s[40:43], s68 offen offset:2048 lds\n"
    "v_cvt_pk_bf16_f32 v140, v76, v77\n"
    "v_mfma_f32_16x16x32_bf16 a[56:59], a[236:239], v[152:155], a[56:59]\n"
    "v_exp_f32 v80, v80\n"
    "v_mfma_f32_16x16x32_bf16 a[60:63], a[236:239], v[156:159], a[60:63]\n"
    "v_exp_f32 v81, v81\n"
    "v_mfma_f32_16x16x32_bf16 v[180:183], v[200:203], v[148:151], v[180:183]\n"
    "ds_read_b64_tr_b16 a[208:209], v228 offset:50176\n"
    "v_add_f32 v199, v199, v77\n"
    "v_mfma_f32_16x16x32_bf16 v[112:115], a[176:179], v[0:3], v[160:163]\n"
    "v_exp_f32 v82, v82\n"
    "v_mfma_f32_16x16x32_bf16 v[116:119], a[176:179], v[16:19], v[164:167]\n"
    "ds_read_b64_tr_b16 a[210:211], v228 offset:54272\n"
    "v_add_f32 v198, v198, v78\n"
    "v_mfma_f32_16x16x32_bf16 v[120:123], a[176:179], v[32:35], v[168:171]\n"
    "v_exp_f32 v83, v83\n"
    "v_mfma_f32_16x16x32_bf16 v[124:127], a[176:179], v[48:51], v[172:175]\n"
    "ds_read_b128 a[144:147], v224 offset:4096\n"
    "v_cvt_pk_bf16_f32 v141, v78, v79\n"
    "v_mfma_f32_16x16x32_bf16 a[64:67], a[240:243], v[144:147], a[64:67]\n"
    "v_exp_f32 v84, v84\n"
    "v_mfma_f32_16x16x32_bf16 a[68:71], a[240:243], v[148:151], a[68:71]\n"
    "ds_read_b64_tr_b16 a[212:213], v229 offset:50176\n"
    "v_add_f32 v199, v199, v79\n"
    "v_mfma_f32_16x16x32_bf16 a[72:75], a[240:243], v[152:155], a[72:75]\n"
    "v_exp_f32 v85, v85\n"
    "v_mfma_f32_16x16x32_bf16 a[76:79], a[240:243], v[156:159], a[76:79]\n"
    "ds_read_b64_tr_b16 a[214:215], v229 offset:54272\n"
    "v_cvt_pk_bf16_f32 v130, v80, v81\n"
    "v_mfma_f32_16x16x32_bf16 v[112:115], a[180:183], v[4:7], v[112:115]\n"
    "v_exp_f32 v86, v86\n"
    "v_mfma_f32_16x16x32_bf16 v[116:119], a[180:183], v[20:23], v[116:119]\n"
    "ds_read_b128 a[148:151], v225 offset:4096\n"
    "v_cvt_pk_bf16_f32 v131, v82, v83\n"
    "v_mfma_f32_16x16x32_bf16 v[120:123], a[180:183], v[36:39], v[120:123]\n"
    "v_exp_f32 v87, v87\n"
    "v_mfma_f32_16x16x32_bf16 v[124:127], a[180:183], v[52:55], v[124:127]\n"
    "buffer_load_dwordx4 v235, s[40:43], s68 offen offset:3072 lds\n"
    "v_cvt_pk_bf16_f32 v134, v84, v85\n"
    "v_mfma_f32_16x16x32_bf16 a[80:83], a[244:247], v[144:147], a[80:83]\n"
    "v_exp_f32 v88, v88\n"
    "v_mfma_f32_16x16x32_bf16 a[84:87], a[244:247], v[148:151], a[84:87]\n"
    "ds_read_b64_tr_b16 a[216:217], v228 offset:50688\n"
    "v_cvt_pk_bf16_f32 v135, v86, v87\n"
    "v_mfma_f32_16x16x32_bf16 a[88:91], a[244:247], v[152:155], a[88:91]\n"
    "v_exp_f32 v89, v89\n"
    "v_mfma_f32_16x16x32_bf16 a[92:95], a[244:247], v[156:159], a[92:95]\n"
    "ds_read_b64_tr_b16 a[218:219], v228 offset:54784\n"
    "v_add_f32 v196, v196, v88\n"
    "v_mfma_f32_16x16x32_bf16 v[112:115], a[184:187], v[8:11], v[112:115]\n"
    "v_exp_f32 v90, v90\n"
    "v_mfma_f32_16x16x32_bf16 v[116:119], a[184:187], v[24:27], v[116:119]\n"
    "ds_read_b128 a[152:155], v226 offset:4096\n"
    "v_cvt_pk_bf16_f32 v138, v88, v89\n"
    "v_mfma_f32_16x16x32_bf16 v[120:123], a[184:187], v[40:43], v[120:123]\n"
    "v_exp_f32 v91, v91\n"
    "v_mfma_f32_16x16x32_bf16 v[124:127], a[184:187], v[56:59], v[124:127]\n"
    "ds_read_b64_tr_b16 a[220:221], v229 offset:50688\n"
    "v_add_f32 v197, v197, v89\n"
    "v_mfma_f32_16x16x32_bf16 a[96:99], a[248:251], v[144:147], a[96:99]\n"
    "v_exp_f32 v92, v92\n"
    "v_mfma_f32_16x16x32_bf16 a[100:103], a[248:251], v[148:151], a[100:103]\n"
    "ds_read_b64_tr_b16 a[222:223], v229 offset:54784\n"
    "v_add_f32 v196, v196, v90\n"
    "v_mfma_f32_16x16x32_bf16 a[104:107], a[248:251], v[152:155], a[104:107]\n"
    "v_exp_f32 v93, v93\n"
    "v_mfma_f32_16x16x32_bf16 a[108:111], a[248:251], v[156:159], a[108:111]\n"
    "ds_read_b128 a[156:159], v227 offset:4096\n"
    "v_cvt_pk_bf16_f32 v139, v90, v91\n"
    "v_mfma_f32_16x16x32_bf16 v[112:115], a[188:191], v[12:15], v[112:115]\n"
    "v_exp_f32 v94, v94\n"
    "v_mfma_f32_16x16x32_bf16 v[116:119], a[188:191], v[28:31], v[116:119]\n"
    "v_add_f32 v197, v197, v91\n"
    "v_add_f32 v198, v198, v92\n"
    "v_mfma_f32_16x16x32_bf16 v[120:123], a[188:191], v[44:47], v[120:123]\n"
    "v_exp_f32 v95, v95\n"
    "v_mfma_f32_16x16x32_bf16 v[124:127], a[188:191], v[60:63], v[124:127]\n"
    "v_cvt_pk_bf16_f32 v142, v92, v93\n"
    "v_add_f32 v199, v199, v93\n"
    "v_mfma_f32_16x16x32_bf16 a[112:115], a[252:255], v[144:147], a[112:115]\n"
    "v_add_f32 v198, v198, v94\n"
    "v_cvt_pk_bf16_f32 v143, v94, v95\n"
    "v_mfma_f32_16x16x32_bf16 a[116:119], a[252:255], v[148:151], a[116:119]\n"
    "v_add_f32 v199, v199, v95\n"
    "v_mfma_f32_16x16x32_bf16 a[120:123], a[252:255], v[152:155], a[120:123]\n"
    "v_mfma_f32_16x16x32_bf16 a[124:127], a[252:255], v[156:159], a[124:127]\n"
    "s_waitcnt lgkmcnt(0)\n"
    "v_mfma_f32_16x16x32_bf16 v[64:67], a[128:131], v[0:3], v[160:163]\n"
    "s_add_u32 m0, s62, 32768\n"
    "v_exp_f32 v96, v96\n"
    "v_mfma_f32_16x16x32_bf16 v[68:71], a[128:131], v[16:19], v[164:167]\n"
    "v_exp_f32 v97, v97\n"
    "v_mfma_f32_16x16x32_bf16 v[72:75], a[128:131], v[32:35], v[168:171]\n"
    "buffer_load_dwordx4 v236, s[44:47], s60 offen lds\n"
    "v_cvt_pk_bf16_f32 v144, v96, v97\n"
    "v_mfma_f32_16x16x32_bf16 v[76:79], a[128:131], v[48:51], v[172:175]\n"
    "v_exp_f32 v98, v98\n"
    "v_mfma_f32_16x16x32_bf16 a[0:3], a[192:195], v[128:131], a[0:3]\n"
    "ds_read_b64_tr_b16 a[224:225], v228 offset:57344\n"
    "v_mfma_f32_16x16x32_bf16 a[4:7], a[192:195], v[132:135], a[4:7]\n"
    "v_exp_f32 v99, v99\n"
    "v_mfma_f32_16x16x32_bf16 a[8:11], a[192:195], v[136:139], a[8:11]\n"
    "ds_read_b64_tr_b16 a[226:227], v228 offset:61440\n"
    "v_cvt_pk_bf16_f32 v145, v98, v99\n"
    "v_mfma_f32_16x16x32_bf16 a[12:15], a[192:195], v[140:143], a[12:15]\n"
    "v_exp_f32 v100, v100\n"
    "v_mfma_f32_16x16x32_bf16 v[64:67], a[132:135], v[4:7], v[64:67]\n"
    "ds_read_b128 a[160:163], v224 offset:8192\n"
    "v_mfma_f32_16x16x32_bf16 v[68:71], a[132:135], v[20:23], v[68:71]\n"
    "v_exp_f32 v101, v101\n"
    "v_mfma_f32_16x16x32_bf16 v[72:75], a[132:135], v[36:39], v[72:75]\n"
    "ds_read_b64_tr_b16 a[228:229], v229 offset:57344\n"
    "v_cvt_pk_bf16_f32 v148, v100, v101\n"
    "v_mfma_f32_16x16x32_bf16 v[76:79], a[132:135], v[52:55], v[76:79]\n"
    "v_exp_f32 v102, v102\n"
    "v_mfma_f32_16x16x32_bf16 a[16:19], a[196:199], v[128:131], a[16:19]\n"
    "ds_read_b64_tr_b16 a[230:231], v229 offset:61440\n"
    "v_mfma_f32_16x16x32_bf16 a[20:23], a[196:199], v[132:135], a[20:23]\n"
    "v_exp_f32 v103, v103\n"
    "v_mfma_f32_16x16x32_bf16 a[24:27], a[196:199], v[136:139], a[24:27]\n"
    "ds_read_b128 a[164:167], v225 offset:8192\n"
    "v_cvt_pk_bf16_f32 v149, v102, v103\n"
    "v_mfma_f32_16x16x32_bf16 a[28:31], a[196:199], v[140:143], a[28:31]\n"
    "s_add_u32 m0, s62, 33664\n"
    "v_exp_f32 v104, v104\n"
    "v_mfma_f32_16x16x32_bf16 v[176:179], v[200:203], v[128:131], v[176:179]\n"
    "buffer_load_dwordx4 v236, s[44:47], s60 offen offset:128 lds\n"
    "v_add_f32 v196, v196, v104\n"
    "v_mfma_f32_16x16x32_bf16 v[64:67], a[136:139], v[8:11], v[64:67]\n"
    "v_exp_f32 v105, v105\n"
    "v_mfma_f32_16x16x32_bf16 v[68:71], a[136:139], v[24:27], v[68:71]\n"
    "ds_read_b64_tr_b16 a[232:233], v228 offset:57856\n"
    "v_cvt_pk_bf16_f32 v152, v104, v105\n"
    "v_mfma_f32_16x16x32_bf16 v[72:75], a[136:139], v[40:43], v[72:75]\n"
    "v_exp_f32 v106, v106\n"
    "v_mfma_f32_16x16x32_bf16 v[76:79], a[136:139], v[56:59], v[76:79]\n"
    "ds_read_b64_tr_b16 a[234:235], v228 offset:61952\n"
    "v_add_f32 v197, v197, v105\n"
    "v_mfma_f32_16x16x32_bf16 a[32:35], a[200:203], v[128:131], a[32:35]\n"
    "v_exp_f32 v107, v107\n"
    "v_mfma_f32_16x16x32_bf16 a[36:39], a[200:203], v[132:135], a[36:39]\n"
    "ds_read_b128 a[168:171], v226 offset:8192\n"
    "v_add_f32 v196, v196, v106\n"
    "v_mfma_f32_16x16x32_bf16 a[40:43], a[200:203], v[136:139], a[40:43]\n"
    "v_exp_f32 v108, v108\n"
    "v_mfma_f32_16x16x32_bf16 a[44:47], a[200:203], v[140:143], a[44:47]\n"
    "ds_read_b64_tr_b16 a[236:237], v229 offset:57856\n"
    "v_cvt_pk_bf16_f32 v153, v106, v107\n"
    "v_mfma_f32_16x16x32_bf16 v[64:67], a[140:143], v[12:15], v[64:67]\n"
    "v_exp_f32 v109, v109\n"
    "v_mfma_f32_16x16x32_bf16 v[68:71], a[140:143], v[28:31], v[68:71]\n"
    "ds_read_b64_tr_b16 a[238:239], v229 offset:61952\n"
    "v_add_f32 v197, v197, v107\n"
    "v_mfma_f32_16x16x32_bf16 v[72:75], a[140:143], v[44:47], v[72:75]\n"
    "v_exp_f32 v110, v110\n"
    "v_mfma_f32_16x16x32_bf16 v[76:79], a[140:143], v[60:63], v[76:79]\n"
    "ds_read_b128 a[172:175], v227 offset:8192\n"
    "v_add_f32 v198, v198, v108\n"
    "v_mfma_f32_16x16x32_bf16 a[48:51], a[204:207], v[128:131], a[48:51]\n"
    "s_add_u32 m0, s62, 32768\n"
    "v_exp_f32 v111, v111\n"
    "v_mfma_f32_16x16x32_bf16 a[52:55], a[204:207], v[132:135], a[52:55]\n"
    "buffer_load_dwordx4 v236, s[44:47], s60 offen offset:2048 lds\n"
    "v_cvt_pk_bf16_f32 v156, v108, v109\n"
    "v_mfma_f32_16x16x32_bf16 a[56:59], a[204:207], v[136:139], a[56:59]\n"
    "v_exp_f32 v112, v112\n"
    "v_mfma_f32_16x16x32_bf16 a[60:63], a[204:207], v[140:143], a[60:63]\n"
    "v_exp_f32 v113, v113\n"
    "v_mfma_f32_16x16x32_bf16 v[180:183], v[200:203], v[132:135], v[180:183]\n"
    "ds_read_b64_tr_b16 a[240:241], v228 offset:58368\n"
    "v_add_f32 v199, v199, v109\n"
    "v_mfma_f32_16x16x32_bf16 v[80:83], a[144:147], v[0:3], v[160:163]\n"
    "v_exp_f32 v114, v114\n"
    "v_mfma_f32_16x16x32_bf16 v[84:87], a[144:147], v[16:19], v[164:167]\n"
    "ds_read_b64_tr_b16 a[242:243], v228 offset:62464\n"
    "v_add_f32 v198, v198, v110\n"
    "v_mfma_f32_16x16x32_bf16 v[88:91], a[144:147], v[32:35], v[168:171]\n"
    "v_exp_f32 v115, v115\n"
    "v_mfma_f32_16x16x32_bf16 v[92:95], a[144:147], v[48:51], v[172:175]\n"
    "ds_read_b128 a[176:179], v224 offset:12288\n"
    "v_cvt_pk_bf16_f32 v157, v110, v111\n"
    "v_mfma_f32_16x16x32_bf16 a[64:67], a[208:211], v[128:131], a[64:67]\n"
    "v_exp_f32 v116, v116\n"
    "v_mfma_f32_16x16x32_bf16 a[68:71], a[208:211], v[132:135], a[68:71]\n"
    "ds_read_b64_tr_b16 a[244:245], v229 offset:58368\n"
    "v_add_f32 v199, v199, v111\n"
    "v_mfma_f32_16x16x32_bf16 a[72:75], a[208:211], v[136:139], a[72:75]\n"
    "v_exp_f32 v117, v117\n"
    "v_mfma_f32_16x16x32_bf16 a[76:79], a[208:211], v[140:143], a[76:79]\n"
    "ds_read_b64_tr_b16 a[246:247], v229 offset:62464\n"
    "v_cvt_pk_bf16_f32 v146, v112, v113\n"
    "v_mfma_f32_16x16x32_bf16 v[80:83], a[148:151], v[4:7], v[80:83]\n"
    "v_exp_f32 v118, v118\n"
    "v_mfma_f32_16x16x32_bf16 v[84:87], a[148:151], v[20:23], v[84:87]\n"
    "ds_read_b128 a[180:183], v225 offset:12288\n"
    "v_cvt_pk_bf16_f32 v147, v114, v115\n"
    "v_mfma_f32_16x16x32_bf16 v[88:91], a[148:151], v[36:39], v[88:91]\n"
    "s_add_u32 m0, s62, 33664\n"
    "v_exp_f32 v119, v119\n"
    "v_mfma_f32_16x16x32_bf16 v[92:95], a[148:151], v[52:55], v[92:95]\n"
    "buffer_load_dwordx4 v236, s[44:47], s60 offen offset:2176 lds\n"
    "v_cvt_pk_bf16_f32 v150, v116, v117\n"
    "v_mfma_f32_16x16x32_bf16 a[80:83], a[212:215], v[128:131], a[80:83]\n"
    "v_exp_f32 v120, v120\n"
    "v_mfma_f32_16x16x32_bf16 a[84:87], a[212:215], v[132:135], a[84:87]\n"
    "ds_read_b64_tr_b16 a[248:249], v228 offset:58880\n"
    "v_cvt_pk_bf16_f32 v151, v118, v119\n"
    "v_mfma_f32_16x16x32_bf16 a[88:91], a[212:215], v[136:139], a[88:91]\n"
    "v_exp_f32 v121, v121\n"
    "v_mfma_f32_16x16x32_bf16 a[92:95], a[212:215], v[140:143], a[92:95]\n"
    "ds_read_b64_tr_b16 a[250:251], v228 offset:62976\n"
    "v_add_f32 v196, v196, v120\n"
    "v_mfma_f32_16x16x32_bf16 v[80:83], a[152:155], v[8:11], v[80:83]\n"
    "v_exp_f32 v122, v122\n"
    "v_mfma_f32_16x16x32_bf16 v[84:87], a[152:155], v[24:27], v[84:87]\n"
    "ds_read_b128 a[184:187], v226 offset:12288\n"
    "v_cvt_pk_bf16_f32 v154, v120, v121\n"
    "v_mfma_f32_16x16x32_bf16 v[88:91], a[152:155], v[40:43], v[88:91]\n"
    "v_exp_f32 v123, v123\n"
    "v_mfma_f32_16x16x32_bf16 v[92:95], a[152:155], v[56:59], v[92:95]\n"
    "ds_read_b64_tr_b16 a[252:253], v229 offset:58880\n"
    "v_add_f32 v197, v197, v121\n"
    "v_mfma_f32_16x16x32_bf16 a[96:99], a[216:219], v[128:131], a[96:99]\n"
    "v_exp_f32 v124, v124\n"
    "v_mfma_f32_16x16x32_bf16 a[100:103], a[216:219], v[132:135], a[100:103]\n"
    "ds_read_b64_tr_b16 a[254:255], v229 offset:62976\n"
    "v_add_f32 v196, v196, v122\n"
    "v_mfma_f32_16x16x32_bf16 a[104:107], a[216:219], v[136:139], a[104:107]\n"
    "v_exp_f32 v125, v125\n"
    "v_mfma_f32_16x16x32_bf16 a[108:111], a[216:219], v[140:143], a[108:111]\n"
    "ds_read_b128 a[188:191], v227 offset:12288\n"
    "v_cvt_pk_bf16_f32 v155, v122, v123\n"
    "v_mfma_f32_16x16x32_bf16 v[80:83], a[156:159], v[12:15], v[80:83]\n"
    "v_exp_f32 v126, v126\n"
    "v_mfma_f32_16x16x32_bf16 v[84:87], a[156:159], v[28:31], v[84:87]\n"
    "v_add_f32 v197, v197, v123\n"
    "v_add_f32 v198, v198, v124\n"
    "v_mfma_f32_16x16x32_bf16 v[88:91], a[156:159], v[44:47], v[88:91]\n"
    "v_exp_f32 v127, v127\n"
    "v_mfma_f32_16x16x32_bf16 v[92:95], a[156:159], v[60:63], v[92:95]\n"
    "v_cvt_pk_bf16_f32 v158, v124, v125\n"
    "v_add_f32 v199, v199, v125\n"
    "v_mfma_f32_16x16x32_bf16 a[112:115], a[220:223], v[128:131], a[112:115]\n"
    "v_add_f32 v198, v198, v126\n"
    "v_cvt_pk_bf16_f32 v159, v126, v127\n"
    "v_mfma_f32_16x16x32_bf16 a[116:119], a[220:223], v[132:135], a[116:119]\n"
    "v_add_f32 v199, v199, v127\n"
    "v_mfma_f32_16x16x32_bf16 a[120:123], a[220:223], v[136:139], a[120:123]\n"
    "v_mfma_f32_16x16x32_bf16 a[124:127], a[220:223], v[140:143], a[124:127]\n"
    "s_add_u32 s60, s60, 16384\n"
    "s_waitcnt vmcnt(16) lgkmcnt(0)\n"
    "s_barrier\n"
    "s_add_u32 s68, s60, 16384\n"
    "s_min_u32 s68, s68, s69\n"
    "v_mfma_f32_16x16x32_bf16 v[96:99], a[160:163], v[0:3], v[160:163]\n"
    "s_add_u32 m0, s61, 0\n"
    "v_exp_f32 v64, v64\n"
    "v_mfma_f32_16x16x32_bf16 v[100:103], a[160:163], v[16:19], v[164:167]\n"
    "v_exp_f32 v65, v65\n"
    "v_mfma_f32_16x16x32_bf16 v[104:107], a[160:163], v[32:35], v[168:171]\n"
    "buffer_load_dwordx4 v232, s[40:43], s68 offen lds\n"
    "v_cvt_pk_bf16_f32 v128, v64, v65\n"
    "v_mfma_f32_16x16x32_bf16 v[108:111], a[160:163], v[48:51], v[172:175]\n"
    "v_exp_f32 v66, v66\n"
    "v_mfma_f32_16x16x32_bf16 a[0:3], a[224:227], v[144:147], a[0:3]\n"
    "ds_read_b64_tr_b16 a[192:193], v228 offset:0\n"
    "v_mfma_f32_16x16x32_bf16 a[4:7], a[224:227], v[148:151], a[4:7]\n"
    "v_exp_f32 v67, v67\n"
    "v_mfma_f32_16x16x32_bf16 a[8:11], a[224:227], v[152:155], a[8:11]\n"
    "ds_read_b64_tr_b16 a[194:195], v228 offset:4096\n"
    "v_cvt_pk_bf16_f32 v129, v66, v67\n"
    "v_mfma_f32_16x16x32_bf16 a[12:15], a[224:227], v[156:159], a[12:15]\n"
    "v_exp_f32 v68, v68\n"
    "v_mfma_f32_16x16x32_bf16 v[96:99], a[164:167], v[4:7], v[96:99]\n"
    "ds_read_b128 a[128:131], v224 offset:16384\n"
    "v_mfma_f32_16x16x32_bf16 v[100:103], a[164:167], v[20:23], v[100:103]\n"
    "v_exp_f32 v69, v69\n"
    "v_mfma_f32_16x16x32_bf16 v[104:107], a[164:167], v[36:39], v[104:107]\n"
    "ds_read_b64_tr_b16 a[196:197], v229 offset:0\n"
    "v_cvt_pk_bf16_f32 v132, v68, v69\n"
    "v_mfma_f32_16x16x32_bf16 v[108:111], a[164:167], v[52:55], v[108:111]\n"
    "v_exp_f32 v70, v70\n"
    "v_mfma_f32_16x16x32_bf16 a[16:19], a[228:231], v[144:147], a[16:19]\n"
    "ds_read_b64_tr_b16 a[198:199], v229 offset:4096\n"
    "v_mfma_f32_16x16x32_bf16 a[20:23], a[228:231], v[148:151], a[20:23]\n"
    "v_exp_f32 v71, v71\n"
    "v_mfma_f32_16x16x32_bf16 a[24:27], a[228:231], v[152:155], a[24:27]\n"
    "ds_read_b128 a[132:135], v225 offset:16384\n"
    "v_cvt_pk_bf16_f32 v133, v70, v71\n"
    "v_mfma_f32_16x16x32_bf16 a[28:31], a[228:231], v[156:159], a[28:31]\n"
    "v_exp_f32 v72, v72\n"
    "v_mfma_f32_16x16x32_bf16 v[176:179], v[200:203], v[144:147], v[176:179]\n"
    "buffer_load_dwordx4 v233, s[40:43], s68 offen offset:1024 lds\n"
    "v_add_f32 v196, v196, v72\n"
    "v_mfma_f32_16x16x32_bf16 v[96:99], a[168:171], v[8:11], v[96:99]\n"
    "v_exp_f32 v73, v73\n"
    "v_mfma_f32_16x16x32_bf16 v[100:103], a[168:171], v[24:27], v[100:103]\n"
    "ds_read_b64_tr_b16 a[200:201], v228 offset:512\n"
    "v_cvt_pk_bf16_f32 v136, v72, v73\n"
    "v_mfma_f32_16x16x32_bf16 v[104:107], a[168:171], v[40:43], v[104:107]\n"
    "v_exp_f32 v74, v74\n"
    "v_mfma_f32_16x16x32_bf16 v[108:111], a[168:171], v[56:59], v[108:111]\n"
    "ds_read_b64_tr_b16 a[202:203], v228 offset:4608\n"
    "v_add_f32 v197, v197, v73\n"
    "v_mfma_f32_16x16x32_bf16 a[32:35], a[232:235], v[144:147], a[32:35]\n"
    "v_exp_f32 v75, v75\n"
    "v_mfma_f32_16x16x32_bf16 a[36:39], a[232:235], v[148:151], a[36:39]\n"
    "ds_read_b128 a[136:139], v226 offset:16384\n"
    "v_add_f32 v196, v196, v74\n"
    "v_mfma_f32_16x16x32_bf16 a[40:43], a[232:235], v[152:155], a[40:43]\n"
    "v_exp_f32 v76, v76\n"
    "v_mfma_f32_16x16x32_bf16 a[44:47], a[232:235], v[156:159], a[44:47]\n"
    "ds_read_b64_tr_b16 a[204:205], v229 offset:512\n"
    "v_cvt_pk_bf16_f32 v137, v74, v75\n"
    "v_mfma_f32_16x16x32_bf16 v[96:99], a[172:175], v[12:15], v[96:99]\n"
    "v_exp_f32 v77, v77\n"
    "v_mfma_f32_16x16x32_bf16 v[100:103], a[172:175], v[28:31], v[100:103]\n"
    "ds_read_b64_tr_b16 a[206:207], v229 offset:4608\n"
    "v_add_f32 v197, v197, v75\n"
    "v_mfma_f32_16x16x32_bf16 v[104:107], a[172:175], v[44:47], v[104:107]\n"
    "v_exp_f32 v78, v78\n"
    "v_mfma_f32_16x16x32_bf16 v[108:111], a[172:175], v[60:63], v[108:111]\n"
    "ds_read_b128 a[140:143], v227 offset:16384\n"
    "v_add_f32 v198, v198, v76\n"
    "v_mfma_f32_16x16x32_bf16 a[48:51], a[236:239], v[144:147], a[48:51]\n"
    "v_exp_f32 v79, v79\n"
    "v_mfma_f32_16x16x32_bf16 a[52:55], a[236:239], v[148:151], a[52:55]\n"
    "buffer_load_dwordx4 v234, s[40:43], s68 offen offset:2048 lds\n"
    "v_cvt_pk_bf16_f32 v140, v76, v77\n"
    "v_mfma_f32_16x16x32_bf16 a[56:59], a[236:239], v[152:155], a[56:59]\n"
    "v_exp_f32 v80, v80\n"
    "v_mfma_f32_16x16x32_bf16 a[60:63], a[236:239], v[156:159], a[60:63]\n"
    "v_exp_f32 v81, v81\n"
    "v_mfma_f32_16x16x32_bf16 v[180:183], v[200:203], v[148:151], v[180:183]\n"
    "ds_read_b64_tr_b16 a[208:209], v228 offset:1024\n"
    "v_add_f32 v199, v199, v77\n"
    "v_mfma_f32_16x16x32_bf16 v[112:115], a[176:179], v[0:3], v[160:163]\n"
    "v_exp_f32 v82, v82\n"
    "v_mfma_f32_16x16x32_bf16 v[116:119], a[176:179], v[16:19], v[164:167]\n"
    "ds_read_b64_tr_b16 a[210:211], v228 offset:5120\n"
    "v_add_f32 v198, v198, v78\n"
    "v_mfma_f32_16x16x32_bf16 v[120:123], a[176:179], v[32:35], v[168:171]\n"
    "v_exp_f32 v83, v83\n"
    "v_mfma_f32_16x16x32_bf16 v[124:127], a[176:179], v[48:51], v[172:175]\n"
    "ds_read_b128 a[144:147], v224 offset:20480\n"
    "v_cvt_pk_bf16_f32 v141, v78, v79\n"
    "v_mfma_f32_16x16x32_bf16 a[64:67], a[240:243], v[144:147], a[64:67]\n"
    "v_exp_f32 v84, v84\n"
    "v_mfma_f32_16x16x32_bf16 a[68:71], a[240:243], v[148:151], a[68:71]\n"
    "ds_read_b64_tr_b16 a[212:213], v229 offset:1024\n"
    "v_add_f32 v199, v199, v79\n"
    "v_mfma_f32_16x16x32_bf16 a[72:75], a[240:243], v[152:155], a[72:75]\n"
    "v_exp_f32 v85, v85\n"
    "v_mfma_f32_16x16x32_bf16 a[76:79], a[240:243], v[156:159], a[76:79]\n"
    "ds_read_b64_tr_b16 a[214:215], v229 offset:5120\n"
    "v_cvt_pk_bf16_f32 v130, v80, v81\n"
    "v_mfma_f32_16x16x32_bf16 v[112:115], a[180:183], v[4:7], v[112:115]\n"
    "v_exp_f32 v86, v86\n"
    "v_mfma_f32_16x16x32_bf16 v[116:119], a[180:183], v[20:23], v[116:119]\n"
    "ds_read_b128 a[148:151], v225 offset:20480\n"
    "v_cvt_pk_bf16_f32 v131, v82, v83\n"
    "v_mfma_f32_16x16x32_bf16 v[120:123], a[180:183], v[36:39], v[120:123]\n"
    "v_exp_f32 v87, v87\n"
    "v_mfma_f32_16x16x32_bf16 v[124:127], a[180:183], v[52:55], v[124:127]\n"
    "buffer_load_dwordx4 v235, s[40:43], s68 offen offset:3072 lds\n"
    "v_cvt_pk_bf16_f32 v134, v84, v85\n"
    "v_mfma_f32_16x16x32_bf16 a[80:83], a[244:247], v[144:147], a[80:83]\n"
    "v_exp_f32 v88, v88\n"
    "v_mfma_f32_16x16x32_bf16 a[84:87], a[244:247], v[148:151], a[84:87]\n"
    "ds_read_b64_tr_b16 a[216:217], v228 offset:1536\n"
    "v_cvt_pk_bf16_f32 v135, v86, v87\n"
    "v_mfma_f32_16x16x32_bf16 a[88:91], a[244:247], v[152:155], a[88:91]\n"
    "v_exp_f32 v89, v89\n"
    "v_mfma_f32_16x16x32_bf16 a[92:95], a[244:247], v[156:159], a[92:95]\n"
    "ds_read_b64_tr_b16 a[218:219], v228 offset:5632\n"
    "v_add_f32 v196, v196, v88\n"
    "v_mfma_f32_16x16x32_bf16 v[112:115], a[184:187], v[8:11], v[112:115]\n"
    "v_exp_f32 v90, v90\n"
    "v_mfma_f32_16x16x32_bf16 v[116:119], a[184:187], v[24:27], v[116:119]\n"
    "ds_read_b128 a[152:155], v226 offset:20480\n"
    "v_cvt_pk_bf16_f32 v138, v88, v89\n"
    "v_mfma_f32_16x16x32_bf16 v[120:123], a[184:187], v[40:43], v[120:123]\n"
    "v_exp_f32 v91, v91\n"
    "v_mfma_f32_16x16x32_bf16 v[124:127], a[184:187], v[56:59], v[124:127]\n"
    "ds_read_b64_tr_b16 a[220:221], v229 offset:1536\n"
    "v_add_f32 v197, v197, v89\n"
    "v_mfma_f32_16x16x32_bf16 a[96:99], a[248:251], v[144:147], a[96:99]\n"
    "v_exp_f32 v92, v92\n"
    "v_mfma_f32_16x16x32_bf16 a[100:103], a[248:251], v[148:151], a[100:103]\n"
    "ds_read_b64_tr_b16 a[222:223], v229 offset:5632\n"
    "v_add_f32 v196, v196, v90\n"
    "v_mfma_f32_16x16x32_bf16 a[104:107], a[248:251], v[152:155], a[104:107]\n"
    "v_exp_f32 v93, v93\n"
    "v_mfma_f32_16x16x32_bf16 a[108:111], a[248:251], v[156:159], a[108:111]\n"
    "ds_read_b128 a[156:159], v227 offset:20480\n"
    "v_cvt_pk_bf16_f32 v139, v90, v91\n"
    "v_mfma_f32_16x16x32_bf16 v[112:115], a[188:191], v[12:15], v[112:115]\n"
    "v_exp_f32 v94, v94\n"
    "v_mfma_f32_16x16x32_bf16 v[116:119], a[188:191], v[28:31], v[116:119]\n"
    "v_add_f32 v197, v197, v91\n"
    "v_add_f32 v198, v198, v92\n"
    "v_mfma_f32_16x16x32_bf16 v[120:123], a[188:191], v[44:47], v[120:123]\n"
    "v_exp_f32 v95, v95\n"
    "v_mfma_f32_16x16x32_bf16 v[124:127], a[188:191], v[60:63], v[124:127]\n"
    "v_cvt_pk_bf16_f32 v142, v92, v93\n"
    "v_add_f32 v199, v199, v93\n"
    "v_mfma_f32_16x16x32_bf16 a[112:115], a[252:255], v[144:147], a[112:115]\n"
    "v_add_f32 v198, v198, v94\n"
    "v_cvt_pk_bf16_f32 v143, v94, v95\n"
    "v_mfma_f32_16x16x32_bf16 a[116:119], a[252:255], v[148:151], a[116:119]\n"
    "v_add_f32 v199, v199, v95\n"
    "v_mfma_f32_16x16x32_bf16 a[120:123], a[252:255], v[152:155], a[120:123]\n"
    "v_mfma_f32_16x16x32_bf16 a[124:127], a[252:255], v[156:159], a[124:127]\n"
    "s_waitcnt lgkmcnt(0)\n"
    "v_mfma_f32_16x16x32_bf16 v[64:67], a[128:131], v[0:3], v[160:163]\n"
    "s_add_u32 m0, s62, 49152\n"
    "v_exp_f32 v96, v96\n"
    "v_mfma_f32_16x16x32_bf16 v[68:71], a[128:131], v[16:19], v[164:167]\n"
    "v_exp_f32 v97, v97\n"
    "v_mfma_f32_16x16x32_bf16 v[72:75], a[128:131], v[32:35], v[168:171]\n"
    "buffer_load_dwordx4 v236, s[44:47], s60 offen lds\n"
    "v_cvt_pk_bf16_f32 v144, v96, v97\n"
    "v_mfma_f32_16x16x32_bf16 v[76:79], a[128:131], v[48:51], v[172:175]\n"
    "v_exp_f32 v98, v98\n"
    "v_mfma_f32_16x16x32_bf16 a[0:3], a[192:195], v[128:131], a[0:3]\n"
    "ds_read_b64_tr_b16 a[224:225], v228 offset:8192\n"
    "v_mfma_f32_16x16x32_bf16 a[4:7], a[192:195], v[132:135], a[4:7]\n"
    "v_exp_f32 v99, v99\n"
    "v_mfma_f32_16x16x32_bf16 a[8:11], a[192:195], v[136:139], a[8:11]\n"
    "ds_read_b64_tr_b16 a[226:227], v228 offset:12288\n"
    "v_cvt_pk_bf16_f32 v145, v98, v99\n"
    "v_mfma_f32_16x16x32_bf16 a[12:15], a[192:195], v[140:143], a[12:15]\n"
    "v_exp_f32 v100, v100\n"
    "v_mfma_f32_16x16x32_bf16 v[64:67], a[132:135], v[4:7], v[64:67]\n"
    "ds_read_b128 a[160:163], v224 offset:24576\n"
    "v_mfma_f32_16x16x32_bf16 v[68:71], a[132:135], v[20:23], v[68:71]\n"
    "v_exp_f32 v101, v101\n"
    "v_mfma_f32_16x16x32_bf16 v[72:75], a[132:135], v[36:39], v[72:75]\n"
    "ds_read_b64_tr_b16 a[228:229], v229 offset:8192\n"
    "v_cvt_pk_bf16_f32 v148, v100, v101\n"
    "v_mfma_f32_16x16x32_bf16 v[76:79], a[132:135], v[52:55], v[76:79]\n"
    "v_exp_f32 v102, v102\n"
    "v_mfma_f32_16x16x32_bf16 a[16:19], a[196:199], v[128:131], a[16:19]\n"
    "ds_read_b64_tr_b16 a[230:231], v229 offset:12288\n"
    "v_mfma_f32_16x16x32_bf16 a[20:23], a[196:199], v[132:135], a[20:23]\n"
    "v_exp_f32 v103, v103\n"
    "v_mfma_f32_16x16x32_bf16 a[24:27], a[196:199], v[136:139], a[24:27]\n"
    "ds_read_b128 a[164:167], v225 offset:24576\n"
    "v_cvt_pk_bf16_f32 v149, v102, v103\n"
    "v_mfma_f32_16x16x32_bf16 a[28:31], a[196:199], v[140:143], a[28:31]\n"
    "s_add_u32 m0, s62, 50048\n"
    "v_exp_f32 v104, v104\n"
    "v_mfma_f32_16x16x32_bf16 v[176:179], v[200:203], v[128:131], v[176:179]\n"
    "buffer_load_dwordx4 v236, s[44:47], s60 offen offset:128 lds\n"
    "v_add_f32 v196, v196, v104\n"
    "v_mfma_f32_16x16x32_bf16 v[64:67], a[136:139], v[8:11], v[64:67]\n"
    "v_exp_f32 v105, v105\n"
    "v_mfma_f32_16x16x32_bf16 v[68:71], a[136:139], v[24:27], v[68:71]\n"
    "ds_read_b64_tr_b16 a[232:233], v228 offset:8704\n"
    "v_cvt_pk_bf16_f32 v152, v104, v105\n"
    "v_mfma_f32_16x16x32_bf16 v[72:75], a[136:139], v[40:43], v[72:75]\n"
    "v_exp_f32 v106, v106\n"
    "v_mfma_f32_16x16x32_bf16 v[76:79], a[136:139], v[56:59], v[76:79]\n"
    "ds_read_b64_tr_b16 a[234:235], v228 offset:12800\n"
    "v_add_f32 v197, v197, v105\n"
    "v_mfma_f32_16x16x32_bf16 a[32:35], a[200:203], v[128:131], a[32:35]\n"
    "v_exp_f32 v107, v107\n"
    "v_mfma_f32_16x16x32_bf16 a[36:39], a[200:203], v[132:135], a[36:39]\n"
    "ds_read_b128 a[168:171], v226 offset:24576\n"
    "v_add_f32 v196, v196, v106\n"
    "v_mfma_f32_16x16x32_bf16 a[40:43], a[200:203], v[136:139], a[40:43]\n"
    "v_exp_f32 v108, v108\n"
    "v_mfma_f32_16x16x32_bf16 a[44:47], a[200:203], v[140:143], a[44:47]\n"
    "ds_read_b64_tr_b16 a[236:237], v229 offset:8704\n"
    "v_cvt_pk_bf16_f32 v153, v106, v107\n"
    "v_mfma_f32_16x16x32_bf16 v[64:67], a[140:143], v[12:15], v[64:67]\n"
    "v_exp_f32 v109, v109\n"
    "v_mfma_f32_16x16x32_bf16 v[68:71], a[140:143], v[28:31], v[68:71]\n"
    "ds_read_b64_tr_b16 a[238:239], v229 offset:12800\n"
    "v_add_f32 v197, v197, v107\n"
    "v_mfma_f32_16x16x32_bf16 v[72:75], a[140:143], v[44:47], v[72:75]\n"
    "v_exp_f32 v110, v110\n"
    "v_mfma_f32_16x16x32_bf16 v[76:79], a[140:143], v[60:63], v[76:79]\n"
    "ds_read_b128 a[172:175], v227 offset:24576\n"
    "v_add_f32 v198, v198, v108\n"
    "v_mfma_f32_16x16x32_bf16 a[48:51], a[204:207], v[128:131], a[48:51]\n"
    "s_add_u32 m0, s62, 49152\n"
    "v_exp_f32 v111, v111\n"
    "v_mfma_f32_16x16x32_bf16 a[52:55], a[204:207], v[132:135], a[52:55]\n"
    "buffer_load_dwordx4 v236, s[44:47], s60 offen offset:2048 lds\n"
    "v_cvt_pk_bf16_f32 v156, v108, v109\n"
    "v_mfma_f32_16x16x32_bf16 a[56:59], a[204:207], v[136:139], a[56:59]\n"
    "v_exp_f32 v112, v112\n"
    "v_mfma_f32_16x16x32_bf16 a[60:63], a[204:207], v[140:143], a[60:63]\n"
    "v_exp_f32 v113, v113\n"
    "v_mfma_f32_16x16x32_bf16 v[180:183], v[200:203], v[132:135], v[180:183]\n"
    "ds_read_b64_tr_b16 a[240:241], v228 offset:9216\n"
    "v_add_f32 v199, v199, v109\n"
    "v_mfma_f32_16x16x32_bf16 v[80:83], a[144:147], v[0:3], v[160:163]\n"
    "v_exp_f32 v114, v114\n"
    "v_mfma_f32_16x16x32_bf16 v[84:87], a[144:147], v[16:19], v[164:167]\n"
    "ds_read_b64_tr_b16 a[242:243], v228 offset:13312\n"
    "v_add_f32 v198, v198, v110\n"
    "v_mfma_f32_16x16x32_bf16 v[88:91], a[144:147], v[32:35], v[168:171]\n"
    "v_exp_f32 v115, v115\n"
    "v_mfma_f32_16x16x32_bf16 v[92:95], a[144:147], v[48:51], v[172:175]\n"
    "ds_read_b128 a[176:179], v224 offset:28672\n"
    "v_cvt_pk_bf16_f32 v157, v110, v111\n"
    "v_mfma_f32_16x16x32_bf16 a[64:67], a[208:211], v[128:131], a[64:67]\n"
    "v_exp_f32 v116, v116\n"
    "v_mfma_f32_16x16x32_bf16 a[68:71], a[208:211], v[132:135], a[68:71]\n"
    "ds_read_b64_tr_b16 a[244:245], v229 offset:9216\n"
    "v_add_f32 v199, v199, v111\n"
    "v_mfma_f32_16x16x32_bf16 a[72:75], a[208:211], v[136:139], a[72:75]\n"
    "v_exp_f32 v117, v117\n"
    "v_mfma_f32_16x16x32_bf16 a[76:79], a[208:211], v[140:143], a[76:79]\n"
    "ds_read_b64_tr_b16 a[246:247], v229 offset:13312\n"
    "v_cvt_pk_bf16_f32 v146, v112, v113\n"
    "v_mfma_f32_16x16x32_bf16 v[80:83], a[148:151], v[4:7], v[80:83]\n"
    "v_exp_f32 v118, v118\n"
    "v_mfma_f32_16x16x32_bf16 v[84:87], a[148:151], v[20:23], v[84:87]\n"
    "ds_read_b128 a[180:183], v225 offset:28672\n"
    "v_cvt_pk_bf16_f32 v147, v114, v115\n"
    "v_mfma_f32_16x16x32_bf16 v[88:91], a[148:151], v[36:39], v[88:91]\n"
    "s_add_u32 m0, s62, 50048\n"
    "v_exp_f32 v119, v119\n"
    "v_mfma_f32_16x16x32_bf16 v[92:95], a[148:151], v[52:55], v[92:95]\n"
    "buffer_load_dwordx4 v236, s[44:47], s60 offen offset:2176 lds\n"
    "v_cvt_pk_bf16_f32 v150, v116, v117\n"
    "v_mfma_f32_16x16x32_bf16 a[80:83], a[212:215], v[128:131], a[80:83]\n"
    "v_exp_f32 v120, v120\n"
    "v_mfma_f32_16x16x32_bf16 a[84:87], a[212:215], v[132:135], a[84:87]\n"
    "ds_read_b64_tr_b16 a[248:249], v228 offset:9728\n"
    "v_cvt_pk_bf16_f32 v151, v118, v119\n"
    "v_mfma_f32_16x16x32_bf16 a[88:91], a[212:215], v[136:139], a[88:91]\n"
    "v_exp_f32 v121, v121\n"
    "v_mfma_f32_16x16x32_bf16 a[92:95], a[212:215], v[140:143], a[92:95]\n"
    "ds_read_b64_tr_b16 a[250:251], v228 offset:13824\n"
    "v_add_f32 v196, v196, v120\n"
    "v_mfma_f32_16x16x32_bf16 v[80:83], a[152:155], v[8:11], v[80:83]\n"
    "v_exp_f32 v122, v122\n"
    "v_mfma_f32_16x16x32_bf16 v[84:87], a[152:155], v[24:27], v[84:87]\n"
    "ds_read_b128 a[184:187], v226 offset:28672\n"
    "v_cvt_pk_bf16_f32 v154, v120, v121\n"
    "v_mfma_f32_16x16x32_bf16 v[88:91], a[152:155], v[40:43], v[88:91]\n"
    "v_exp_f32 v123, v123\n"
    "v_mfma_f32_16x16x32_bf16 v[92:95], a[152:155], v[56:59], v[92:95]\n"
    "ds_read_b64_tr_b16 a[252:253], v229 offset:9728\n"
    "v_add_f32 v197, v197, v121\n"
    "v_mfma_f32_16x16x32_bf16 a[96:99], a[216:219], v[128:131], a[96:99]\n"
    "v_exp_f32 v124, v124\n"
    "v_mfma_f32_16x16x32_bf16 a[100:103], a[216:219], v[132:135], a[100:103]\n"
    "ds_read_b64_tr_b16 a[254:255], v229 offset:13824\n"
    "v_add_f32 v196, v196, v122\n"
    "v_mfma_f32_16x16x32_bf16 a[104:107], a[216:219], v[136:139], a[104:107]\n"
    "v_exp_f32 v125, v125\n"
    "v_mfma_f32_16x16x32_bf16 a[108:111], a[216:219], v[140:143], a[108:111]\n"
    "ds_read_b128 a[188:191], v227 offset:28672\n"
    "v_cvt_pk_bf16_f32 v155, v122, v123\n"
    "v_mfma_f32_16x16x32_bf16 v[80:83], a[156:159], v[12:15], v[80:83]\n"
    "v_exp_f32 v126, v126\n"
    "v_mfma_f32_16x16x32_bf16 v[84:87], a[156:159], v[28:31], v[84:87]\n"
    "v_add_f32 v197, v197, v123\n"
    "v_add_f32 v198, v198, v124\n"
    "v_mfma_f32_16x16x32_bf16 v[88:91], a[156:159], v[44:47], v[88:91]\n"
    "v_exp_f32 v127, v127\n"
    "v_mfma_f32_16x16x32_bf16 v[92:95], a[156:159], v[60:63], v[92:95]\n"
    "v_cvt_pk_bf16_f32 v158, v124, v125\n"
    "v_add_f32 v199, v199, v125\n"
    "v_mfma_f32_16x16x32_bf16 a[112:115], a[220:223], v[128:131], a[112:115]\n"
    "v_add_f32 v198, v198, v126\n"
    "v_cvt_pk_bf16_f32 v159, v126, v127\n"
    "v_mfma_f32_16x16x32_bf16 a[116:119], a[220:223], v[132:135], a[116:119]\n"
    "v_add_f32 v199, v199, v127\n"
    "v_mfma_f32_16x16x32_bf16 a[120:123], a[220:223], v[136:139], a[120:123]\n"
    "v_mfma_f32_16x16x32_bf16 a[124:127], a[220:223], v[140:143], a[124:127]\n"
    "s_add_u32 s60, s60, 16384\n"
    "s_waitcnt vmcnt(16) lgkmcnt(0)\n"
    "s_barrier\n"
    "s_sub_u32 s63, s63, 1\n"
    "s_cmp_lg_u32 s63, 0\n"
    "s_cbranch_scc1 L_loop_%=\n"
    "v_mfma_f32_16x16x32_bf16 v[96:99], a[160:163], v[0:3], v[160:163]\n"
    "v_exp_f32 v64, v64\n"
    "v_mfma_f32_16x16x32_bf16 v[100:103], a[160:163], v[16:19], v[164:167]\n"
    "v_exp_f32 v65, v65\n"
    "v_mfma_f32_16x16x32_bf16 v[104:107], a[160:163], v[32:35], v[168:171]\n"
    "global_load_dword v247, v246, %[pf0]\n"
    "v_cvt_pk_bf16_f32 v128, v64, v65\n"
    "v_mfma_f32_16x16x32_bf16 v[108:111], a[160:163], v[48:51], v[172:175]\n"
    "v_exp_f32 v66, v66\n"
    "v_mfma_f32_16x16x32_bf16 a[0:3], a[224:227], v[144:147], a[0:3]\n"
    "ds_read_b64_tr_b16 a[192:193], v228 offset:16384\n"
    "v_mfma_f32_16x16x32_bf16 a[4:7], a[224:227], v[148:151], a[4:7]\n"
    "v_exp_f32 v67, v67\n"
    "v_mfma_f32_16x16x32_bf16 a[8:11], a[224:227], v[152:155], a[8:11]\n"
    "ds_read_b64_tr_b16 a[194:195], v228 offset:20480\n"
    "v_cvt_pk_bf16_f32 v129, v66, v67\n"
    "v_mfma_f32_16x16x32_bf16 a[12:15], a[224:227], v[156:159], a[12:15]\n"
    "v_exp_f32 v68, v68\n"
    "v_mfma_f32_16x16x32_bf16 v[96:99], a[164:167], v[4:7], v[96:99]\n"
    "ds_read_b128 a[128:131], v224 offset:32768\n"
    "v_mfma_f32_16x16x32_bf16 v[100:103], a[164:167], v[20:23], v[100:103]\n"
    "v_exp_f32 v69, v69\n"
    "v_mfma_f32_16x16x32_bf16 v[104:107], a[164:167], v[36:39], v[104:107]\n"
    "ds_read_b64_tr_b16 a[196:197], v229 offset:16384\n"
    "v_cvt_pk_bf16_f32 v132, v68, v69\n"
    "v_mfma_f32_16x16x32_bf16 v[108:111], a[164:167], v[52:55], v[108:111]\n"
    "v_exp_f32 v70, v70\n"
    "v_mfma_f32_16x16x32_bf16 a[16:19], a[228:231], v[144:147], a[16:19]\n"
    "global_load_dword v247, v246, %[pf1]\n"
    "v_mfma_f32_16x16x32_bf16 a[20:23], a[228:231], v[148:151], a[20:23]\n"
    "v_exp_f32 v71, v71\n"
    "v_mfma_f32_16x16x32_bf16 a[24:27], a[228:231], v[152:155], a[24:27]\n"
    "ds_read_b64_tr_b16 a[198:199], v229 offset:20480\n"
    "v_cvt_pk_bf16_f32 v133, v70, v71\n"
    "v_mfma_f32_16x16x32_bf16 a[28:31], a[228:231], v[156:159], a[28:31]\n"
    "v_exp_f32 v72, v72\n"
    "v_mfma_f32_16x16x32_bf16 v[176:179], v[200:203], v[144:147], v[176:179]\n"
    "ds_read_b128 a[132:135], v225 offset:32768\n"
    "v_add_f32 v196, v196, v72\n"
    "v_mfma_f32_16x16x32_bf16 v[96:99], a[168:171], v[8:11], v[96:99]\n"
    "v_exp_f32 v73, v73\n"
    "v_mfma_f32_16x16x32_bf16 v[100:103], a[168:171], v[24:27], v[100:103]\n"
    "ds_read_b64_tr_b16 a[200:201], v228 offset:16896\n"
    "v_cvt_pk_bf16_f32 v136, v72, v73\n"
    "v_mfma_f32_16x16x32_bf16 v[104:107], a[168:171], v[40:43], v[104:107]\n"
    "v_exp_f32 v74, v74\n"
    "v_mfma_f32_16x16x32_bf16 v[108:111], a[168:171], v[56:59], v[108:111]\n"
    "ds_read_b64_tr_b16 a[202:203], v228 offset:20992\n"
    "v_add_f32 v197, v197, v73\n"
    "v_mfma_f32_16x16x32_bf16 a[32:35], a[232:235], v[144:147], a[32:35]\n"
    "v_exp_f32 v75, v75\n"
    "v_mfma_f32_16x16x32_bf16 a[36:39], a[232:235], v[148:151], a[36:39]\n"
    "global_load_dword v247, v246, %[pf2]\n"
    "v_add_f32 v196, v196, v74\n"
    "v_mfma_f32_16x16x32_bf16 a[40:43], a[232:235], v[152:155], a[40:43]\n"
    "v_exp_f32 v76, v76\n"
    "v_mfma_f32_16x16x32_bf16 a[44:47], a[232:235], v[156:159], a[44:47]\n"
    "ds_read_b128 a[136:139], v226 offset:32768\n"
    "v_cvt_pk_bf16_f32 v137, v74, v75\n"
    "v_mfma_f32_16x16x32_bf16 v[96:99], a[172:175], v[12:15], v[96:99]\n"
    "v_exp_f32 v77, v77\n"
    "v_mfma_f32_16x16x32_bf16 v[100:103], a[172:175], v[28:31], v[100:103]\n"
    "ds_read_b64_tr_b16 a[204:205], v229 offset:16896\n"
    "v_add_f32 v197, v197, v75\n"
    "v_mfma_f32_16x16x32_bf16 v[104:107], a[172:175], v[44:47], v[104:107]\n"
    "v_exp_f32 v78, v78\n"
    "v_mfma_f32_16x16x32_bf16 v[108:111], a[172:175], v[60:63], v[108:111]\n"
    "ds_read_b64_tr_b16 a[206:207], v229 offset:20992\n"
    "v_add_f32 v198, v198, v76\n"
    "v_mfma_f32_16x16x32_bf16 a[48:51], a[236:239], v[144:147], a[48:51]\n"
    "v_exp_f32 v79, v79\n"
    "v_mfma_f32_16x16x32_bf16 a[52:55], a[236:239], v[148:151], a[52:55]\n"
    "ds_read_b128 a[140:143], v227 offset:32768\n"
    "v_cvt_pk_bf16_f32 v140, v76, v77\n"
    "v_mfma_f32_16x16x32_bf16 a[56:59], a[236:239], v[152:155], a[56:59]\n"
    "v_exp_f32 v80, v80\n"
    "v_mfma_f32_16x16x32_bf16 a[60:63], a[236:239], v[156:159], a[60:63]\n"
    "v_exp_f32 v81, v81\n"
    "v_mfma_f32_16x16x32_bf16 v[180:183], v[200:203], v[148:151], v[180:183]\n"
    "global_load_dword v247, v246, %[pf3]\n"
    "v_add_f32 v199, v199, v77\n"
    "v_mfma_f32_16x16x32_bf16 v[112:115], a[176:179], v[0:3], v[160:163]\n"
    "v_exp_f32 v82, v82\n"
    "v_mfma_f32_16x16x32_bf16 v[116:119], a[176:179], v[16:19], v[164:167]\n"
    "ds_read_b64_tr_b16 a[208:209], v228 offset:17408\n"
    "v_add_f32 v198, v198, v78\n"
    "v_mfma_f32_16x16x32_bf16 v[120:123], a[176:179], v[32:35], v[168:171]\n"
    "v_exp_f32 v83, v83\n"
    "v_mfma_f32_16x16x32_bf16 v[124:127], a[176:179], v[48:51], v[172:175]\n"
    "ds_read_b64_tr_b16 a[210:211], v228 offset:21504\n"
    "v_cvt_pk_bf16_f32 v141, v78, v79\n"
    "v_mfma_f32_16x16x32_bf16 a[64:67], a[240:243], v[144:147], a[64:67]\n"
    "v_exp_f32 v84, v84\n"
    "v_mfma_f32_16x16x32_bf16 a[68:71], a[240:243], v[148:151], a[68:71]\n"
    "ds_read_b128 a[144:147], v224 offset:36864\n"
    "v_add_f32 v199, v199, v79\n"
    "v_mfma_f32_16x16x32_bf16 a[72:75], a[240:243], v[152:155], a[72:75]\n"
    "v_exp_f32 v85, v85\n"
    "v_mfma_f32_16x16x32_bf16 a[76:79], a[240:243], v[156:159], a[76:79]\n"
    "ds_read_b64_tr_b16 a[212:213], v229 offset:17408\n"
    "v_cvt_pk_bf16_f32 v130, v80, v81\n"
    "v_mfma_f32_16x16x32_bf16 v[112:115], a[180:183], v[4:7], v[112:115]\n"
    "v_exp_f32 v86, v86\n"
    "v_mfma_f32_16x16x32_bf16 v[116:119], a[180:183], v[20:23], v[116:119]\n"
    "global_load_dword v247, v246, %[pf4]\n"
    "v_cvt_pk_bf16_f32 v131, v82, v83\n"
    "v_mfma_f32_16x16x32_bf16 v[120:123], a[180:183], v[36:39], v[120:123]\n"
    "v_exp_f32 v87, v87\n"
    "v_mfma_f32_16x16x32_bf16 v[124:127], a[180:183], v[52:55], v[124:127]\n"
    "ds_read_b64_tr_b16 a[214:215], v229 offset:21504\n"
    "v_cvt_pk_bf16_f32 v134, v84, v85\n"
    "v_mfma_f32_16x16x32_bf16 a[80:83], a[244:247], v[144:147], a[80:83]\n"
    "v_exp_f32 v88, v88\n"
    "v_mfma_f32_16x16x32_bf16 a[84:87], a[244:247], v[148:151], a[84:87]\n"
    "ds_read_b128 a[148:151], v225 offset:36864\n"
    "v_cvt_pk_bf16_f32 v135, v86, v87\n"
    "v_mfma_f32_16x16x32_bf16 a[88:91], a[244:247], v[152:155], a[88:91]\n"
    "v_exp_f32 v89, v89\n"
    "v_mfma_f32_16x16x32_bf16 a[92:95], a[244:247], v[156:159], a[92:95]\n"
    "ds_read_b64_tr_b16 a[216:217], v228 offset:17920\n"
    "v_add_f32 v196, v196, v88\n"
    "v_mfma_f32_16x16x32_bf16 v[112:115], a[184:187], v[8:11], v[112:115]\n"
    "v_exp_f32 v90, v90\n"
    "v_mfma_f32_16x16x32_bf16 v[116:119], a[184:187], v[24:27], v[116:119]\n"
    "ds_read_b64_tr_b16 a[218:219], v228 offset:22016\n"
    "v_cvt_pk_bf16_f32 v138, v88, v89\n"
    "v_mfma_f32_16x16x32_bf16 v[120:123], a[184:187], v[40:43], v[120:123]\n"
    "v_exp_f32 v91, v91\n"
    "v_mfma_f32_16x16x32_bf16 v[124:127], a[184:187], v[56:59], v[124:127]\n"
    "ds_read_b128 a[152:155], v226 offset:36864\n"
    "v_add_f32 v197, v197, v89\n"
    "v_mfma_f32_16x16x32_bf16 a[96:99], a[248:251], v[144:147], a[96:99]\n"
    "v_exp_f32 v92, v92\n"
    "v_mfma_f32_16x16x32_bf16 a[100:103], a[248:251], v[148:151], a[100:103]\n"
    "ds_read_b64_tr_b16 a[220:221], v229 offset:17920\n"
    "v_add_f32 v196, v196, v90\n"
    "v_mfma_f32_16x16x32_bf16 a[104:107], a[248:251], v[152:155], a[104:107]\n"
    "v_exp_f32 v93, v93\n"
    "v_mfma_f32_16x16x32_bf16 a[108:111], a[248:251], v[156:159], a[108:111]\n"
    "ds_read_b64_tr_b16 a[222:223], v229 offset:22016\n"
    "v_cvt_pk_bf16_f32 v139, v90, v91\n"
    "v_mfma_f32_16x16x32_bf16 v[112:115], a[188:191], v[12:15], v[112:115]\n"
    "v_exp_f32 v94, v94\n"
    "v_mfma_f32_16x16x32_bf16 v[116:119], a[188:191], v[28:31], v[116:119]\n"
    "ds_read_b128 a[156:159], v227 offset:36864\n"
    "v_add_f32 v197, v197, v91\n"
    "v_mfma_f32_16x16x32_bf16 v[120:123], a[188:191], v[44:47], v[120:123]\n"
    "v_exp_f32 v95, v95\n"
    "v_mfma_f32_16x16x32_bf16 v[124:127], a[188:191], v[60:63], v[124:127]\n"
    "v_add_f32 v198, v198, v92\n"
    "v_cvt_pk_bf16_f32 v142, v92, v93\n"
    "v_mfma_f32_16x16x32_bf16 a[112:115], a[252:255], v[144:147], a[112:115]\n"
    "v_add_f32 v199, v199, v93\n"
    "v_add_f32 v198, v198, v94\n"
    "v_mfma_f32_16x16x32_bf16 a[116:119], a[252:255], v[148:151], a[116:119]\n"
    "v_cvt_pk_bf16_f32 v143, v94, v95\n"
    "v_add_f32 v199, v199, v95\n"
    "v_mfma_f32_16x16x32_bf16 a[120:123], a[252:255], v[152:155], a[120:123]\n"
    "v_mfma_f32_16x16x32_bf16 a[124:127], a[252:255], v[156:159], a[124:127]\n"
    "s_waitcnt lgkmcnt(0)\n"
    "v_mfma_f32_16x16x32_bf16 v[64:67], a[128:131], v[0:3], v[160:163]\n"
    "v_exp_f32 v96, v96\n"
    "v_mfma_f32_16x16x32_bf16 v[68:71], a[128:131], v[16:19], v[164:167]\n"
    "v_exp_f32 v97, v97\n"
    "v_mfma_f32_16x16x32_bf16 v[72:75], a[128:131], v[32:35], v[168:171]\n"
    "ds_read_b64_tr_b16 a[224:225], v228 offset:24576\n"
    "v_cvt_pk_bf16_f32 v144, v96, v97\n"
    "v_mfma_f32_16x16x32_bf16 v[76:79], a[128:131], v[48:51], v[172:175]\n"
    "v_exp_f32 v98, v98\n"
    "v_mfma_f32_16x16x32_bf16 a[0:3], a[192:195], v[128:131], a[0:3]\n"
    "ds_read_b64_tr_b16 a[226:227], v228 offset:28672\n"
    "v_mfma_f32_16x16x32_bf16 a[4:7], a[192:195], v[132:135], a[4:7]\n"
    "v_exp_f32 v99, v99\n"
    "v_mfma_f32_16x16x32_bf16 a[8:11], a[192:195], v[136:139], a[8:11]\n"
    "ds_read_b128 a[160:163], v224 offset:40960\n"
    "v_cvt_pk_bf16_f32 v145, v98, v99\n"
    "v_mfma_f32_16x16x32_bf16 a[12:15], a[192:195], v[140:143], a[12:15]\n"
    "v_exp_f32 v100, v100\n"
    "v_mfma_f32_16x16x32_bf16 v[64:67], a[132:135], v[4:7], v[64:67]\n"
    "ds_read_b64_tr_b16 a[228:229], v229 offset:24576\n"
    "v_mfma_f32_16x16x32_bf16 v[68:71], a[132:135], v[20:23], v[68:71]\n"
    "v_exp_f32 v101, v101\n"
    "v_mfma_f32_16x16x32_bf16 v[72:75], a[132:135], v[36:39], v[72:75]\n"
    "ds_read_b64_tr_b16 a[230:231], v229 offset:28672\n"
    "v_cvt_pk_bf16_f32 v148, v100, v101\n"
    "v_mfma_f32_16x16x32_bf16 v[76:79], a[132:135], v[52:55], v[76:79]\n"
    "v_exp_f32 v102, v102\n"
    "v_mfma_f32_16x16x32_bf16 a[16:19], a[196:199], v[128:131], a[16:19]\n"
    "ds_read_b128 a[164:167], v225 offset:40960\n"
    "v_mfma_f32_16x16x32_bf16 a[20:23], a[196:199], v[132:135], a[20:23]\n"
    "v_exp_f32 v103, v103\n"
    "v_mfma_f32_16x16x32_bf16 a[24:27], a[196:199], v[136:139], a[24:27]\n"
    "ds_read_b64_tr_b16 a[232:233], v228 offset:25088\n"
    "v_cvt_pk_bf16_f32 v149, v102, v103\n"
    "v_mfma_f32_16x16x32_bf16 a[28:31], a[196:199], v[140:143], a[28:31]\n"
    "v_exp_f32 v104, v104\n"
    "v_mfma_f32_16x16x32_bf16 v[176:179], v[200:203], v[128:131], v[176:179]\n"
    "ds_read_b64_tr_b16 a[234:235], v228 offset:29184\n"
    "v_add_f32 v196, v196, v104\n"
    "v_mfma_f32_16x16x32_bf16 v[64:67], a[136:139], v[8:11], v[64:67]\n"
    "v_exp_f32 v105, v105\n"
    "v_mfma_f32_16x16x32_bf16 v[68:71], a[136:139], v[24:27], v[68:71]\n"
    "ds_read_b128 a[168:171], v226 offset:40960\n"
    "v_cvt_pk_bf16_f32 v152, v104, v105\n"
    "v_mfma_f32_16x16x32_bf16 v[72:75], a[136:139], v[40:43], v[72:75]\n"
    "v_exp_f32 v106, v106\n"
    "v_mfma_f32_16x16x32_bf16 v[76:79], a[136:139], v[56:59], v[76:79]\n"
    "ds_read_b64_tr_b16 a[236:237], v229 offset:25088\n"
    "v_add_f32 v197, v197, v105\n"
    "v_mfma_f32_16x16x32_bf16 a[32:35], a[200:203], v[128:131], a[32:35]\n"
    "v_exp_f32 v107, v107\n"
    "v_mfma_f32_16x16x32_bf16 a[36:39], a[200:203], v[132:135], a[36:39]\n"
    "ds_read_b64_tr_b16 a[238:239], v229 offset:29184\n"
    "v_add_f32 v196, v196, v106\n"
    "v_mfma_f32_16x16x32_bf16 a[40:43], a[200:203], v[136:139], a[40:43]\n"
    "v_exp_f32 v108, v108\n"
    "v_mfma_f32_16x16x32_bf16 a[44:47], a[200:203], v[140:143], a[44:47]\n"
    "ds_read_b128 a[172:175], v227 offset:40960\n"
    "v_cvt_pk_bf16_f32 v153, v106, v107\n"
    "v_mfma_f32_16x16x32_bf16 v[64:67], a[140:143], v[12:15], v[64:67]\n"
    "v_exp_f32 v109, v109\n"
    "v_mfma_f32_16x16x32_bf16 v[68:71], a[140:143], v[28:31], v[68:71]\n"
    "ds_read_b64_tr_b16 a[240:241], v228 offset:25600\n"
    "v_add_f32 v197, v197, v107\n"
    "v_mfma_f32_16x16x32_bf16 v[72:75], a[140:143], v[44:47], v[72:75]\n"
    "v_exp_f32 v110, v110\n"
    "v_mfma_f32_16x16x32_bf16 v[76:79], a[140:143], v[60:63], v[76:79]\n"
    "ds_read_b64_tr_b16 a[242:243], v228 offset:29696\n"
    "v_add_f32 v198, v198, v108\n"
    "v_mfma_f32_16x16x32_bf16 a[48:51], a[204:207], v[128:131], a[48:51]\n"
    "v_exp_f32 v111, v111\n"
    "v_mfma_f32_16x16x32_bf16 a[52:55], a[204:207], v[132:135], a[52:55]\n"
    "ds_read_b128 a[176:179], v224 offset:45056\n"
    "v_cvt_pk_bf16_f32 v156, v108, v109\n"
    "v_mfma_f32_16x16x32_bf16 a[56:59], a[204:207], v[136:139], a[56:59]\n"
    "v_exp_f32 v112, v112\n"
    "v_mfma_f32_16x16x32_bf16 a[60:63], a[204:207], v[140:143], a[60:63]\n"
    "v_exp_f32 v113, v113\n"
    "v_mfma_f32_16x16x32_bf16 v[180:183], v[200:203], v[132:135], v[180:183]\n"
    "ds_read_b64_tr_b16 a[244:245], v229 offset:25600\n"
    "v_add_f32 v199, v199, v109\n"
    "v_mfma_f32_16x16x32_bf16 v[80:83], a[144:147], v[0:3], v[160:163]\n"
    "v_exp_f32 v114, v114\n"
    "v_mfma_f32_16x16x32_bf16 v[84:87], a[144:147], v[16:19], v[164:167]\n"
    "ds_read_b64_tr_b16 a[246:247], v229 offset:29696\n"
    "v_add_f32 v198, v198, v110\n"
    "v_mfma_f32_16x16x32_bf16 v[88:91], a[144:147], v[32:35], v[168:171]\n"
    "v_exp_f32 v115, v115\n"
    "v_mfma_f32_16x16x32_bf16 v[92:95], a[144:147], v[48:51], v[172:175]\n"
    "ds_read_b128 a[180:183], v225 offset:45056\n"
    "v_cvt_pk_bf16_f32 v157, v110, v111\n"
    "v_mfma_f32_16x16x32_bf16 a[64:67], a[208:211], v[128:131], a[64:67]\n"
    "v_exp_f32 v116, v116\n"
    "v_mfma_f32_16x16x32_bf16 a[68:71], a[208:211], v[132:135], a[68:71]\n"
    "ds_read_b64_tr_b16 a[248:249], v228 offset:26112\n"
    "v_add_f32 v199, v199, v111\n"
    "v_mfma_f32_16x16x32_bf16 a[72:75], a[208:211], v[136:139], a[72:75]\n"
    "v_exp_f32 v117, v117\n"
    "v_mfma_f32_16x16x32_bf16 a[76:79], a[208:211], v[140:143], a[76:79]\n"
    "ds_read_b64_tr_b16 a[250:251], v228 offset:30208\n"
    "v_cvt_pk_bf16_f32 v146, v112, v113\n"
    "v_mfma_f32_16x16x32_bf16 v[80:83], a[148:151], v[4:7], v[80:83]\n"
    "v_exp_f32 v118, v118\n"
    "v_mfma_f32_16x16x32_bf16 v[84:87], a[148:151], v[20:23], v[84:87]\n"
    "ds_read_b128 a[184:187], v226 offset:45056\n"
    "v_cvt_pk_bf16_f32 v147, v114, v115\n"
    "v_mfma_f32_16x16x32_bf16 v[88:91], a[148:151], v[36:39], v[88:91]\n"
    "v_exp_f32 v119, v119\n"
    "v_mfma_f32_16x16x32_bf16 v[92:95], a[148:151], v[52:55], v[92:95]\n"
    "ds_read_b64_tr_b16 a[252:253], v229 offset:26112\n"
    "v_cvt_pk_bf16_f32 v150, v116, v117\n"
    "v_mfma_f32_16x16x32_bf16 a[80:83], a[212:215], v[128:131], a[80:83]\n"
    "v_exp_f32 v120, v120\n"
    "v_mfma_f32_16x16x32_bf16 a[84:87], a[212:215], v[132:135], a[84:87]\n"
    "ds_read_b64_tr_b16 a[254:255], v229 offset:30208\n"
    "v_cvt_pk_bf16_f32 v151, v118, v119\n"
    "v_mfma_f32_16x16x32_bf16 a[88:91], a[212:215], v[136:139], a[88:91]\n"
    "v_exp_f32 v121, v121\n"
    "v_mfma_f32_16x16x32_bf16 a[92:95], a[212:215], v[140:143], a[92:95]\n"
    "ds_read_b128 a[188:191], v227 offset:45056\n"
    "v_add_f32 v196, v196, v120\n"
    "v_mfma_f32_16x16x32_bf16 v[80:83], a[152:155], v[8:11], v[80:83]\n"
    "v_exp_f32 v122, v122\n"
    "v_mfma_f32_16x16x32_bf16 v[84:87], a[152:155], v[24:27], v[84:87]\n"
    "v_cvt_pk_bf16_f32 v154, v120, v121\n"
    "v_add_f32 v197, v197, v121\n"
    "v_mfma_f32_16x16x32_bf16 v[88:91], a[152:155], v[40:43], v[88:91]\n"
    "v_exp_f32 v123, v123\n"
    "v_mfma_f32_16x16x32_bf16 v[92:95], a[152:155], v[56:59], v[92:95]\n"
    "v_add_f32 v196, v196, v122\n"
    "v_cvt_pk_bf16_f32 v155, v122, v123\n"
    "v_mfma_f32_16x16x32_bf16 a[96:99], a[216:219], v[128:131], a[96:99]\n"
    "v_exp_f32 v124, v124\n"
    "v_mfma_f32_16x16x32_bf16 a[100:103], a[216:219], v[132:135], a[100:103]\n"
    "v_add_f32 v197, v197, v123\n"
    "v_add_f32 v198, v198, v124\n"
    "v_mfma_f32_16x16x32_bf16 a[104:107], a[216:219], v[136:139], a[104:107]\n"
    "v_exp_f32 v125, v125\n"
    "v_mfma_f32_16x16x32_bf16 a[108:111], a[216:219], v[140:143], a[108:111]\n"
    "v_cvt_pk_bf16_f32 v158, v124, v125\n"
    "v_add_f32 v199, v199, v125\n"
    "v_mfma_f32_16x16x32_bf16 v[80:83], a[156:159], v[12:15], v[80:83]\n"
    "v_exp_f32 v126, v126\n"
    "v_mfma_f32_16x16x32_bf16 v[84:87], a[156:159], v[28:31], v[84:87]\n"
    "v_add_f32 v198, v198, v126\n"
    "v_mfma_f32_16x16x32_bf16 v[88:91], a[156:159], v[44:47], v[88:91]\n"
    "v_exp_f32 v127, v127\n"
    "v_mfma_f32_16x16x32_bf16 v[92:95], a[156:159], v[60:63], v[92:95]\n"
    "v_cvt_pk_bf16_f32 v159, v126, v127\n"
    "v_add_f32 v199, v199, v127\n"
    "v_mfma_f32_16x16x32_bf16 a[112:115], a[220:223], v[128:131], a[112:115]\n"
    "v_mfma_f32_16x16x32_bf16 a[116:119], a[220:223], v[132:135], a[116:119]\n"
    "v_mfma_f32_16x16x32_bf16 a[120:123], a[220:223], v[136:139], a[120:123]\n"
    "v_mfma_f32_16x16x32_bf16 a[124:127], a[220:223], v[140:143], a[124:127]\n"
    "s_waitcnt vmcnt(13) lgkmcnt(0)\n"
    "s_barrier\n"
    "v_mfma_f32_16x16x32_bf16 v[96:99], a[160:163], v[0:3], v[160:163]\n"
    "v_exp_f32 v64, v64\n"
    "v_mfma_f32_16x16x32_bf16 v[100:103], a[160:163], v[16:19], v[164:167]\n"
    "v_exp_f32 v65, v65\n"
    "v_mfma_f32_16x16x32_bf16 v[104:107], a[160:163], v[32:35], v[168:171]\n"
    "ds_read_b64_tr_b16 a[192:193], v228 offset:32768\n"
    "v_cvt_pk_bf16_f32 v128, v64, v65\n"
    "v_mfma_f32_16x16x32_bf16 v[108:111], a[160:163], v[48:51], v[172:175]\n"
    "v_exp_f32 v66, v66\n"
    "v_mfma_f32_16x16x32_bf16 a[0:3], a[224:227], v[144:147], a[0:3]\n"
    "ds_read_b64_tr_b16 a[194:195], v228 offset:36864\n"
    "v_mfma_f32_16x16x32_bf16 a[4:7], a[224:227], v[148:151], a[4:7]\n"
    "v_exp_f32 v67, v67\n"
    "v_mfma_f32_16x16x32_bf16 a[8:11], a[224:227], v[152:155], a[8:11]\n"
    "ds_read_b128 a[128:131], v224 offset:49152\n"
    "v_cvt_pk_bf16_f32 v129, v66, v67\n"
    "v_mfma_f32_16x16x32_bf16 a[12:15], a[224:227], v[156:159], a[12:15]\n"
    "v_exp_f32 v68, v68\n"
    "v_mfma_f32_16x16x32_bf16 v[96:99], a[164:167], v[4:7], v[96:99]\n"
    "ds_read_b64_tr_b16 a[196:197], v229 offset:32768\n"
    "v_mfma_f32_16x16x32_bf16 v[100:103], a[164:167], v[20:23], v[100:103]\n"
    "v_exp_f32 v69, v69\n"
    "v_mfma_f32_16x16x32_bf16 v[104:107], a[164:167], v[36:39], v[104:107]\n"
    "ds_read_b64_tr_b16 a[198:199], v229 offset:36864\n"
    "v_cvt_pk_bf16_f32 v132, v68, v69\n"
    "v_mfma_f32_16x16x32_bf16 v[108:111], a[164:167], v[52:55], v[108:111]\n"
    "v_exp_f32 v70, v70\n"
    "v_mfma_f32_16x16x32_bf16 a[16:19], a[228:231], v[144:147], a[16:19]\n"
    "ds_read_b128 a[132:135], v225 offset:49152\n"
    "v_mfma_f32_16x16x32_bf16 a[20:23], a[228:231], v[148:151], a[20:23]\n"
    "v_exp_f32 v71, v71\n"
    "v_mfma_f32_16x16x32_bf16 a[24:27], a[228:231], v[152:155], a[24:27]\n"
    "ds_read_b64_tr_b16 a[200:201], v228 offset:33280\n"
    "v_cvt_pk_bf16_f32 v133, v70, v71\n"
    "v_mfma_f32_16x16x32_bf16 a[28:31], a[228:231], v[156:159], a[28:31]\n"
    "v_exp_f32 v72, v72\n"
    "v_mfma_f32_16x16x32_bf16 v[176:179], v[200:203], v[144:147], v[176:179]\n"
    "ds_read_b64_tr_b16 a[202:203], v228 offset:37376\n"
    "v_add_f32 v196, v196, v72\n"
    "v_mfma_f32_16x16x32_bf16 v[96:99], a[168:171], v[8:11], v[96:99]\n"
    "v_exp_f32 v73, v73\n"
    "v_mfma_f32_16x16x32_bf16 v[100:103], a[168:171], v[24:27], v[100:103]\n"
    "ds_read_b128 a[136:139], v226 offset:49152\n"
    "v_cvt_pk_bf16_f32 v136, v72, v73\n"
    "v_mfma_f32_16x16x32_bf16 v[104:107], a[168:171], v[40:43], v[104:107]\n"
    "v_exp_f32 v74, v74\n"
    "v_mfma_f32_16x16x32_bf16 v[108:111], a[168:171], v[56:59], v[108:111]\n"
    "ds_read_b64_tr_b16 a[204:205], v229 offset:33280\n"
    "v_add_f32 v197, v197, v73\n"
    "v_mfma_f32_16x16x32_bf16 a[32:35], a[232:235], v[144:147], a[32:35]\n"
    "v_exp_f32 v75, v75\n"
    "v_mfma_f32_16x16x32_bf16 a[36:39], a[232:235], v[148:151], a[36:39]\n"
    "ds_read_b64_tr_b16 a[206:207], v229 offset:37376\n"
    "v_add_f32 v196, v196, v74\n"
    "v_mfma_f32_16x16x32_bf16 a[40:43], a[232:235], v[152:155], a[40:43]\n"
    "v_exp_f32 v76, v76\n"
    "v_mfma_f32_16x16x32_bf16 a[44:47], a[232:235], v[156:159], a[44:47]\n"
    "ds_read_b128 a[140:143], v227 offset:49152\n"
    "v_cvt_pk_bf16_f32 v137, v74, v75\n"
    "v_mfma_f32_16x16x32_bf16 v[96:99], a[172:175], v[12:15], v[96:99]\n"
    "v_exp_f32 v77, v77\n"
    "v_mfma_f32_16x16x32_bf16 v[100:103], a[172:175], v[28:31], v[100:103]\n"
    "ds_read_b64_tr_b16 a[208:209], v228 offset:33792\n"
    "v_add_f32 v197, v197, v75\n"
    "v_mfma_f32_16x16x32_bf16 v[104:107], a[172:175], v[44:47], v[104:107]\n"
    "v_exp_f32 v78, v78\n"
    "v_mfma_f32_16x16x32_bf16 v[108:111], a[172:175], v[60:63], v[108:111]\n"
    "ds_read_b64_tr_b16 a[210:211], v228 offset:37888\n"
    "v_add_f32 v198, v198, v76\n"
    "v_mfma_f32_16x16x32_bf16 a[48:51], a[236:239], v[144:147], a[48:51]\n"
    "v_exp_f32 v79, v79\n"
    "v_mfma_f32_16x16x32_bf16 a[52:55], a[236:239], v[148:151], a[52:55]\n"
    "ds_read_b128 a[144:147], v224 offset:53248\n"
    "v_cvt_pk_bf16_f32 v140, v76, v77\n"
    "v_mfma_f32_16x16x32_bf16 a[56:59], a[236:239], v[152:155], a[56:59]\n"
    "v_exp_f32 v80, v80\n"
    "v_mfma_f32_16x16x32_bf16 a[60:63], a[236:239], v[156:159], a[60:63]\n"
    "v_exp_f32 v81, v81\n"
    "v_mfma_f32_16x16x32_bf16 v[180:183], v[200:203], v[148:151], v[180:183]\n"
    "ds_read_b64_tr_b16 a[212:213], v229 offset:33792\n"
    "v_add_f32 v199, v199, v77\n"
    "v_mfma_f32_16x16x32_bf16 v[112:115], a[176:179], v[0:3], v[160:163]\n"
    "v_exp_f32 v82, v82\n"
    "v_mfma_f32_16x16x32_bf16 v[116:119], a[176:179], v[16:19], v[164:167]\n"
    "ds_read_b64_tr_b16 a[214:215], v229 offset:37888\n"
    "v_add_f32 v198, v198, v78\n"
    "v_mfma_f32_16x16x32_bf16 v[120:123], a[176:179], v[32:35], v[168:171]\n"
    "v_exp_f32 v83, v83\n"
    "v_mfma_f32_16x16x32_bf16 v[124:127], a[176:179], v[48:51], v[172:175]\n"
    "ds_read_b128 a[148:151], v225 offset:53248\n"
    "v_cvt_pk_bf16_f32 v141, v78, v79\n"
    "v_mfma_f32_16x16x32_bf16 a[64:67], a[240:243], v[144:147], a[64:67]\n"
    "v_exp_f32 v84, v84\n"
    "v_mfma_f32_16x16x32_bf16 a[68:71], a[240:243], v[148:151], a[68:71]\n"
    "ds_read_b64_tr_b16 a[216:217], v228 offset:34304\n"
    "v_add_f32 v199, v199, v79\n"
    "v_mfma_f32_16x16x32_bf16 a[72:75], a[240:243], v[152:155], a[72:75]\n"
    "v_exp_f32 v85, v85\n"
    "v_mfma_f32_16x16x32_bf16 a[76:79], a[240:243], v[156:159], a[76:79]\n"
    "ds_read_b64_tr_b16 a[218:219], v228 offset:38400\n"
    "v_cvt_pk_bf16_f32 v130, v80, v81\n"
    "v_mfma_f32_16x16x32_bf16 v[112:115], a[180:183], v[4:7], v[112:115]\n"
    "v_exp_f32 v86, v86\n"
    "v_mfma_f32_16x16x32_bf16 v[116:119], a[180:183], v[20:23], v[116:119]\n"
    "ds_read_b128 a[152:155], v226 offset:53248\n"
    "v_cvt_pk_bf16_f32 v131, v82, v83\n"
    "v_mfma_f32_16x16x32_bf16 v[120:123], a[180:183], v[36:39], v[120:123]\n"
    "v_exp_f32 v87, v87\n"
    "v_mfma_f32_16x16x32_bf16 v[124:127], a[180:183], v[52:55], v[124:127]\n"
    "ds_read_b64_tr_b16 a[220:221], v229 offset:34304\n"
    "v_cvt_pk_bf16_f32 v134, v84, v85\n"
    "v_mfma_f32_16x16x32_bf16 a[80:83], a[244:247], v[144:147], a[80:83]\n"
    "v_exp_f32 v88, v88\n"
    "v_mfma_f32_16x16x32_bf16 a[84:87], a[244:247], v[148:151], a[84:87]\n"
    "ds_read_b64_tr_b16 a[222:223], v229 offset:38400\n"
    "v_cvt_pk_bf16_f32 v135, v86, v87\n"
    "v_mfma_f32_16x16x32_bf16 a[88:91], a[244:247], v[152:155], a[88:91]\n"
    "v_exp_f32 v89, v89\n"
    "v_mfma_f32_16x16x32_bf16 a[92:95], a[244:247], v[156:159], a[92:95]\n"
    "ds_read_b128 a[156:159], v227 offset:53248\n"
    "v_add_f32 v196, v196, v88\n"
    "v_mfma_f32_16x16x32_bf16 v[112:115], a[184:187], v[8:11], v[112:115]\n"
    "v_exp_f32 v90, v90\n"
    "v_mfma_f32_16x16x32_bf16 v[116:119], a[184:187], v[24:27], v[116:119]\n"
    "v_cvt_pk_bf16_f32 v138, v88, v89\n"
    "v_add_f32 v197, v197, v89\n"
    "v_mfma_f32_16x16x32_bf16 v[120:123], a[184:187], v[40:43], v[120:123]\n"
    "v_exp_f32 v91, v91\n"
    "v_mfma_f32_16x16x32_bf16 v[124:127], a[184:187], v[56:59], v[124:127]\n"
    "v_add_f32 v196, v196, v90\n"
    "v_cvt_pk_bf16_f32 v139, v90, v91\n"
    "v_mfma_f32_16x16x32_bf16 a[96:99], a[248:251], v[144:147], a[96:99]\n"
    "v_exp_f32 v92, v92\n"
    "v_mfma_f32_16x16x32_bf16 a[100:103], a[248:251], v[148:151], a[100:103]\n"
    "v_add_f32 v197, v197, v91\n"
    "v_add_f32 v198, v198, v92\n"
    "v_mfma_f32_16x16x32_bf16 a[104:107], a[248:251], v[152:155], a[104:107]\n"
    "v_exp_f32 v93, v93\n"
    "v_mfma_f32_16x16x32_bf16 a[108:111], a[248:251], v[156:159], a[108:111]\n"
    "v_cvt_pk_bf16_f32 v142, v92, v93\n"
    "v_add_f32 v199, v199, v93\n"
    "v_mfma_f32_16x16x32_bf16 v[112:115], a[188:191], v[12:15], v[112:115]\n"
    "v_exp_f32 v94, v94\n"
    "v_mfma_f32_16x16x32_bf16 v[116:119], a[188:191], v[28:31], v[116:119]\n"
    "v_add_f32 v198, v198, v94\n"
    "v_mfma_f32_16x16x32_bf16 v[120:123], a[188:191], v[44:47], v[120:123]\n"
    "v_exp_f32 v95, v95\n"
    "v_mfma_f32_16x16x32_bf16 v[124:127], a[188:191], v[60:63], v[124:127]\n"
    "v_cvt_pk_bf16_f32 v143, v94, v95\n"
    "v_add_f32 v199, v199, v95\n"
    "v_mfma_f32_16x16x32_bf16 a[112:115], a[252:255], v[144:147], a[112:115]\n"
    "v_mfma_f32_16x16x32_bf16 a[116:119], a[252:255], v[148:151], a[116:119]\n"
    "v_mfma_f32_16x16x32_bf16 a[120:123], a[252:255], v[152:155], a[120:123]\n"
    "v_mfma_f32_16x16x32_bf16 a[124:127], a[252:255], v[156:159], a[124:127]\n"
    "s_waitcnt lgkmcnt(0)\n"
    "v_mfma_f32_16x16x32_bf16 v[64:67], a[128:131], v[0:3], v[160:163]\n"
    "v_exp_f32 v96, v96\n"
    "v_mfma_f32_16x16x32_bf16 v[68:71], a[128:131], v[16:19], v[164:167]\n"
    "v_exp_f32 v97, v97\n"
    "v_mfma_f32_16x16x32_bf16 v[72:75], a[128:131], v[32:35], v[168:171]\n"
    "ds_read_b64_tr_b16 a[224:225], v228 offset:40960\n"
    "v_cvt_pk_bf16_f32 v144, v96, v97\n"
    "v_mfma_f32_16x16x32_bf16 v[76:79], a[128:131], v[48:51], v[172:175]\n"
    "v_exp_f32 v98, v98\n"
    "v_mfma_f32_16x16x32_bf16 a[0:3], a[192:195], v[128:131], a[0:3]\n"
    "ds_read_b64_tr_b16 a[226:227], v228 offset:45056\n"
    "v_mfma_f32_16x16x32_bf16 a[4:7], a[192:195], v[132:135], a[4:7]\n"
    "v_exp_f32 v99, v99\n"
    "v_mfma_f32_16x16x32_bf16 a[8:11], a[192:195], v[136:139], a[8:11]\n"
    "ds_read_b128 a[160:163], v224 offset:57344\n"
    "v_cvt_pk_bf16_f32 v145, v98, v99\n"
    "v_mfma_f32_16x16x32_bf16 a[12:15], a[192:195], v[140:143], a[12:15]\n"
    "v_exp_f32 v100, v100\n"
    "v_mfma_f32_16x16x32_bf16 v[64:67], a[132:135], v[4:7], v[64:67]\n"
    "ds_read_b64_tr_b16 a[228:229], v229 offset:40960\n"
    "v_mfma_f32_16x16x32_bf16 v[68:71], a[132:135], v[20:23], v[68:71]\n"
    "v_exp_f32 v101, v101\n"
    "v_mfma_f32_16x16x32_bf16 v[72:75], a[132:135], v[36:39], v[72:75]\n"
    "ds_read_b64_tr_b16 a[230:231], v229 offset:45056\n"
    "v_cvt_pk_bf16_f32 v148, v100, v101\n"
    "v_mfma_f32_16x16x32_bf16 v[76:79], a[132:135], v[52:55], v[76:79]\n"
    "v_exp_f32 v102, v102\n"
    "v_mfma_f32_16x16x32_bf16 a[16:19], a[196:199], v[128:131], a[16:19]\n"
    "ds_read_b128 a[164:167], v225 offset:57344\n"
    "v_mfma_f32_16x16x32_bf16 a[20:23], a[196:199], v[132:135], a[20:23]\n"
    "v_exp_f32 v103, v103\n"
    "v_mfma_f32_16x16x32_bf16 a[24:27], a[196:199], v[136:139], a[24:27]\n"
    "ds_read_b64_tr_b16 a[232:233], v228 offset:41472\n"
    "v_cvt_pk_bf16_f32 v149, v102, v103\n"
    "v_mfma_f32_16x16x32_bf16 a[28:31], a[196:199], v[140:143], a[28:31]\n"
    "v_exp_f32 v104, v104\n"
    "v_mfma_f32_16x16x32_bf16 v[176:179], v[200:203], v[128:131], v[176:179]\n"
    "ds_read_b64_tr_b16 a[234:235], v228 offset:45568\n"
    "v_add_f32 v196, v196, v104\n"
    "v_mfma_f32_16x16x32_bf16 v[64:67], a[136:139], v[8:11], v[64:67]\n"
    "v_exp_f32 v105, v105\n"
    "v_mfma_f32_16x16x32_bf16 v[68:71], a[136:139], v[24:27], v[68:71]\n"
    "ds_read_b128 a[168:171], v226 offset:57344\n"
    "v_cvt_pk_bf16_f32 v152, v104, v105\n"
    "v_mfma_f32_16x16x32_bf16 v[72:75], a[136:139], v[40:43], v[72:75]\n"
    "v_exp_f32 v106, v106\n"
    "v_mfma_f32_16x16x32_bf16 v[76:79], a[136:139], v[56:59], v[76:79]\n"
    "ds_read_b64_tr_b16 a[236:237], v229 offset:41472\n"
    "v_add_f32 v197, v197, v105\n"
    "v_mfma_f32_16x16x32_bf16 a[32:35], a[200:203], v[128:131], a[32:35]\n"
    "v_exp_f32 v107, v107\n"
    "v_mfma_f32_16x16x32_bf16 a[36:39], a[200:203], v[132:135], a[36:39]\n"
    "ds_read_b64_tr_b16 a[238:239], v229 offset:45568\n"
    "v_add_f32 v196, v196, v106\n"
    "v_mfma_f32_16x16x32_bf16 a[40:43], a[200:203], v[136:139], a[40:43]\n"
    "v_exp_f32 v108, v108\n"
    "v_mfma_f32_16x16x32_bf16 a[44:47], a[200:203], v[140:143], a[44:47]\n"
    "ds_read_b128 a[172:175], v227 offset:57344\n"
    "v_cvt_pk_bf16_f32 v153, v106, v107\n"
    "v_mfma_f32_16x16x32_bf16 v[64:67], a[140:143], v[12:15], v[64:67]\n"
    "v_exp_f32 v109, v109\n"
    "v_mfma_f32_16x16x32_bf16 v[68:71], a[140:143], v[28:31], v[68:71]\n"
    "ds_read_b64_tr_b16 a[240:241], v228 offset:41984\n"
    "v_add_f32 v197, v197, v107\n"
    "v_mfma_f32_16x16x32_bf16 v[72:75], a[140:143], v[44:47], v[72:75]\n"
    "v_exp_f32 v110, v110\n"
    "v_mfma_f32_16x16x32_bf16 v[76:79], a[140:143], v[60:63], v[76:79]\n"
    "ds_read_b64_tr_b16 a[242:243], v228 offset:46080\n"
    "v_add_f32 v198, v198, v108\n"
    "v_mfma_f32_16x16x32_bf16 a[48:51], a[204:207], v[128:131], a[48:51]\n"
    "v_exp_f32 v111, v111\n"
    "v_mfma_f32_16x16x32_bf16 a[52:55], a[204:207], v[132:135], a[52:55]\n"
    "ds_read_b128 a[176:179], v224 offset:61440\n"
    "v_cvt_pk_bf16_f32 v156, v108, v109\n"
    "v_mfma_f32_16x16x32_bf16 a[56:59], a[204:207], v[136:139], a[56:59]\n"
    "v_exp_f32 v112, v112\n"
    "v_mfma_f32_16x16x32_bf16 a[60:63], a[204:207], v[140:143], a[60:63]\n"
    "v_exp_f32 v113, v113\n"
    "v_mfma_f32_16x16x32_bf16 v[180:183], v[200:203], v[132:135], v[180:183]\n"
    "ds_read_b64_tr_b16 a[244:245], v229 offset:41984\n"
    "v_add_f32 v199, v199, v109\n"
    "v_mfma_f32_16x16x32_bf16 v[80:83], a[144:147], v[0:3], v[160:163]\n"
    "v_exp_f32 v114, v114\n"
    "v_mfma_f32_16x16x32_bf16 v[84:87], a[144:147], v[16:19], v[164:167]\n"
    "ds_read_b64_tr_b16 a[246:247], v229 offset:46080\n"
    "v_add_f32 v198, v198, v110\n"
    "v_mfma_f32_16x16x32_bf16 v[88:91], a[144:147], v[32:35], v[168:171]\n"
    "v_exp_f32 v115, v115\n"
    "v_mfma_f32_16x16x32_bf16 v[92:95], a[144:147], v[48:51], v[172:175]\n"
    "ds_read_b128 a[180:183], v225 offset:61440\n"
    "v_cvt_pk_bf16_f32 v157, v110, v111\n"
    "v_mfma_f32_16x16x32_bf16 a[64:67], a[208:211], v[128:131], a[64:67]\n"
    "v_exp_f32 v116, v116\n"
    "v_mfma_f32_16x16x32_bf16 a[68:71], a[208:211], v[132:135], a[68:71]\n"
    "ds_read_b64_tr_b16 a[248:249], v228 offset:42496\n"
    "v_add_f32 v199, v199, v111\n"
    "v_mfma_f32_16x16x32_bf16 a[72:75], a[208:211], v[136:139], a[72:75]\n"
    "v_exp_f32 v117, v117\n"
    "v_mfma_f32_16x16x32_bf16 a[76:79], a[208:211], v[140:143], a[76:79]\n"
    "ds_read_b64_tr_b16 a[250:251], v228 offset:46592\n"
    "v_cvt_pk_bf16_f32 v146, v112, v113\n"
    "v_mfma_f32_16x16x32_bf16 v[80:83], a[148:151], v[4:7], v[80:83]\n"
    "v_exp_f32 v118, v118\n"
    "v_mfma_f32_16x16x32_bf16 v[84:87], a[148:151], v[20:23], v[84:87]\n"
    "ds_read_b128 a[184:187], v226 offset:61440\n"
    "v_cvt_pk_bf16_f32 v147, v114, v115\n"
    "v_mfma_f32_16x16x32_bf16 v[88:91], a[148:151], v[36:39], v[88:91]\n"
    "v_exp_f32 v119, v119\n"
    "v_mfma_f32_16x16x32_bf16 v[92:95], a[148:151], v[52:55], v[92:95]\n"
    "ds_read_b64_tr_b16 a[252:253], v229 offset:42496\n"
    "v_cvt_pk_bf16_f32 v150, v116, v117\n"
    "v_mfma_f32_16x16x32_bf16 a[80:83], a[212:215], v[128:131], a[80:83]\n"
    "v_exp_f32 v120, v120\n"
    "v_mfma_f32_16x16x32_bf16 a[84:87], a[212:215], v[132:135], a[84:87]\n"
    "ds_read_b64_tr_b16 a[254:255], v229 offset:46592\n"
    "v_cvt_pk_bf16_f32 v151, v118, v119\n"
    "v_mfma_f32_16x16x32_bf16 a[88:91], a[212:215], v[136:139], a[88:91]\n"
    "v_exp_f32 v121, v121\n"
    "v_mfma_f32_16x16x32_bf16 a[92:95], a[212:215], v[140:143], a[92:95]\n"
    "ds_read_b128 a[188:191], v227 offset:61440\n"
    "v_add_f32 v196, v196, v120\n"
    "v_mfma_f32_16x16x32_bf16 v[80:83], a[152:155], v[8:11], v[80:83]\n"
    "v_exp_f32 v122, v122\n"
    "v_mfma_f32_16x16x32_bf16 v[84:87], a[152:155], v[24:27], v[84:87]\n"
    "v_cvt_pk_bf16_f32 v154, v120, v121\n"
    "v_add_f32 v197, v197, v121\n"
    "v_mfma_f32_16x16x32_bf16 v[88:91], a[152:155], v[40:43], v[88:91]\n"
    "v_exp_f32 v123, v123\n"
    "v_mfma_f32_16x16x32_bf16 v[92:95], a[152:155], v[56:59], v[92:95]\n"
    "v_add_f32 v196, v196, v122\n"
    "v_cvt_pk_bf16_f32 v155, v122, v123\n"
    "v_mfma_f32_16x16x32_bf16 a[96:99], a[216:219], v[128:131], a[96:99]\n"
    "v_exp_f32 v124, v124\n"
    "v_mfma_f32_16x16x32_bf16 a[100:103], a[216:219], v[132:135], a[100:103]\n"
    "v_add_f32 v197, v197, v123\n"
    "v_add_f32 v198, v198, v124\n"
    "v_mfma_f32_16x16x32_bf16 a[104:107], a[216:219], v[136:139], a[104:107]\n"
    "v_exp_f32 v125, v125\n"
    "v_mfma_f32_16x16x32_bf16 a[108:111], a[216:219], v[140:143], a[108:111]\n"
    "v_cvt_pk_bf16_f32 v158, v124, v125\n"
    "v_add_f32 v199, v199, v125\n"
    "v_mfma_f32_16x16x32_bf16 v[80:83], a[156:159], v[12:15], v[80:83]\n"
    "v_exp_f32 v126, v126\n"
    "v_mfma_f32_16x16x32_bf16 v[84:87], a[156:159], v[28:31], v[84:87]\n"
    "v_add_f32 v198, v198, v126\n"
    "v_mfma_f32_16x16x32_bf16 v[88:91], a[156:159], v[44:47], v[88:91]\n"
    "v_exp_f32 v127, v127\n"
    "v_mfma_f32_16x16x32_bf16 v[92:95], a[156:159], v[60:63], v[92:95]\n"
    "v_cvt_pk_bf16_f32 v159, v126, v127\n"
    "v_add_f32 v199, v199, v127\n"
    "v_mfma_f32_16x16x32_bf16 a[112:115], a[220:223], v[128:131], a[112:115]\n"
    "v_mfma_f32_16x16x32_bf16 a[116:119], a[220:223], v[132:135], a[116:119]\n"
    "v_mfma_f32_16x16x32_bf16 a[120:123], a[220:223], v[136:139], a[120:123]\n"
    "v_mfma_f32_16x16x32_bf16 a[124:127], a[220:223], v[140:143], a[124:127]\n"
    "s_waitcnt vmcnt(5) lgkmcnt(0)\n"
    "s_barrier\n"
    "v_mfma_f32_16x16x32_bf16 v[96:99], a[160:163], v[0:3], v[160:163]\n"
    "v_exp_f32 v64, v64\n"
    "v_mfma_f32_16x16x32_bf16 v[100:103], a[160:163], v[16:19], v[164:167]\n"
    "v_exp_f32 v65, v65\n"
    "v_mfma_f32_16x16x32_bf16 v[104:107], a[160:163], v[32:35], v[168:171]\n"
    "ds_read_b64_tr_b16 a[192:193], v228 offset:49152\n"
    "v_cvt_pk_bf16_f32 v128, v64, v65\n"
    "v_mfma_f32_16x16x32_bf16 v[108:111], a[160:163], v[48:51], v[172:175]\n"
    "v_exp_f32 v66, v66\n"
    "v_mfma_f32_16x16x32_bf16 a[0:3], a[224:227], v[144:147], a[0:3]\n"
    "ds_read_b64_tr_b16 a[194:195], v228 offset:53248\n"
    "v_mfma_f32_16x16x32_bf16 a[4:7], a[224:227], v[148:151], a[4:7]\n"
    "v_exp_f32 v67, v67\n"
    "v_mfma_f32_16x16x32_bf16 a[8:11], a[224:227], v[152:155], a[8:11]\n"
    "ds_read_b64_tr_b16 a[196:197], v229 offset:49152\n"
    "v_cvt_pk_bf16_f32 v129, v66, v67\n"
    "v_mfma_f32_16x16x32_bf16 a[12:15], a[224:227], v[156:159], a[12:15]\n"
    "v_exp_f32 v68, v68\n"
    "v_mfma_f32_16x16x32_bf16 v[96:99], a[164:167], v[4:7], v[96:99]\n"
    "ds_read_b64_tr_b16 a[198:199], v229 offset:53248\n"
    "v_mfma_f32_16x16x32_bf16 v[100:103], a[164:167], v[20:23], v[100:103]\n"
    "v_exp_f32 v69, v69\n"
    "v_mfma_f32_16x16x32_bf16 v[104:107], a[164:167], v[36:39], v[104:107]\n"
    "ds_read_b64_tr_b16 a[200:201], v228 offset:49664\n"
    "v_cvt_pk_bf16_f32 v132, v68, v69\n"
    "v_mfma_f32_16x16x32_bf16 v[108:111], a[164:167], v[52:55], v[108:111]\n"
    "v_exp_f32 v70, v70\n"
    "v_mfma_f32_16x16x32_bf16 a[16:19], a[228:231], v[144:147], a[16:19]\n"
    "ds_read_b64_tr_b16 a[202:203], v228 offset:53760\n"
    "v_mfma_f32_16x16x32_bf16 a[20:23], a[228:231], v[148:151], a[20:23]\n"
    "v_exp_f32 v71, v71\n"
    "v_mfma_f32_16x16x32_bf16 a[24:27], a[228:231], v[152:155], a[24:27]\n"
    "ds_read_b64_tr_b16 a[204:205], v229 offset:49664\n"
    "v_cvt_pk_bf16_f32 v133, v70, v71\n"
    "v_mfma_f32_16x16x32_bf16 a[28:31], a[228:231], v[156:159], a[28:31]\n"
    "v_exp_f32 v72, v72\n"
    "v_mfma_f32_16x16x32_bf16 v[176:179], v[200:203], v[144:147], v[176:179]\n"
    "ds_read_b64_tr_b16 a[206:207], v229 offset:53760\n"
    "v_add_f32 v196, v196, v72\n"
    "v_mfma_f32_16x16x32_bf16 v[96:99], a[168:171], v[8:11], v[96:99]\n"
    "v_exp_f32 v73, v73\n"
    "v_mfma_f32_16x16x32_bf16 v[100:103], a[168:171], v[24:27], v[100:103]\n"
    "ds_read_b64_tr_b16 a[208:209], v228 offset:50176\n"
    "v_cvt_pk_bf16_f32 v136, v72, v73\n"
    "v_mfma_f32_16x16x32_bf16 v[104:107], a[168:171], v[40:43], v[104:107]\n"
    "v_exp_f32 v74, v74\n"
    "v_mfma_f32_16x16x32_bf16 v[108:111], a[168:171], v[56:59], v[108:111]\n"
    "ds_read_b64_tr_b16 a[210:211], v228 offset:54272\n"
    "v_add_f32 v197, v197, v73\n"
    "v_mfma_f32_16x16x32_bf16 a[32:35], a[232:235], v[144:147], a[32:35]\n"
    "v_exp_f32 v75, v75\n"
    "v_mfma_f32_16x16x32_bf16 a[36:39], a[232:235], v[148:151], a[36:39]\n"
    "ds_read_b64_tr_b16 a[212:213], v229 offset:50176\n"
    "v_add_f32 v196, v196, v74\n"
    "v_mfma_f32_16x16x32_bf16 a[40:43], a[232:235], v[152:155], a[40:43]\n"
    "v_exp_f32 v76, v76\n"
    "v_mfma_f32_16x16x32_bf16 a[44:47], a[232:235], v[156:159], a[44:47]\n"
    "ds_read_b64_tr_b16 a[214:215], v229 offset:54272\n"
    "v_cvt_pk_bf16_f32 v137, v74, v75\n"
    "v_mfma_f32_16x16x32_bf16 v[96:99], a[172:175], v[12:15], v[96:99]\n"
    "v_exp_f32 v77, v77\n"
    "v_mfma_f32_16x16x32_bf16 v[100:103], a[172:175], v[28:31], v[100:103]\n"
    "ds_read_b64_tr_b16 a[216:217], v228 offset:50688\n"
    "v_add_f32 v197, v197, v75\n"
    "v_mfma_f32_16x16x32_bf16 v[104:107], a[172:175], v[44:47], v[104:107]\n"
    "v_exp_f32 v78, v78\n"
    "v_mfma_f32_16x16x32_bf16 v[108:111], a[172:175], v[60:63], v[108:111]\n"
    "ds_read_b64_tr_b16 a[218:219], v228 offset:54784\n"
    "v_add_f32 v198, v198, v76\n"
    "v_mfma_f32_16x16x32_bf16 a[48:51], a[236:239], v[144:147], a[48:51]\n"
    "v_exp_f32 v79, v79\n"
    "v_mfma_f32_16x16x32_bf16 a[52:55], a[236:239], v[148:151], a[52:55]\n"
    "ds_read_b64_tr_b16 a[220:221], v229 offset:50688\n"
    "v_cvt_pk_bf16_f32 v140, v76, v77\n"
    "v_mfma_f32_16x16x32_bf16 a[56:59], a[236:239], v[152:155], a[56:59]\n"
    "v_exp_f32 v80, v80\n"
    "v_mfma_f32_16x16x32_bf16 a[60:63], a[236:239], v[156:159], a[60:63]\n"
    "v_exp_f32 v81, v81\n"
    "v_mfma_f32_16x16x32_bf16 v[180:183], v[200:203], v[148:151], v[180:183]\n"
    "ds_read_b64_tr_b16 a[222:223], v229 offset:54784\n"
    "v_add_f32 v199, v199, v77\n"
    "v_mfma_f32_16x16x32_bf16 v[112:115], a[176:179], v[0:3], v[160:163]\n"
    "v_exp_f32 v82, v82\n"
    "v_mfma_f32_16x16x32_bf16 v[116:119], a[176:179], v[16:19], v[164:167]\n"
    "v_add_f32 v198, v198, v78\n"
    "v_cvt_pk_bf16_f32 v141, v78, v79\n"
    "v_mfma_f32_16x16x32_bf16 v[120:123], a[176:179], v[32:35], v[168:171]\n"
    "v_exp_f32 v83, v83\n"
    "v_mfma_f32_16x16x32_bf16 v[124:127], a[176:179], v[48:51], v[172:175]\n"
    "v_add_f32 v199, v199, v79\n"
    "v_cvt_pk_bf16_f32 v130, v80, v81\n"
    "v_mfma_f32_16x16x32_bf16 a[64:67], a[240:243], v[144:147], a[64:67]\n"
    "v_exp_f32 v84, v84\n"
    "v_mfma_f32_16x16x32_bf16 a[68:71], a[240:243], v[148:151], a[68:71]\n"
    "v_cvt_pk_bf16_f32 v131, v82, v83\n"
    "v_mfma_f32_16x16x32_bf16 a[72:75], a[240:243], v[152:155], a[72:75]\n"
    "v_exp_f32 v85, v85\n"
    "v_mfma_f32_16x16x32_bf16 a[76:79], a[240:243], v[156:159], a[76:79]\n"
    "v_cvt_pk_bf16_f32 v134, v84, v85\n"
    "v_mfma_f32_16x16x32_bf16 v[112:115], a[180:183], v[4:7], v[112:115]\n"
    "v_exp_f32 v86, v86\n"
    "v_mfma_f32_16x16x32_bf16 v[116:119], a[180:183], v[20:23], v[116:119]\n"
    "v_mfma_f32_16x16x32_bf16 v[120:123], a[180:183], v[36:39], v[120:123]\n"
    "v_exp_f32 v87, v87\n"
    "v_mfma_f32_16x16x32_bf16 v[124:127], a[180:183], v[52:55], v[124:127]\n"
    "v_cvt_pk_bf16_f32 v135, v86, v87\n"
    "v_mfma_f32_16x16x32_bf16 a[80:83], a[244:247], v[144:147], a[80:83]\n"
    "v_exp_f32 v88, v88\n"
    "v_mfma_f32_16x16x32_bf16 a[84:87], a[244:247], v[148:151], a[84:87]\n"
    "v_add_f32 v196, v196, v88\n"
    "v_mfma_f32_16x16x32_bf16 a[88:91], a[244:247], v[152:155], a[88:91]\n"
    "v_exp_f32 v89, v89\n"
    "v_mfma_f32_16x16x32_bf16 a[92:95], a[244:247], v[156:159], a[92:95]\n"
    "v_cvt_pk_bf16_f32 v138, v88, v89\n"
    "v_add_f32 v197, v197, v89\n"
    "v_mfma_f32_16x16x32_bf16 v[112:115], a[184:187], v[8:11], v[112:115]\n"
    "v_exp_f32 v90, v90\n"
    "v_mfma_f32_16x16x32_bf16 v[116:119], a[184:187], v[24:27], v[116:119]\n"
    "v_add_f32 v196, v196, v90\n"
    "v_mfma_f32_16x16x32_bf16 v[120:123], a[184:187], v[40:43], v[120:123]\n"
    "v_exp_f32 v91, v91\n"
    "v_mfma_f32_16x16x32_bf16 v[124:127], a[184:187], v[56:59], v[124:127]\n"
    "v_cvt_pk_bf16_f32 v139, v90, v91\n"
    "v_add_f32 v197, v197, v91\n"
    "v_mfma_f32_16x16x32_bf16 a[96:99], a[248:251], v[144:147], a[96:99]\n"
    "v_exp_f32 v92, v92\n"
    "v_mfma_f32_16x16x32_bf16 a[100:103], a[248:251], v[148:151], a[100:103]\n"
    "v_add_f32 v198, v198, v92\n"
    "v_mfma_f32_16x16x32_bf16 a[104:107], a[248:251], v[152:155], a[104:107]\n"
    "v_exp_f32 v93, v93\n"
    "v_mfma_f32_16x16x32_bf16 a[108:111], a[248:251], v[156:159], a[108:111]\n"
    "v_cvt_pk_bf16_f32 v142, v92, v93\n"
    "v_add_f32 v199, v199, v93\n"
    "v_mfma_f32_16x16x32_bf16 v[112:115], a[188:191], v[12:15], v[112:115]\n"
    "v_exp_f32 v94, v94\n"
    "v_mfma_f32_16x16x32_bf16 v[116:119], a[188:191], v[28:31], v[116:119]\n"
    "v_add_f32 v198, v198, v94\n"
    "v_mfma_f32_16x16x32_bf16 v[120:123], a[188:191], v[44:47], v[120:123]\n"
    "v_exp_f32 v95, v95\n"
    "v_mfma_f32_16x16x32_bf16 v[124:127], a[188:191], v[60:63], v[124:127]\n"
    "v_cvt_pk_bf16_f32 v143, v94, v95\n"
    "v_add_f32 v199, v199, v95\n"
    "v_mfma_f32_16x16x32_bf16 a[112:115], a[252:255], v[144:147], a[112:115]\n"
    "v_mfma_f32_16x16x32_bf16 a[116:119], a[252:255], v[148:151], a[116:119]\n"
    "v_mfma_f32_16x16x32_bf16 a[120:123], a[252:255], v[152:155], a[120:123]\n"
    "v_mfma_f32_16x16x32_bf16 a[124:127], a[252:255], v[156:159], a[124:127]\n"
    "s_waitcnt lgkmcnt(0)\n"
    "v_mfma_f32_16x16x32_bf16 a[0:3], a[192:195], v[128:131], a[0:3]\n"
    "v_exp_f32 v96, v96\n"
    "v_mfma_f32_16x16x32_bf16 a[4:7], a[192:195], v[132:135], a[4:7]\n"
    "v_exp_f32 v97, v97\n"
    "v_mfma_f32_16x16x32_bf16 a[8:11], a[192:195], v[136:139], a[8:11]\n"
    "v_exp_f32 v98, v98\n"
    "v_mfma_f32_16x16x32_bf16 a[12:15], a[192:195], v[140:143], a[12:15]\n"
    "v_exp_f32 v99, v99\n"
    "v_mfma_f32_16x16x32_bf16 a[16:19], a[196:199], v[128:131], a[16:19]\n"
    "v_exp_f32 v100, v100\n"
    "v_mfma_f32_16x16x32_bf16 a[20:23], a[196:199], v[132:135], a[20:23]\n"
    "v_exp_f32 v101, v101\n"
    "v_mfma_f32_16x16x32_bf16 a[24:27], a[196:199], v[136:139], a[24:27]\n"
    "v_exp_f32 v102, v102\n"
    "v_mfma_f32_16x16x32_bf16 a[28:31], a[196:199], v[140:143], a[28:31]\n"
    "v_exp_f32 v103, v103\n"
    "v_mfma_f32_16x16x32_bf16 v[176:179], v[200:203], v[128:131], v[176:179]\n"
    "v_exp_f32 v104, v104\n"
    "v_mfma_f32_16x16x32_bf16 a[32:35], a[200:203], v[128:131], a[32:35]\n"
    "v_exp_f32 v105, v105\n"
    "v_mfma_f32_16x16x32_bf16 a[36:39], a[200:203], v[132:135], a[36:39]\n"
    "v_exp_f32 v106, v106\n"
    "v_mfma_f32_16x16x32_bf16 a[40:43], a[200:203], v[136:139], a[40:43]\n"
    "v_exp_f32 v107, v107\n"
    "v_mfma_f32_16x16x32_bf16 a[44:47], a[200:203], v[140:143], a[44:47]\n"
    "v_exp_f32 v108, v108\n"
    "v_mfma_f32_16x16x32_bf16 a[48:51], a[204:207], v[128:131], a[48:51]\n"
    "v_exp_f32 v109, v109\n"
    "v_mfma_f32_16x16x32_bf16 a[52:55], a[204:207], v[132:135], a[52:55]\n"
    "v_exp_f32 v110, v110\n"
    "v_mfma_f32_16x16x32_bf16 a[56:59], a[204:207], v[136:139], a[56:59]\n"
    "v_exp_f32 v111, v111\n"
    "v_exp_f32 v112, v112\n"
    "v_mfma_f32_16x16x32_bf16 a[60:63], a[204:207], v[140:143], a[60:63]\n"
    "v_exp_f32 v113, v113\n"
    "v_mfma_f32_16x16x32_bf16 v[180:183], v[200:203], v[132:135], v[180:183]\n"
    "v_exp_f32 v114, v114\n"
    "v_mfma_f32_16x16x32_bf16 a[64:67], a[208:211], v[128:131], a[64:67]\n"
    "v_exp_f32 v115, v115\n"
    "v_mfma_f32_16x16x32_bf16 a[68:71], a[208:211], v[132:135], a[68:71]\n"
    "v_exp_f32 v116, v116\n"
    "v_mfma_f32_16x16x32_bf16 a[72:75], a[208:211], v[136:139], a[72:75]\n"
    "v_exp_f32 v117, v117\n"
    "v_mfma_f32_16x16x32_bf16 a[76:79], a[208:211], v[140:143], a[76:79]\n"
    "v_exp_f32 v118, v118\n"
    "v_mfma_f32_16x16x32_bf16 a[80:83], a[212:215], v[128:131], a[80:83]\n"
    "v_exp_f32 v119, v119\n"
    "v_mfma_f32_16x16x32_bf16 a[84:87], a[212:215], v[132:135], a[84:87]\n"
    "v_exp_f32 v120, v120\n"
    "v_mfma_f32_16x16x32_bf16 a[88:91], a[212:215], v[136:139], a[88:91]\n"
    "v_exp_f32 v121, v121\n"
    "v_mfma_f32_16x16x32_bf16 a[92:95], a[212:215], v[140:143], a[92:95]\n"
    "v_exp_f32 v122, v122\n"
    "v_mfma_f32_16x16x32_bf16 a[96:99], a[216:219], v[128:131], a[96:99]\n"
    "v_exp_f32 v123, v123\n"
    "v_mfma_f32_16x16x32_bf16 a[100:103], a[216:219], v[132:135], a[100:103]\n"
    "v_exp_f32 v124, v124\n"
    "v_mfma_f32_16x16x32_bf16 a[104:107], a[216:219], v[136:139], a[104:107]\n"
    "v_exp_f32 v125, v125\n"
    "v_mfma_f32_16x16x32_bf16 a[108:111], a[216:219], v[140:143], a[108:111]\n"
    "v_exp_f32 v126, v126\n"
    "v_mfma_f32_16x16x32_bf16 a[112:115], a[220:223], v[128:131], a[112:115]\n"
    "v_exp_f32 v127, v127\n"
    "v_mfma_f32_16x16x32_bf16 a[116:119], a[220:223], v[132:135], a[116:119]\n"
    "ds_read_b64_tr_b16 a[224:225], v228 offset:57344\n"
    "v_cvt_pk_bf16_f32 v144, v96, v97\n"
    "v_mfma_f32_16x16x32_bf16 a[120:123], a[220:223], v[136:139], a[120:123]\n"
    "ds_read_b64_tr_b16 a[226:227], v228 offset:61440\n"
    "v_cvt_pk_bf16_f32 v145, v98, v99\n"
    "v_mfma_f32_16x16x32_bf16 a[124:127], a[220:223], v[140:143], a[124:127]\n"
    "ds_read_b64_tr_b16 a[228:229], v229 offset:57344\n"
    "v_cvt_pk_bf16_f32 v148, v100, v101\n"
    "s_nop 0\n"
    "ds_read_b64_tr_b16 a[230:231], v229 offset:61440\n"
    "ds_read_b64_tr_b16 a[232:233], v228 offset:57856\n"
    "ds_read_b64_tr_b16 a[234:235], v228 offset:61952\n"
    "ds_read_b64_tr_b16 a[236:237], v229 offset:57856\n"
    "ds_read_b64_tr_b16 a[238:239], v229 offset:61952\n"
    "ds_read_b64_tr_b16 a[240:241], v228 offset:58368\n"
    "ds_read_b64_tr_b16 a[242:243], v228 offset:62464\n"
    "ds_read_b64_tr_b16 a[244:245], v229 offset:58368\n"
    "ds_read_b64_tr_b16 a[246:247], v229 offset:62464\n"
    "ds_read_b64_tr_b16 a[248:249], v228 offset:58880\n"
    "ds_read_b64_tr_b16 a[250:251], v228 offset:62976\n"
    "ds_read_b64_tr_b16 a[252:253], v229 offset:58880\n"
    "ds_read_b64_tr_b16 a[254:255], v229 offset:62976\n"
    "v_cvt_pk_bf16_f32 v149, v102, v103\n"
    "v_add_f32 v196, v196, v104\n"
    "v_cvt_pk_bf16_f32 v152, v104, v105\n"
    "v_add_f32 v197, v197, v105\n"
    "v_add_f32 v196, v196, v106\n"
    "v_cvt_pk_bf16_f32 v153, v106, v107\n"
    "v_add_f32 v197, v197, v107\n"
    "v_add_f32 v198, v198, v108\n"
    "v_cvt_pk_bf16_f32 v156, v108, v109\n"
    "v_add_f32 v199, v199, v109\n"
    "v_add_f32 v198, v198, v110\n"
    "v_cvt_pk_bf16_f32 v157, v110, v111\n"
    "v_add_f32 v199, v199, v111\n"
    "v_cvt_pk_bf16_f32 v146, v112, v113\n"
    "v_cvt_pk_bf16_f32 v147, v114, v115\n"
    "v_cvt_pk_bf16_f32 v150, v116, v117\n"
    "v_cvt_pk_bf16_f32 v151, v118, v119\n"
    "v_add_f32 v196, v196, v120\n"
    "v_cvt_pk_bf16_f32 v154, v120, v121\n"
    "v_add_f32 v197, v197, v121\n"
    "v_add_f32 v196, v196, v122\n"
    "v_cvt_pk_bf16_f32 v155, v122, v123\n"
    "v_add_f32 v197, v197, v123\n"
    "v_add_f32 v198, v198, v124\n"
    "v_cvt_pk_bf16_f32 v158, v124, v125\n"
    "v_add_f32 v199, v199, v125\n"
    "v_add_f32 v198, v198, v126\n"
    "v_cvt_pk_bf16_f32 v159, v126, v127\n"
    "v_add_f32 v199, v199, v127\n"
    "s_waitcnt lgkmcnt(0)\n"
    "s_nop 1\n"
    "v_mfma_f32_16x16x32_bf16 v[176:179], v[200:203], v[144:147], v[176:179]\n"
    "s_mov_b32 s64, 0x53800000\n"
    "s_mov_b64 s[70:71], 0\n"
    "v_add_f32 v210, v196, v197\n"
    "v_mov_b32 v214, v210\n"
    "v_mfma_f32_16x16x32_bf16 v[180:183], v[200:203], v[148:151], v[180:183]\n"
    "v_add_f32 v211, v198, v199\n"
    "v_mov_b32 v215, v211\n"
    "v_mfma_f32_16x16x32_bf16 a[0:3], a[224:227], v[144:147], a[0:3]\n"
    "v_permlane16_swap_b32 v210, v214\n"
    "v_add_f32 v210, v210, v214\n"
    "v_mfma_f32_16x16x32_bf16 a[16:19], a[228:231], v[144:147], a[16:19]\n"
    "v_mov_b32 v214, v210\n"
    "v_permlane16_swap_b32 v211, v215\n"
    "v_mfma_f32_16x16x32_bf16 a[32:35], a[232:235], v[144:147], a[32:35]\n"
    "v_permlane32_swap_b32 v210, v214\n"
    "v_add_f32 v210, v210, v214\n"
    "v_mfma_f32_16x16x32_bf16 a[48:51], a[236:239], v[144:147], a[48:51]\n"
    "v_rcp_f32 v206, v210\n"
    "v_mfma_f32_16x16x32_bf16 a[64:67], a[240:243], v[144:147], a[64:67]\n"
    "v_cmp_nge_f32 vcc, s64, v210\n"
    "v_add_f32 v211, v211, v215\n"
    "v_mfma_f32_16x16x32_bf16 a[80:83], a[244:247], v[144:147], a[80:83]\n"
    "v_mov_b32 v215, v211\n"
    "v_mov_b32 v208, v176\n"
    "v_mfma_f32_16x16x32_bf16 a[96:99], a[248:251], v[144:147], a[96:99]\n"
    "s_or_b64 s[70:71], s[70:71], vcc\n"
    "v_permlane32_swap_b32 v211, v215\n"
    "v_add_f32 v211, v211, v215\n"
    "v_mfma_f32_16x16x32_bf16 a[112:115], a[252:255], v[144:147], a[112:115]\n"
    "v_rcp_f32 v207, v211\n"
    "v_mfma_f32_16x16x32_bf16 a[4:7], a[224:227], v[148:151], a[4:7]\n"
    "v_cmp_nge_f32 vcc, s64, v211\n"
    "v_mov_b32 v212, v208\n"
    "v_mfma_f32_16x16x32_bf16 a[20:23], a[228:231], v[148:151], a[20:23]\n"
    "v_mov_b32 v209, v180\n"
    "v_mov_b32 v213, v209\n"
    "v_mfma_f32_16x16x32_bf16 a[36:39], a[232:235], v[148:151], a[36:39]\n"
    "s_or_b64 s[70:71], s[70:71], vcc\n"
    "v_permlane16_swap_b32 v208, v212\n"
    "v_add_f32 v208, v208, v212\n"
    "v_mfma_f32_16x16x32_bf16 a[52:55], a[236:239], v[148:151], a[52:55]\n"
    "v_mov_b32 v212, v208\n"
    "v_permlane16_swap_b32 v209, v213\n"
    "v_mfma_f32_16x16x32_bf16 a[68:71], a[240:243], v[148:151], a[68:71]\n"
    "v_permlane32_swap_b32 v208, v212\n"
    "v_add_f32 v208, v208, v212\n"
    "v_mfma_f32_16x16x32_bf16 a[84:87], a[244:247], v[148:151], a[84:87]\n"
    "v_rcp_f32 v204, v208\n"
    "v_mfma_f32_16x16x32_bf16 a[100:103], a[248:251], v[148:151], a[100:103]\n"
    "v_cmp_nge_f32 vcc, s64, v208\n"
    "v_add_f32 v209, v209, v213\n"
    "v_mfma_f32_16x16x32_bf16 a[116:119], a[252:255], v[148:151], a[116:119]\n"
    "v_mov_b32 v213, v209\n"
    "v_mov_b32 v216, 0\n"
    "v_mfma_f32_16x16x32_bf16 a[8:11], a[224:227], v[152:155], a[8:11]\n"
    "s_or_b64 s[70:71], s[70:71], vcc\n"
    "v_permlane32_swap_b32 v209, v213\n"
    "v_add_f32 v209, v209, v213\n"
    "v_mfma_f32_16x16x32_bf16 a[24:27], a[228:231], v[152:155], a[24:27]\n"
    "v_rcp_f32 v205, v209\n"
    "v_mfma_f32_16x16x32_bf16 a[40:43], a[232:235], v[152:155], a[40:43]\n"
    "v_cmp_nge_f32 vcc, s64, v209\n"
    "v_mov_b32 v217, 0\n"
    "v_mfma_f32_16x16x32_bf16 a[56:59], a[236:239], v[152:155], a[56:59]\n"
    "v_accvgpr_read_b32 v64, a0\n"
    "v_accvgpr_read_b32 v65, a1\n"
    "v_mfma_f32_16x16x32_bf16 a[72:75], a[240:243], v[152:155], a[72:75]\n"
    "s_or_b64 s[70:71], s[70:71], vcc\n"
    "s_mov_b32 s65, 0\n"
    "v_accvgpr_read_b32 v66, a2\n"
    "v_accvgpr_read_b32 v67, a3\n"
    "v_mfma_f32_16x16x32_bf16 a[88:91], a[244:247], v[152:155], a[88:91]\n"
    "v_accvgpr_read_b32 v68, a16\n"
    "v_accvgpr_read_b32 v69, a17\n"
    "v_mfma_f32_16x16x32_bf16 a[104:107], a[248:251], v[152:155], a[104:107]\n"
    "v_accvgpr_read_b32 v70, a18\n"
    "v_accvgpr_read_b32 v71, a19\n"
    "v_mfma_f32_16x16x32_bf16 a[120:123], a[252:255], v[152:155], a[120:123]\n"
    "v_mul_f32 v64, v64, v204\n"
    "v_mul_f32 v65, v65, v204\n"
    "v_mfma_f32_16x16x32_bf16 a[12:15], a[224:227], v[156:159], a[12:15]\n"
    "v_mul_f32 v66, v66, v204\n"
    "v_mul_f32 v67, v67, v204\n"
    "v_mfma_f32_16x16x32_bf16 a[28:31], a[228:231], v[156:159], a[28:31]\n"
    "v_mul_f32 v68, v68, v204\n"
    "v_mul_f32 v69, v69, v204\n"
    "v_mfma_f32_16x16x32_bf16 a[44:47], a[232:235], v[156:159], a[44:47]\n"
    "v_mul_f32 v70, v70, v204\n"
    "v_mul_f32 v71, v71, v204\n"
    "v_mfma_f32_16x16x32_bf16 a[60:63], a[236:239], v[156:159], a[60:63]\n"
    "v_fma_f32 v216, v64, 0, v216\n"
    "v_fma_f32 v217, v65, 0, v217\n"
    "v_mfma_f32_16x16x32_bf16 a[76:79], a[240:243], v[156:159], a[76:79]\n"
    "v_fma_f32 v216, v66, 0, v216\n"
    "v_fma_f32 v217, v67, 0, v217\n"
    "v_mfma_f32_16x16x32_bf16 a[92:95], a[244:247], v[156:159], a[92:95]\n"
    "v_fma_f32 v216, v68, 0, v216\n"
    "v_fma_f32 v217, v69, 0, v217\n"
    "v_mfma_f32_16x16x32_bf16 a[108:111], a[248:251], v[156:159], a[108:111]\n"
    "v_fma_f32 v216, v70, 0, v216\n"
    "v_fma_f32 v217, v71, 0, v217\n"
    "v_mfma_f32_16x16x32_bf16 a[124:127], a[252:255], v[156:159], a[124:127]\n"
    "v_cvt_pk_bf16_f32 v96, v64, v65\n"
    "v_cvt_pk_bf16_f32 v97, v66, v67\n"
    "v_cvt_pk_bf16_f32 v98, v68, v69\n"
    "v_cvt_pk_bf16_f32 v99, v70, v71\n"
    "buffer_store_dwordx4 v[96:99], v238, s[56:59], s65 offen sc1\n"
    "v_accvgpr_read_b32 v72, a32\n"
    "v_accvgpr_read_b32 v73, a33\n"
    "v_accvgpr_read_b32 v74, a34\n"
    "v_accvgpr_read_b32 v75, a35\n"
    "v_accvgpr_read_b32 v76, a48\n"
    "v_accvgpr_read_b32 v77, a49\n"
    "v_accvgpr_read_b32 v78, a50\n"
    "v_accvgpr_read_b32 v79, a51\n"
    "v_mul_f32 v72, v72, v204\n"
    "v_mul_f32 v73, v73, v204\n"
    "v_mul_f32 v74, v74, v204\n"
    "v_mul_f32 v75, v75, v204\n"
    "v_mul_f32 v76, v76, v204\n"
    "v_mul_f32 v77, v77, v204\n"
    "v_mul_f32 v78, v78, v204\n"
    "v_mul_f32 v79, v79, v204\n"
    "v_fma_f32 v216, v72, 0, v216\n"
    "v_fma_f32 v217, v73, 0, v217\n"
    "v_fma_f32 v216, v74, 0, v216\n"
    "v_fma_f32 v217, v75, 0, v217\n"
    "v_fma_f32 v216, v76, 0, v216\n"
    "v_fma_f32 v217, v77, 0, v217\n"
    "v_fma_f32 v216, v78, 0, v216\n"
    "v_fma_f32 v217, v79, 0, v217\n"
    "v_cvt_pk_bf16_f32 v100, v72, v73\n"
    "v_cvt_pk_bf16_f32 v101, v74, v75\n"
    "v_cvt_pk_bf16_f32 v102, v76, v77\n"
    "v_cvt_pk_bf16_f32 v103, v78, v79\n"
    "buffer_store_dwordx4 v[100:103], v238, s[56:59], s65 offen offset:1024 sc1\n"
    "v_accvgpr_read_b32 v80, a64\n"
    "v_accvgpr_read_b32 v81, a65\n"
    "v_accvgpr_read_b32 v82, a66\n"
    "v_accvgpr_read_b32 v83, a67\n"
    "v_accvgpr_read_b32 v84, a80\n"
    "v_accvgpr_read_b32 v85, a81\n"
    "v_accvgpr_read_b32 v86, a82\n"
    "v_accvgpr_read_b32 v87, a83\n"
    "v_mul_f32 v80, v80, v204\n"
    "v_mul_f32 v81, v81, v204\n"
    "v_mul_f32 v82, v82, v204\n"
    "v_mul_f32 v83, v83, v204\n"
    "v_mul_f32 v84, v84, v204\n"
    "v_mul_f32 v85, v85, v204\n"
    "v_mul_f32 v86, v86, v204\n"
    "v_mul_f32 v87, v87, v204\n"
    "v_fma_f32 v216, v80, 0, v216\n"
    "v_fma_f32 v217, v81, 0, v217\n"
    "v_fma_f32 v216, v82, 0, v216\n"
    "v_fma_f32 v217, v83, 0, v217\n"
    "v_fma_f32 v216, v84, 0, v216\n"
    "v_fma_f32 v217, v85, 0, v217\n"
    "v_fma_f32 v216, v86, 0, v216\n"
    "v_fma_f32 v217, v87, 0, v217\n"
    "v_cvt_pk_bf16_f32 v104, v80, v81\n"
    "v_cvt_pk_bf16_f32 v105, v82, v83\n"
    "v_cvt_pk_bf16_f32 v106, v84, v85\n"
    "v_cvt_pk_bf16_f32 v107, v86, v87\n"
    "buffer_store_dwordx4 v[104:107], v238, s[56:59], s65 offen offset:2048 sc1\n"
    "v_accvgpr_read_b32 v88, a96\n"
    "v_accvgpr_read_b32 v89, a97\n"
    "v_accvgpr_read_b32 v90, a98\n"
    "v_accvgpr_read_b32 v91, a99\n"
    "v_accvgpr_read_b32 v92, a112\n"
    "v_accvgpr_read_b32 v93, a113\n"
    "v_accvgpr_read_b32 v94, a114\n"
    "v_accvgpr_read_b32 v95, a115\n"
    "v_mul_f32 v88, v88, v204\n"
    "v_mul_f32 v89, v89, v204\n"
    "v_mul_f32 v90, v90, v204\n"
    "v_mul_f32 v91, v91, v204\n"
    "v_mul_f32 v92, v92, v204\n"
    "v_mul_f32 v93, v93, v204\n"
    "v_mul_f32 v94, v94, v204\n"
    "v_mul_f32 v95, v95, v204\n"
    "v_fma_f32 v216, v88, 0, v216\n"
    "v_fma_f32 v217, v89, 0, v217\n"
    "v_fma_f32 v216, v90, 0, v216\n"
    "v_fma_f32 v217, v91, 0, v217\n"
    "v_fma_f32 v216, v92, 0, v216\n"
    "v_fma_f32 v217, v93, 0, v217\n"
    "v_fma_f32 v216, v94, 0, v216\n"
    "v_fma_f32 v217, v95, 0, v217\n"
    "v_cvt_pk_bf16_f32 v108, v88, v89\n"
    "v_cvt_pk_bf16_f32 v109, v90, v91\n"
    "v_cvt_pk_bf16_f32 v110, v92, v93\n"
    "v_cvt_pk_bf16_f32 v111, v94, v95\n"
    "buffer_store_dwordx4 v[108:111], v238, s[56:59], s65 offen offset:3072 sc1\n"
    "s_mov_b32 s66, 131072\n"
    "v_accvgpr_read_b32 v64, a4\n"
    "v_accvgpr_read_b32 v65, a5\n"
    "v_accvgpr_read_b32 v66, a6\n"
    "v_accvgpr_read_b32 v67, a7\n"
    "v_accvgpr_read_b32 v68, a20\n"
    "v_accvgpr_read_b32 v69, a21\n"
    "v_accvgpr_read_b32 v70, a22\n"
    "v_accvgpr_read_b32 v71, a23\n"
    "v_mul_f32 v64, v64, v205\n"
    "v_mul_f32 v65, v65, v205\n"
    "v_mul_f32 v66, v66, v205\n"
    "v_mul_f32 v67, v67, v205\n"
    "v_mul_f32 v68, v68, v205\n"
    "v_mul_f32 v69, v69, v205\n"
    "v_mul_f32 v70, v70, v205\n"
    "v_mul_f32 v71, v71, v205\n"
    "v_fma_f32 v216, v64, 0, v216\n"
    "v_fma_f32 v217, v65, 0, v217\n"
    "v_fma_f32 v216, v66, 0, v216\n"
    "v_fma_f32 v217, v67, 0, v217\n"
    "v_fma_f32 v216, v68, 0, v216\n"
    "v_fma_f32 v217, v69, 0, v217\n"
    "v_fma_f32 v216, v70, 0, v216\n"
    "v_fma_f32 v217, v71, 0, v217\n"
    "v_cvt_pk_bf16_f32 v112, v64, v65\n"
    "v_cvt_pk_bf16_f32 v113, v66, v67\n"
    "v_cvt_pk_bf16_f32 v114, v68, v69\n"
    "v_cvt_pk_bf16_f32 v115, v70, v71\n"
    "buffer_store_dwordx4 v[112:115], v238, s[56:59], s66 offen sc1\n"
    "v_accvgpr_read_b32 v72, a36\n"
    "v_accvgpr_read_b32 v73, a37\n"
    "v_accvgpr_read_b32 v74, a38\n"
    "v_accvgpr_read_b32 v75, a39\n"
    "v_accvgpr_read_b32 v76, a52\n"
    "v_accvgpr_read_b32 v77, a53\n"
    "v_accvgpr_read_b32 v78, a54\n"
    "v_accvgpr_read_b32 v79, a55\n"
    "v_mul_f32 v72, v72, v205\n"
    "v_mul_f32 v73, v73, v205\n"
    "v_mul_f32 v74, v74, v205\n"
    "v_mul_f32 v75, v75, v205\n"
    "v_mul_f32 v76, v76, v205\n"
    "v_mul_f32 v77, v77, v205\n"
    "v_mul_f32 v78, v78, v205\n"
    "v_mul_f32 v79, v79, v205\n"
    "v_fma_f32 v216, v72, 0, v216\n"
    "v_fma_f32 v217, v73, 0, v217\n"
    "v_fma_f32 v216, v74, 0, v216\n"
    "v_fma_f32 v217, v75, 0, v217\n"
    "v_fma_f32 v216, v76, 0, v216\n"
    "v_fma_f32 v217, v77, 0, v217\n"
    "v_fma_f32 v216, v78, 0, v216\n"
    "v_fma_f32 v217, v79, 0, v217\n"
    "v_cvt_pk_bf16_f32 v116, v72, v73\n"
    "v_cvt_pk_bf16_f32 v117, v74, v75\n"
    "v_cvt_pk_bf16_f32 v118, v76, v77\n"
    "v_cvt_pk_bf16_f32 v119, v78, v79\n"
    "buffer_store_dwordx4 v[116:119], v238, s[56:59], s66 offen offset:1024 sc1\n"
    "v_accvgpr_read_b32 v80, a68\n"
    "v_accvgpr_read_b32 v81, a69\n"
    "v_accvgpr_read_b32 v82, a70\n"
    "v_accvgpr_read_b32 v83, a71\n"
    "v_accvgpr_read_b32 v84, a84\n"
    "v_accvgpr_read_b32 v85, a85\n"
    "v_accvgpr_read_b32 v86, a86\n"
    "v_accvgpr_read_b32 v87, a87\n"
    "v_mul_f32 v80, v80, v205\n"
    "v_mul_f32 v81, v81, v205\n"
    "v_mul_f32 v82, v82, v205\n"
    "v_mul_f32 v83, v83, v205\n"
    "v_mul_f32 v84, v84, v205\n"
    "v_mul_f32 v85, v85, v205\n"
    "v_mul_f32 v86, v86, v205\n"
    "v_mul_f32 v87, v87, v205\n"
    "v_fma_f32 v216, v80, 0, v216\n"
    "v_fma_f32 v217, v81, 0, v217\n"
    "v_fma_f32 v216, v82, 0, v216\n"
    "v_fma_f32 v217, v83, 0, v217\n"
    "v_fma_f32 v216, v84, 0, v216\n"
    "v_fma_f32 v217, v85, 0, v217\n"
    "v_fma_f32 v216, v86, 0, v216\n"
    "v_fma_f32 v217, v87, 0, v217\n"
    "v_cvt_pk_bf16_f32 v120, v80, v81\n"
    "v_cvt_pk_bf16_f32 v121, v82, v83\n"
    "v_cvt_pk_bf16_f32 v122, v84, v85\n"
    "v_cvt_pk_bf16_f32 v123, v86, v87\n"
    "buffer_store_dwordx4 v[120:123], v238, s[56:59], s66 offen offset:2048 sc1\n"
    "v_accvgpr_read_b32 v88, a100\n"
    "v_accvgpr_read_b32 v89, a101\n"
    "v_accvgpr_read_b32 v90, a102\n"
    "v_accvgpr_read_b32 v91, a103\n"
    "v_accvgpr_read_b32 v92, a116\n"
    "v_accvgpr_read_b32 v93, a117\n"
    "v_accvgpr_read_b32 v94, a118\n"
    "v_accvgpr_read_b32 v95, a119\n"
    "v_mul_f32 v88, v88, v205\n"
    "v_mul_f32 v89, v89, v205\n"
    "v_mul_f32 v90, v90, v205\n"
    "v_mul_f32 v91, v91, v205\n"
    "v_mul_f32 v92, v92, v205\n"
    "v_mul_f32 v93, v93, v205\n"
    "v_mul_f32 v94, v94, v205\n"
    "v_mul_f32 v95, v95, v205\n"
    "v_fma_f32 v216, v88, 0, v216\n"
    "v_fma_f32 v217, v89, 0, v217\n"
    "v_fma_f32 v216, v90, 0, v216\n"
    "v_fma_f32 v217, v91, 0, v217\n"
    "v_fma_f32 v216, v92, 0, v216\n"
    "v_fma_f32 v217, v93, 0, v217\n"
    "v_fma_f32 v216, v94, 0, v216\n"
    "v_fma_f32 v217, v95, 0, v217\n"
    "v_cvt_pk_bf16_f32 v124, v88, v89\n"
    "v_cvt_pk_bf16_f32 v125, v90, v91\n"
    "v_cvt_pk_bf16_f32 v126, v92, v93\n"
    "v_cvt_pk_bf16_f32 v127, v94, v95\n"
    "buffer_store_dwordx4 v[124:127], v238, s[56:59], s66 offen offset:3072 sc1\n"
    "s_mov_b32 s65, 262144\n"
    "v_accvgpr_read_b32 v64, a8\n"
    "v_accvgpr_read_b32 v65, a9\n"
    "v_accvgpr_read_b32 v66, a10\n"
    "v_accvgpr_read_b32 v67, a11\n"
    "v_accvgpr_read_b32 v68, a24\n"
    "v_accvgpr_read_b32 v69, a25\n"
    "v_accvgpr_read_b32 v70, a26\n"
    "v_accvgpr_read_b32 v71, a27\n"
    "v_mul_f32 v64, v64, v206\n"
    "v_mul_f32 v65, v65, v206\n"
    "v_mul_f32 v66, v66, v206\n"
    "v_mul_f32 v67, v67, v206\n"
    "v_mul_f32 v68, v68, v206\n"
    "v_mul_f32 v69, v69, v206\n"
    "v_mul_f32 v70, v70, v206\n"
    "v_mul_f32 v71, v71, v206\n"
    "v_fma_f32 v216, v64, 0, v216\n"
    "v_fma_f32 v217, v65, 0, v217\n"
    "v_fma_f32 v216, v66, 0, v216\n"
    "v_fma_f32 v217, v67, 0, v217\n"
    "v_fma_f32 v216, v68, 0, v216\n"
    "v_fma_f32 v217, v69, 0, v217\n"
    "v_fma_f32 v216, v70, 0, v216\n"
    "v_fma_f32 v217, v71, 0, v217\n"
    "v_cvt_pk_bf16_f32 v96, v64, v65\n"
    "v_cvt_pk_bf16_f32 v97, v66, v67\n"
    "v_cvt_pk_bf16_f32 v98, v68, v69\n"
    "v_cvt_pk_bf16_f32 v99, v70, v71\n"
    "buffer_store_dwordx4 v[96:99], v238, s[56:59], s65 offen sc1\n"
    "v_accvgpr_read_b32 v72, a40\n"
    "v_accvgpr_read_b32 v73, a41\n"
    "v_accvgpr_read_b32 v74, a42\n"
    "v_accvgpr_read_b32 v75, a43\n"
    "v_accvgpr_read_b32 v76, a56\n"
    "v_accvgpr_read_b32 v77, a57\n"
    "v_accvgpr_read_b32 v78, a58\n"
    "v_accvgpr_read_b32 v79, a59\n"
    "v_mul_f32 v72, v72, v206\n"
    "v_mul_f32 v73, v73, v206\n"
    "v_mul_f32 v74, v74, v206\n"
    "v_mul_f32 v75, v75, v206\n"
    "v_mul_f32 v76, v76, v206\n"
    "v_mul_f32 v77, v77, v206\n"
    "v_mul_f32 v78, v78, v206\n"
    "v_mul_f32 v79, v79, v206\n"
    "v_fma_f32 v216, v72, 0, v216\n"
    "v_fma_f32 v217, v73, 0, v217\n"
    "v_fma_f32 v216, v74, 0, v216\n"
    "v_fma_f32 v217, v75, 0, v217\n"
    "v_fma_f32 v216, v76, 0, v216\n"
    "v_fma_f32 v217, v77, 0, v217\n"
    "v_fma_f32 v216, v78, 0, v216\n"
    "v_fma_f32 v217, v79, 0, v217\n"
    "v_cvt_pk_bf16_f32 v100, v72, v73\n"
    "v_cvt_pk_bf16_f32 v101, v74, v75\n"
    "v_cvt_pk_bf16_f32 v102, v76, v77\n"
    "v_cvt_pk_bf16_f32 v103, v78, v79\n"
    "buffer_store_dwordx4 v[100:103], v238, s[56:59], s65 offen offset:1024 sc1\n"
    "v_accvgpr_read_b32 v80, a72\n"
    "v_accvgpr_read_b32 v81, a73\n"
    "v_accvgpr_read_b32 v82, a74\n"
    "v_accvgpr_read_b32 v83, a75\n"
    "v_accvgpr_read_b32 v84, a88\n"
    "v_accvgpr_read_b32 v85, a89\n"
    "v_accvgpr_read_b32 v86, a90\n"
    "v_accvgpr_read_b32 v87, a91\n"
    "v_mul_f32 v80, v80, v206\n"
    "v_mul_f32 v81, v81, v206\n"
    "v_mul_f32 v82, v82, v206\n"
    "v_mul_f32 v83, v83, v206\n"
    "v_mul_f32 v84, v84, v206\n"
    "v_mul_f32 v85, v85, v206\n"
    "v_mul_f32 v86, v86, v206\n"
    "v_mul_f32 v87, v87, v206\n"
    "v_fma_f32 v216, v80, 0, v216\n"
    "v_fma_f32 v217, v81, 0, v217\n"
    "v_fma_f32 v216, v82, 0, v216\n"
    "v_fma_f32 v217, v83, 0, v217\n"
    "v_fma_f32 v216, v84, 0, v216\n"
    "v_fma_f32 v217, v85, 0, v217\n"
    "v_fma_f32 v216, v86, 0, v216\n"
    "v_fma_f32 v217, v87, 0, v217\n"
    "v_cvt_pk_bf16_f32 v104, v80, v81\n"
    "v_cvt_pk_bf16_f32 v105, v82, v83\n"
    "v_cvt_pk_bf16_f32 v106, v84, v85\n"
    "v_cvt_pk_bf16_f32 v107, v86, v87\n"
    "buffer_store_dwordx4 v[104:107], v238, s[56:59], s65 offen offset:2048 sc1\n"
    "v_accvgpr_read_b32 v88, a104\n"
    "v_accvgpr_read_b32 v89, a105\n"
    "v_accvgpr_read_b32 v90, a106\n"
    "v_accvgpr_read_b32 v91, a107\n"
    "v_accvgpr_read_b32 v92, a120\n"
    "v_accvgpr_read_b32 v93, a121\n"
    "v_accvgpr_read_b32 v94, a122\n"
    "v_accvgpr_read_b32 v95, a123\n"
    "v_mul_f32 v88, v88, v206\n"
    "v_mul_f32 v89, v89, v206\n"
    "v_mul_f32 v90, v90, v206\n"
    "v_mul_f32 v91, v91, v206\n"
    "v_mul_f32 v92, v92, v206\n"
    "v_mul_f32 v93, v93, v206\n"
    "v_mul_f32 v94, v94, v206\n"
    "v_mul_f32 v95, v95, v206\n"
    "v_fma_f32 v216, v88, 0, v216\n"
    "v_fma_f32 v217, v89, 0, v217\n"
    "v_fma_f32 v216, v90, 0, v216\n"
    "v_fma_f32 v217, v91, 0, v217\n"
    "v_fma_f32 v216, v92, 0, v216\n"
    "v_fma_f32 v217, v93, 0, v217\n"
    "v_fma_f32 v216, v94, 0, v216\n"
    "v_fma_f32 v217, v95, 0, v217\n"
    "v_cvt_pk_bf16_f32 v108, v88, v89\n"
    "v_cvt_pk_bf16_f32 v109, v90, v91\n"
    "v_cvt_pk_bf16_f32 v110, v92, v93\n"
    "v_cvt_pk_bf16_f32 v111, v94, v95\n"
    "buffer_store_dwordx4 v[108:111], v238, s[56:59], s65 offen offset:3072 sc1\n"
    "s_mov_b32 s66, 393216\n"
    "v_accvgpr_read_b32 v64, a12\n"
    "v_accvgpr_read_b32 v65, a13\n"
    "v_accvgpr_read_b32 v66, a14\n"
    "v_accvgpr_read_b32 v67, a15\n"
    "v_accvgpr_read_b32 v68, a28\n"
    "v_accvgpr_read_b32 v69, a29\n"
    "v_accvgpr_read_b32 v70, a30\n"
    "v_accvgpr_read_b32 v71, a31\n"
    "v_mul_f32 v64, v64, v207\n"
    "v_mul_f32 v65, v65, v207\n"
    "v_mul_f32 v66, v66, v207\n"
    "v_mul_f32 v67, v67, v207\n"
    "v_mul_f32 v68, v68, v207\n"
    "v_mul_f32 v69, v69, v207\n"
    "v_mul_f32 v70, v70, v207\n"
    "v_mul_f32 v71, v71, v207\n"
    "v_fma_f32 v216, v64, 0, v216\n"
    "v_fma_f32 v217, v65, 0, v217\n"
    "v_fma_f32 v216, v66, 0, v216\n"
    "v_fma_f32 v217, v67, 0, v217\n"
    "v_fma_f32 v216, v68, 0, v216\n"
    "v_fma_f32 v217, v69, 0, v217\n"
    "v_fma_f32 v216, v70, 0, v216\n"
    "v_fma_f32 v217, v71, 0, v217\n"
    "v_cvt_pk_bf16_f32 v112, v64, v65\n"
    "v_cvt_pk_bf16_f32 v113, v66, v67\n"
    "v_cvt_pk_bf16_f32 v114, v68, v69\n"
    "v_cvt_pk_bf16_f32 v115, v70, v71\n"
    "buffer_store_dwordx4 v[112:115], v238, s[56:59], s66 offen sc1\n"
    "v_accvgpr_read_b32 v72, a44\n"
    "v_accvgpr_read_b32 v73, a45\n"
    "v_accvgpr_read_b32 v74, a46\n"
    "v_accvgpr_read_b32 v75, a47\n"
    "v_accvgpr_read_b32 v76, a60\n"
    "v_accvgpr_read_b32 v77, a61\n"
    "v_accvgpr_read_b32 v78, a62\n"
    "v_accvgpr_read_b32 v79, a63\n"
    "v_mul_f32 v72, v72, v207\n"
    "v_mul_f32 v73, v73, v207\n"
    "v_mul_f32 v74, v74, v207\n"
    "v_mul_f32 v75, v75, v207\n"
    "v_mul_f32 v76, v76, v207\n"
    "v_mul_f32 v77, v77, v207\n"
    "v_mul_f32 v78, v78, v207\n"
    "v_mul_f32 v79, v79, v207\n"
    "v_fma_f32 v216, v72, 0, v216\n"
    "v_fma_f32 v217, v73, 0, v217\n"
    "v_fma_f32 v216, v74, 0, v216\n"
    "v_fma_f32 v217, v75, 0, v217\n"
    "v_fma_f32 v216, v76, 0, v216\n"
    "v_fma_f32 v217, v77, 0, v217\n"
    "v_fma_f32 v216, v78, 0, v216\n"
    "v_fma_f32 v217, v79, 0, v217\n"
    "v_cvt_pk_bf16_f32 v116, v72, v73\n"
    "v_cvt_pk_bf16_f32 v117, v74, v75\n"
    "v_cvt_pk_bf16_f32 v118, v76, v77\n"
    "v_cvt_pk_bf16_f32 v119, v78, v79\n"
    "buffer_store_dwordx4 v[116:119], v238, s[56:59], s66 offen offset:1024 sc1\n"
    "v_accvgpr_read_b32 v80, a76\n"
    "v_accvgpr_read_b32 v81, a77\n"
    "v_accvgpr_read_b32 v82, a78\n"
    "v_accvgpr_read_b32 v83, a79\n"
    "v_accvgpr_read_b32 v84, a92\n"
    "v_accvgpr_read_b32 v85, a93\n"
    "v_accvgpr_read_b32 v86, a94\n"
    "v_accvgpr_read_b32 v87, a95\n"
    "v_mul_f32 v80, v80, v207\n"
    "v_mul_f32 v81, v81, v207\n"
    "v_mul_f32 v82, v82, v207\n"
    "v_mul_f32 v83, v83, v207\n"
    "v_mul_f32 v84, v84, v207\n"
    "v_mul_f32 v85, v85, v207\n"
    "v_mul_f32 v86, v86, v207\n"
    "v_mul_f32 v87, v87, v207\n"
    "v_fma_f32 v216, v80, 0, v216\n"
    "v_fma_f32 v217, v81, 0, v217\n"
    "v_fma_f32 v216, v82, 0, v216\n"
    "v_fma_f32 v217, v83, 0, v217\n"
    "v_fma_f32 v216, v84, 0, v216\n"
    "v_fma_f32 v217, v85, 0, v217\n"
    "v_fma_f32 v216, v86, 0, v216\n"
    "v_fma_f32 v217, v87, 0, v217\n"
    "v_cvt_pk_bf16_f32 v120, v80, v81\n"
    "v_cvt_pk_bf16_f32 v121, v82, v83\n"
    "v_cvt_pk_bf16_f32 v122, v84, v85\n"
    "v_cvt_pk_bf16_f32 v123, v86, v87\n"
    "buffer_store_dwordx4 v[120:123], v238, s[56:59], s66 offen offset:2048 sc1\n"
    "v_accvgpr_read_b32 v88, a108\n"
    "v_accvgpr_read_b32 v89, a109\n"
    "v_accvgpr_read_b32 v90, a110\n"
    "v_accvgpr_read_b32 v91, a111\n"
    "v_accvgpr_read_b32 v92, a124\n"
    "v_accvgpr_read_b32 v93, a125\n"
    "v_accvgpr_read_b32 v94, a126\n"
    "v_accvgpr_read_b32 v95, a127\n"
    "v_mul_f32 v88, v88, v207\n"
    "v_mul_f32 v89, v89, v207\n"
    "v_mul_f32 v90, v90, v207\n"
    "v_mul_f32 v91, v91, v207\n"
    "v_mul_f32 v92, v92, v207\n"
    "v_mul_f32 v93, v93, v207\n"
    "v_mul_f32 v94, v94, v207\n"
    "v_mul_f32 v95, v95, v207\n"
    "v_fma_f32 v216, v88, 0, v216\n"
    "v_fma_f32 v217, v89, 0, v217\n"
    "v_fma_f32 v216, v90, 0, v216\n"
    "v_fma_f32 v217, v91, 0, v217\n"
    "v_fma_f32 v216, v92, 0, v216\n"
    "v_fma_f32 v217, v93, 0, v217\n"
    "v_fma_f32 v216, v94, 0, v216\n"
    "v_fma_f32 v217, v95, 0, v217\n"
    "v_cvt_pk_bf16_f32 v124, v88, v89\n"
    "v_cvt_pk_bf16_f32 v125, v90, v91\n"
    "v_cvt_pk_bf16_f32 v126, v92, v93\n"
    "v_cvt_pk_bf16_f32 v127, v94, v95\n"
    "buffer_store_dwordx4 v[124:127], v238, s[56:59], s66 offen offset:3072 sc1\n"
    "v_add_f32 v216, v216, v217\n"
    "v_cmp_u_f32 vcc, v216, v216\n"
    "s_nop 0\n"
    "s_nop 0\n"
    "s_nop 0\n"
    "s_nop 0\n"
    "s_nop 0\n"
    "s_or_b64 s[70:71], s[70:71], vcc\n"
    "s_or_b32 %[flag], s70, s71\n"

    : [flag] "=&s"(flag)
    : [wq] "s"(p_wq), [hf] "s"(p_hf), [tab] "s"((uint64_t)tab), [kc] "s"(p_k), [vc] "s"(p_v), [of] "s"(p_of), [wv] "s"(wv), [lds] "s"(ldsb),
      [pf0] "s"(pf0), [pf1] "s"(pf1), [pf2] "s"(pf2), [pf3] "s"(pf3), [pf4] "s"(pf4)
    : "memory", "vcc", "scc",
      "v0","v1","v2","v3","v4","v5","v6","v7","v8","v9","v10","v11","v12","v13","v14","v15","v16","v17","v18","v19","v20","v21","v22","v23","v24","v25","v26","v27","v28","v29","v30","v31","v32","v33","v34","v35","v36","v37","v38","v39","v40","v41","v42","v43","v44","v45","v46","v47","v48","v49","v50","v51","v52","v53","v54","v55","v56","v57","v58","v59","v60","v61","v62","v63","v64","v65","v66","v67","v68","v69","v70","v71","v72","v73","v74","v75","v76","v77","v78","v79","v80","v81","v82","v83","v84","v85","v86","v87","v88","v89","v90","v91","v92","v93","v94","v95","v96","v97","v98","v99","v100","v101","v102","v103","v104","v105","v106","v107","v108","v109","v110","v111","v112","v113","v114","v115","v116","v117","v118","v119","v120","v121","v122","v123","v124","v125","v126","v127","v128","v129","v130","v131","v132","v133","v134","v135","v136","v137","v138","v139","v140","v141","v142","v143","v144","v145","v146","v147","v148","v149","v150","v151","v152","v153","v154","v155","v156","v157","v158","v159","v160","v161","v162","v163","v164","v165","v166","v167","v168","v169","v170","v171","v172","v173","v174","v175","v176","v177","v178","v179","v180","v181","v182","v183","v184","v185","v186","v187","v188","v189","v190","v191","v192","v193","v194","v195","v196","v197","v198","v199","v200","v201","v202","v203","v204","v205","v206","v207","v208","v209","v210","v211","v212","v213","v214","v215","v216","v217","v218","v219","v220","v221","v222","v223","v224","v225","v226","v227","v228","v229","v230","v231","v232","v233","v234","v235","v236","v237","v238","v239","v240","v241","v242","v243","v244","v245","v246","v247",
      "a0","a1","a2","a3","a4","a5","a6","a7","a8","a9","a10","a11","a12","a13","a14","a15","a16","a17","a18","a19","a20","a21","a22","a23","a24","a25","a26","a27","a28","a29","a30","a31","a32","a33","a34","a35","a36","a37","a38","a39","a40","a41","a42","a43","a44","a45","a46","a47","a48","a49","a50","a51","a52","a53","a54","a55","a56","a57","a58","a59","a60","a61","a62","a63","a64","a65","a66","a67","a68","a69","a70","a71","a72","a73","a74","a75","a76","a77","a78","a79","a80","a81","a82","a83","a84","a85","a86","a87","a88","a89","a90","a91","a92","a93","a94","a95","a96","a97","a98","a99","a100","a101","a102","a103","a104","a105","a106","a107","a108","a109","a110","a111","a112","a113","a114","a115","a116","a117","a118","a119","a120","a121","a122","a123","a124","a125","a126","a127","a128","a129","a130","a131","a132","a133","a134","a135","a136","a137","a138","a139","a140","a141","a142","a143","a144","a145","a146","a147","a148","a149","a150","a151","a152","a153","a154","a155","a156","a157","a158","a159","a160","a161","a162","a163","a164","a165","a166","a167","a168","a169","a170","a171","a172","a173","a174","a175","a176","a177","a178","a179","a180","a181","a182","a183","a184","a185","a186","a187","a188","a189","a190","a191","a192","a193","a194","a195","a196","a197","a198","a199","a200","a201","a202","a203","a204","a205","a206","a207","a208","a209","a210","a211","a212","a213","a214","a215","a216","a217","a218","a219","a220","a221","a222","a223","a224","a225","a226","a227","a228","a229","a230","a231","a232","a233","a234","a235","a236","a237","a238","a239","a240","a241","a242","a243","a244","a245","a246","a247","a248","a249","a250","a251","a252","a253","a254","a255",
      "s40","s41","s42","s43","s44","s45","s46","s47","s48","s49","s50","s51","s52","s53","s54","s55","s56","s57","s58","s59","s60","s61","s62","s63","s64","s65","s66","s67","s68","s69","s70","s71");
  if (flag) {
    asm volatile("s_waitcnt vmcnt(0)" ::: "memory");
    attn_slow_wave(hf, tab, wq, kc, vc, of, head, rowblk0);
  }
}
extern "C" __global__ __launch_bounds__(512, 2) void adaln_oproj(const u16* __restrict__ of, const u16* __restrict__ wo, u16* __restrict__ pp) {
  extern __shared__ __attribute__((aligned(16))) char smem[];
  const int b = blockIdx.x, ksl = (b & 7) >> 1, rb = (b & 1) * 8 + (b >> 5), cb = (b >> 3) & 3;
  const int wq8 = __builtin_amdgcn_readfirstlane(threadIdx.x >> 6), wv = wq8 & 3;
  const uint64_t p_of = (uint64_t)(of + ((size_t)rb * 8 * 128 + ksl * 32) * 512);
  const uint64_t p_wo = (uint64_t)(wo + (size_t)ksl * 1024 * DM + cb * 128);
  const uint64_t p_pp = (uint64_t)(pp + ((size_t)ksl * SEQ + rb * 128) * DM + cb * 128);
  const unsigned ldsb = __builtin_amdgcn_readfirstlane((unsigned)(size_t)smem);
  asm volatile(
    "s_cmp_ge_u32 %[wq8], 4\n"
    "s_cbranch_scc1 L_oload_%=\n"
    "v_mbcnt_lo_u32_b32 v199, -1, 0\n"
    "v_mbcnt_hi_u32_b32 v199, -1, v199\n"
    "v_lshlrev_b32 v200, 4, v199\n"
    "s_lshr_b32 s55, %[wv], 1\n"
    "s_lshl_b32 s55, s55, 13\n"
    "s_add_u32 s55, s55, %[lds]\n"
    "v_add_u32 v203, s55, v200\n"
    "v_lshrrev_b32 v211, 4, v199\n"
    "v_bfe_u32 v212, v199, 2, 2\n"
    "v_and_b32 v213, 3, v199\n"
    "v_and_b32 v214, 1, v211\n"
    "v_lshrrev_b32 v215, 1, v211\n"
    "v_lshlrev_b32 v215, 11, v215\n"
    "v_lshl_add_u32 v216, v214, 2, v212\n"
    "v_lshl_add_u32 v215, v216, 6, v215\n"
    "v_lshl_add_u32 v215, v213, 3, v215\n"
    "s_and_b32 s56, %[wv], 1\n"
    "s_lshl_b32 s56, s56, 10\n"
    "s_add_u32 s56, s56, %[lds]\n"
    "v_xor_b32 v216, 0, v214\n"
    "v_lshl_add_u32 v216, v216, 5, v215\n"
    "v_add_u32 v204, s56, v216\n"
    "v_xor_b32 v216, 1, v214\n"
    "v_lshl_add_u32 v216, v216, 5, v215\n"
    "v_add_u32 v205, s56, v216\n"
    "v_add_u32 v208, 0x10000, v203\n"
    "v_add_u32 v209, 0x10000, v204\n"
    "v_add_u32 v210, 0x10000, v205\n"
    "s_mov_b64 s[48:49], %[pp]\n"
    "s_and_b32 s49, s49, 0xffff\n"
    "s_mov_b32 s50, 131072\n"
    "s_mov_b32 s51, 0x20000\n"
    "s_barrier\n"
    "ds_read_b128 v[0:3], v203 offset:0\n"
    "ds_read_b128 v[4:7], v203 offset:2048\n"
    "ds_read_b128 v[8:11], v203 offset:4096\n"
    "ds_read_b128 v[12:15], v203 offset:6144\n"
    "ds_read_b64_tr_b16 v[16:17], v204 offset:16384\n"
    "ds_read_b64_tr_b16 v[18:19], v204 offset:20480\n"
    "ds_read_b64_tr_b16 v[20:21], v205 offset:16384\n"
    "ds_read_b64_tr_b16 v[22:23], v205 offset:20480\n"
    "ds_read_b64_tr_b16 v[24:25], v204 offset:16896\n"
    "ds_read_b64_tr_b16 v[26:27], v204 offset:20992\n"
    "ds_read_b64_tr_b16 v[28:29], v205 offset:16896\n"
    "ds_read_b64_tr_b16 v[30:31], v205 offset:20992\n"
    "ds_read_b128 v[32:35], v203 offset:1024\n"
    "ds_read_b128 v[36:39], v203 offset:3072\n"
    "ds_read_b128 v[40:43], v203 offset:5120\n"
    "ds_read_b128 v[44:47], v203 offset:7168\n"
    "ds_read_b64_tr_b16 v[48:49], v204 offset:24576\n"
    "ds_read_b64_tr_b16 v[50:51], v204 offset:28672\n"
    "ds_read_b64_tr_b16 v[52:53], v205 offset:24576\n"
    "ds_read_b64_tr_b16 v[54:55], v205 offset:28672\n"
    "ds_read_b64_tr_b16 v[56:57], v204 offset:25088\n"
    "ds_read_b64_tr_b16 v[58:59], v204 offset:29184\n"
    "ds_read_b64_tr_b16 v[60:61], v205 offset:25088\n"
    "ds_read_b64_tr_b16 v[62:63], v205 offset:29184\n"
    "s_waitcnt lgkmcnt(0)\n"
    "s_barrier\n"
    "v_mfma_f32_16x16x32_bf16 v[128:131], v[16:19], v[0:3], 0\n"
    "ds_read_b128 v[64:67], v203 offset:32768\n"
    "v_mfma_f32_16x16x32_bf16 v[132:135], v[16:19], v[4:7], 0\n"
    "ds_read_b128 v[68:71], v203 offset:34816\n"
    "v_mfma_f32_16x16x32_bf16 v[136:139], v[16:19], v[8:11], 0\n"
    "ds_read_b128 v[72:75], v203 offset:36864\n"
    "v_mfma_f32_16x16x32_bf16 v[140:143], v[16:19], v[12:15], 0\n"
    "ds_read_b128 v[76:79], v203 offset:38912\n"
    "v_mfma_f32_16x16x32_bf16 v[144:147], v[20:23], v[0:3], 0\n"
    "ds_read_b64_tr_b16 v[80:81], v204 offset:49152\n"
    "v_mfma_f32_16x16x32_bf16 v[148:151], v[20:23], v[4:7], 0\n"
    "ds_read_b64_tr_b16 v[82:83], v204 offset:53248\n"
    "v_mfma_f32_16x16x32_bf16 v[152:155], v[20:23], v[8:11], 0\n"
    "ds_read_b64_tr_b16 v[84:85], v205 offset:49152\n"
    "v_mfma_f32_16x16x32_bf16 v[156:159], v[20:23], v[12:15], 0\n"
    "ds_read_b64_tr_b16 v[86:87], v205 offset:53248\n"
    "v_mfma_f32_16x16x32_bf16 v[160:163], v[24:27], v[0:3], 0\n"
    "ds_read_b64_tr_b16 v[88:89], v204 offset:49664\n"
    "v_mfma_f32_16x16x32_bf16 v[164:167], v[24:27], v[4:7], 0\n"
    "ds_read_b64_tr_b16 v[90:91], v204 offset:53760\n"
    "v_mfma_f32_16x16x32_bf16 v[168:171], v[24:27], v[8:11], 0\n"
    "ds_read_b64_tr_b16 v[92:93], v205 offset:49664\n"
    "v_mfma_f32_16x16x32_bf16 v[172:175], v[24:27], v[12:15], 0\n"
    "ds_read_b64_tr_b16 v[94:95], v205 offset:53760\n"
    "v_mfma_f32_16x16x32_bf16 v[176:179], v[28:31], v[0:3], 0\n"
    "ds_read_b128 v[96:99], v203 offset:33792\n"
    "v_mfma_f32_16x16x32_bf16 v[180:183], v[28:31], v[4:7], 0\n"
    "ds_read_b128 v[100:103], v203 offset:35840\n"
    "v_mfma_f32_16x16x32_bf16 v[184:187], v[28:31], v[8:11], 0\n"
    "ds_read_b128 v[104:107], v203 offset:37888\n"
    "v_mfma_f32_16x16x32_bf16 v[188:191], v[28:31], v[12:15], 0\n"
    "ds_read_b128 v[108:111], v203 offset:39936\n"
    "v_mfma_f32_16x16x32_bf16 v[128:131], v[48:51], v[32:35], v[128:131]\n"
    "ds_read_b64_tr_b16 v[112:113], v204 offset:57344\n"
    "v_mfma_f32_16x16x32_bf16 v[132:135], v[48:51], v[36:39], v[132:135]\n"
    "ds_read_b64_tr_b16 v[114:115], v204 offset:61440\n"
    "v_mfma_f32_16x16x32_bf16 v[136:139], v[48:51], v[40:43], v[136:139]\n"
    "ds_read_b64_tr_b16 v[116:117], v205 offset:57344\n"
    "v_mfma_f32_16x16x32_bf16 v[140:143], v[48:51], v[44:47], v[140:143]\n"
    "ds_read_b64_tr_b16 v[118:119], v205 offset:61440\n"
    "v_mfma_f32_16x16x32_bf16 v[144:147], v[52:55], v[32:35], v[144:147]\n"
    "ds_read_b64_tr_b16 v[120:121], v204 offset:57856\n"
    "v_mfma_f32_16x16x32_bf16 v[148:151], v[52:55], v[36:39], v[148:151]\n"
    "ds_read_b64_tr_b16 v[122:123], v204 offset:61952\n"
    "v_mfma_f32_16x16x32_bf16 v[152:155], v[52:55], v[40:43], v[152:155]\n"
    "ds_read_b64_tr_b16 v[124:125], v205 offset:57856\n"
    "v_mfma_f32_16x16x32_bf16 v[156:159], v[52:55], v[44:47], v[156:159]\n"
    "ds_read_b64_tr_b16 v[126:127], v205 offset:61952\n"
    "v_mfma_f32_16x16x32_bf16 v[160:163], v[56:59], v[32:35], v[160:163]\n"
    "v_mfma_f32_16x16x32_bf16 v[164:167], v[56:59], v[36:39], v[164:167]\n"
    "v_mfma_f32_16x16x32_bf16 v[168:171], v[56:59], v[40:43], v[168:171]\n"
    "v_mfma_f32_16x16x32_bf16 v[172:175], v[56:59], v[44:47], v[172:175]\n"
    "v_mfma_f32_16x16x32_bf16 v[176:179], v[60:63], v[32:35], v[176:179]\n"
    "v_mfma_f32_16x16x32_bf16 v[180:183], v[60:63], v[36:39], v[180:183]\n"
    "v_mfma_f32_16x16x32_bf16 v[184:187], v[60:63], v[40:43], v[184:187]\n"
    "v_mfma_f32_16x16x32_bf16 v[188:191], v[60:63], v[44:47], v[188:191]\n"
    "s_waitcnt lgkmcnt(0)\n"
    "s_barrier\n"
    "s_mov_b32 s57, 2\n"
    "L_oloop_%=:\n"
    "v_mfma_f32_16x16x32_bf16 v[128:131], v[80:83], v[64:67], v[128:131]\n"
    "ds_read_b128 v[0:3], v208 offset:0\n"
    "v_mfma_f32_16x16x32_bf16 v[132:135], v[80:83], v[68:71], v[132:135]\n"
    "ds_read_b128 v[4:7], v208 offset:2048\n"
    "v_mfma_f32_16x16x32_bf16 v[136:139], v[80:83], v[72:75], v[136:139]\n"
    "ds_read_b128 v[8:11], v208 offset:4096\n"
    "v_mfma_f32_16x16x32_bf16 v[140:143], v[80:83], v[76:79], v[140:143]\n"
    "ds_read_b128 v[12:15], v208 offset:6144\n"
    "v_mfma_f32_16x16x32_bf16 v[144:147], v[84:87], v[64:67], v[144:147]\n"
    "ds_read_b64_tr_b16 v[16:17], v209 offset:16384\n"
    "v_mfma_f32_16x16x32_bf16 v[148:151], v[84:87], v[68:71], v[148:151]\n"
    "ds_read_b64_tr_b16 v[18:19], v209 offset:20480\n"
    "v_mfma_f32_16x16x32_bf16 v[152:155], v[84:87], v[72:75], v[152:155]\n"
    "ds_read_b64_tr_b16 v[20:21], v210 offset:16384\n"
    "v_mfma_f32_16x16x32_bf16 v[156:159], v[84:87], v[76:79], v[156:159]\n"
    "ds_read_b64_tr_b16 v[22:23], v210 offset:20480\n"
    "v_mfma_f32_16x16x32_bf16 v[160:163], v[88:91], v[64:67], v[160:163]\n"
    "ds_read_b64_tr_b16 v[24:25], v209 offset:16896\n"
    "v_mfma_f32_16x16x32_bf16 v[164:167], v[88:91], v[68:71], v[164:167]\n"
    "ds_read_b64_tr_b16 v[26:27], v209 offset:20992\n"
    "v_mfma_f32_16x16x32_bf16 v[168:171], v[88:91], v[72:75], v[168:171]\n"
    "ds_read_b64_tr_b16 v[28:29], v210 offset:16896\n"
    "v_mfma_f32_16x16x32_bf16 v[172:175], v[88:91], v[76:79], v[172:175]\n"
    "ds_read_b64_tr_b16 v[30:31], v210 offset:20992\n"
    "v_mfma_f32_16x16x32_bf16 v[176:179], v[92:95], v[64:67], v[176:179]\n"
    "ds_read_b128 v[32:35], v208 offset:1024\n"
    "v_mfma_f32_16x16x32_bf16 v[180:183], v[92:95], v[68:71], v[180:183]\n"
    "ds_read_b128 v[36:39], v208 offset:3072\n"
    "v_mfma_f32_16x16x32_bf16 v[184:187], v[92:95], v[72:75], v[184:187]\n"
    "ds_read_b128 v[40:43], v208 offset:5120\n"
    "v_mfma_f32_16x16x32_bf16 v[188:191], v[92:95], v[76:79], v[188:191]\n"
    "ds_read_b128 v[44:47], v208 offset:7168\n"
    "v_mfma_f32_16x16x32_bf16 v[128:131], v[112:115], v[96:99], v[128:131]\n"
    "ds_read_b64_tr_b16 v[48:49], v209 offset:24576\n"
    "v_mfma_f32_16x16x32_bf16 v[132:135], v[112:115], v[100:103], v[132:135]\n"
    "ds_read_b64_tr_b16 v[50:51], v209 offset:28672\n"
    "v_mfma_f32_16x16x32_bf16 v[136:139], v[112:115], v[104:107], v[136:139]\n"
    "ds_read_b64_tr_b16 v[52:53], v210 offset:24576\n"
    "v_mfma_f32_16x16x32_bf16 v[140:143], v[112:115], v[108:111], v[140:143]\n"
    "ds_read_b64_tr_b16 v[54:55], v210 offset:28672\n"
    "v_mfma_f32_16x16x32_bf16 v[144:147], v[116:119], v[96:99], v[144:147]\n"
    "ds_read_b64_tr_b16 v[56:57], v209 offset:25088\n"
    "v_mfma_f32_16x16x32_bf16 v[148:151], v[116:119], v[100:103], v[148:151]\n"
    "ds_read_b64_tr_b16 v[58:59], v209 offset:29184\n"
    "v_mfma_f32_16x16x32_bf16 v[152:155], v[116:119], v[104:107], v[152:155]\n"
    "ds_read_b64_tr_b16 v[60:61], v210 offset:25088\n"
    "v_mfma_f32_16x16x32_bf16 v[156:159], v[116:119], v[108:111], v[156:159]\n"
    "ds_read_b64_tr_b16 v[62:63], v210 offset:29184\n"
    "v_mfma_f32_16x16x32_bf16 v[160:163], v[120:123], v[96:99], v[160:163]\n"
    "v_mfma_f32_16x16x32_bf16 v[164:167], v[120:123], v[100:103], v[164:167]\n"
    "v_mfma_f32_16x16x32_bf16 v[168:171], v[120:123], v[104:107], v[168:171]\n"
    "v_mfma_f32_16x16x32_bf16 v[172:175], v[120:123], v[108:111], v[172:175]\n"
    "v_mfma_f32_16x16x32_bf16 v[176:179], v[124:127], v[96:99], v[176:179]\n"
    "v_mfma_f32_16x16x32_bf16 v[180:183], v[124:127], v[100:103], v[180:183]\n"
    "v_mfma_f32_16x16x32_bf16 v[184:187], v[124:127], v[104:107], v[184:187]\n"
    "v_mfma_f32_16x16x32_bf16 v[188:191], v[124:127], v[108:111], v[188:191]\n"
    "s_waitcnt lgkmcnt(0)\n"
    "s_barrier\n"
    "v_mfma_f32_16x16x32_bf16 v[128:131], v[16:19], v[0:3], v[128:131]\n"
    "ds_read_b128 v[64:67], v208 offset:32768\n"
    "v_mfma_f32_16x16x32_bf16 v[132:135], v[16:19], v[4:7], v[132:135]\n"
    "ds_read_b128 v[68:71], v208 offset:34816\n"
    "v_mfma_f32_16x16x32_bf16 v[136:139], v[16:19], v[8:11], v[136:139]\n"
    "ds_read_b128 v[72:75], v208 offset:36864\n"
    "v_mfma_f32_16x16x32_bf16 v[140:143], v[16:19], v[12:15], v[140:143]\n"
    "ds_read_b128 v[76:79], v208 offset:38912\n"
    "v_mfma_f32_16x16x32_bf16 v[144:147], v[20:23], v[0:3], v[144:147]\n"
    "ds_read_b64_tr_b16 v[80:81], v209 offset:49152\n"
    "v_mfma_f32_16x16x32_bf16 v[148:151], v[20:23], v[4:7], v[148:151]\n"
    "ds_read_b64_tr_b16 v[82:83], v209 offset:53248\n"
    "v_mfma_f32_16x16x32_bf16 v[152:155], v[20:23], v[8:11], v[152:155]\n"
    "ds_read_b64_tr_b16 v[84:85], v210 offset:49152\n"
    "v_mfma_f32_16x16x32_bf16 v[156:159], v[20:23], v[12:15], v[156:159]\n"
    "ds_read_b64_tr_b16 v[86:87], v210 offset:53248\n"
    "v_mfma_f32_16x16x32_bf16 v[160:163], v[24:27], v[0:3], v[160:163]\n"
    "ds_read_b64_tr_b16 v[88:89], v209 offset:49664\n"
    "v_mfma_f32_16x16x32_bf16 v[164:167], v[24:27], v[4:7], v[164:167]\n"
    "ds_read_b64_tr_b16 v[90:91], v209 offset:53760\n"
    "v_mfma_f32_16x16x32_bf16 v[168:171], v[24:27], v[8:11], v[168:171]\n"
    "ds_read_b64_tr_b16 v[92:93], v210 offset:49664\n"
    "v_mfma_f32_16x16x32_bf16 v[172:175], v[24:27], v[12:15], v[172:175]\n"
    "ds_read_b64_tr_b16 v[94:95], v210 offset:53760\n"
    "v_mfma_f32_16x16x32_bf16 v[176:179], v[28:31], v[0:3], v[176:179]\n"
    "ds_read_b128 v[96:99], v208 offset:33792\n"
    "v_mfma_f32_16x16x32_bf16 v[180:183], v[28:31], v[4:7], v[180:183]\n"
    "ds_read_b128 v[100:103], v208 offset:35840\n"
    "v_mfma_f32_16x16x32_bf16 v[184:187], v[28:31], v[8:11], v[184:187]\n"
    "ds_read_b128 v[104:107], v208 offset:37888\n"
    "v_mfma_f32_16x16x32_bf16 v[188:191], v[28:31], v[12:15], v[188:191]\n"
    "ds_read_b128 v[108:111], v208 offset:39936\n"
    "v_mfma_f32_16x16x32_bf16 v[128:131], v[48:51], v[32:35], v[128:131]\n"
    "ds_read_b64_tr_b16 v[112:113], v209 offset:57344\n"
    "v_mfma_f32_16x16x32_bf16 v[132:135], v[48:51], v[36:39], v[132:135]\n"
    "ds_read_b64_tr_b16 v[114:115], v209 offset:61440\n"
    "v_mfma_f32_16x16x32_bf16 v[136:139], v[48:51], v[40:43], v[136:139]\n"
    "ds_read_b64_tr_b16 v[116:117], v210 offset:57344\n"
    "v_mfma_f32_16x16x32_bf16 v[140:143], v[48:51], v[44:47], v[140:143]\n"
    "ds_read_b64_tr_b16 v[118:119], v210 offset:61440\n"
    "v_mfma_f32_16x16x32_bf16 v[144:147], v[52:55], v[32:35], v[144:147]\n"
    "ds_read_b64_tr_b16 v[120:121], v209 offset:57856\n"
    "v_mfma_f32_16x16x32_bf16 v[148:151], v[52:55], v[36:39], v[148:151]\n"
    "ds_read_b64_tr_b16 v[122:123], v209 offset:61952\n"
    "v_mfma_f32_16x16x32_bf16 v[152:155], v[52:55], v[40:43], v[152:155]\n"
    "ds_read_b64_tr_b16 v[124:125], v210 offset:57856\n"
    "v_mfma_f32_16x16x32_bf16 v[156:159], v[52:55], v[44:47], v[156:159]\n"
    "ds_read_b64_tr_b16 v[126:127], v210 offset:61952\n"
    "v_mfma_f32_16x16x32_bf16 v[160:163], v[56:59], v[32:35], v[160:163]\n"
    "v_mfma_f32_16x16x32_bf16 v[164:167], v[56:59], v[36:39], v[164:167]\n"
    "v_mfma_f32_16x16x32_bf16 v[168:171], v[56:59], v[40:43], v[168:171]\n"
    "v_mfma_f32_16x16x32_bf16 v[172:175], v[56:59], v[44:47], v[172:175]\n"
    "v_mfma_f32_16x16x32_bf16 v[176:179], v[60:63], v[32:35], v[176:179]\n"
    "v_mfma_f32_16x16x32_bf16 v[180:183], v[60:63], v[36:39], v[180:183]\n"
    "v_mfma_f32_16x16x32_bf16 v[184:187], v[60:63], v[40:43], v[184:187]\n"
    "v_mfma_f32_16x16x32_bf16 v[188:191], v[60:63], v[44:47], v[188:191]\n"
    "s_waitcnt lgkmcnt(0)\n"
    "s_barrier\n"
    "v_mfma_f32_16x16x32_bf16 v[128:131], v[80:83], v[64:67], v[128:131]\n"
    "ds_read_b128 v[0:3], v203 offset:0\n"
    "v_mfma_f32_16x16x32_bf16 v[132:135], v[80:83], v[68:71], v[132:135]\n"
    "ds_read_b128 v[4:7], v203 offset:2048\n"
    "v_mfma_f32_16x16x32_bf16 v[136:139], v[80:83], v[72:75], v[136:139]\n"
    "ds_read_b128 v[8:11], v203 offset:4096\n"
    "v_mfma_f32_16x16x32_bf16 v[140:143], v[80:83], v[76:79], v[140:143]\n"
    "ds_read_b128 v[12:15], v203 offset:6144\n"
    "v_mfma_f32_16x16x32_bf16 v[144:147], v[84:87], v[64:67], v[144:147]\n"
    "ds_read_b64_tr_b16 v[16:17], v204 offset:16384\n"
    "v_mfma_f32_16x16x32_bf16 v[148:151], v[84:87], v[68:71], v[148:151]\n"
    "ds_read_b64_tr_b16 v[18:19], v204 offset:20480\n"
    "v_mfma_f32_16x16x32_bf16 v[152:155], v[84:87], v[72:75], v[152:155]\n"
    "ds_read_b64_tr_b16 v[20:21], v205 offset:16384\n"
    "v_mfma_f32_16x16x32_bf16 v[156:159], v[84:87], v[76:79], v[156:159]\n"
    "ds_read_b64_tr_b16 v[22:23], v205 offset:20480\n"
    "v_mfma_f32_16x16x32_bf16 v[160:163], v[88:91], v[64:67], v[160:163]\n"
    "ds_read_b64_tr_b16 v[24:25], v204 offset:16896\n"
    "v_mfma_f32_16x16x32_bf16 v[164:167], v[88:91], v[68:71], v[164:167]\n"
    "ds_read_b64_tr_b16 v[26:27], v204 offset:20992\n"
    "v_mfma_f32_16x16x32_bf16 v[168:171], v[88:91], v[72:75], v[168:171]\n"
    "ds_read_b64_tr_b16 v[28:29], v205 offset:16896\n"
    "v_mfma_f32_16x16x32_bf16 v[172:175], v[88:91], v[76:79], v[172:175]\n"
    "ds_read_b64_tr_b16 v[30:31], v205 offset:20992\n"
    "v_mfma_f32_16x16x32_bf16 v[176:179], v[92:95], v[64:67], v[176:179]\n"
    "ds_read_b128 v[32:35], v203 offset:1024\n"
    "v_mfma_f32_16x16x32_bf16 v[180:183], v[92:95], v[68:71], v[180:183]\n"
    "ds_read_b128 v[36:39], v203 offset:3072\n"
    "v_mfma_f32_16x16x32_bf16 v[184:187], v[92:95], v[72:75], v[184:187]\n"
    "ds_read_b128 v[40:43], v203 offset:5120\n"
    "v_mfma_f32_16x16x32_bf16 v[188:191], v[92:95], v[76:79], v[188:191]\n"
    "ds_read_b128 v[44:47], v203 offset:7168\n"
    "v_mfma_f32_16x16x32_bf16 v[128:131], v[112:115], v[96:99], v[128:131]\n"
    "ds_read_b64_tr_b16 v[48:49], v204 offset:24576\n"
    "v_mfma_f32_16x16x32_bf16 v[132:135], v[112:115], v[100:103], v[132:135]\n"
    "ds_read_b64_tr_b16 v[50:51], v204 offset:28672\n"
    "v_mfma_f32_16x16x32_bf16 v[136:139], v[112:115], v[104:107], v[136:139]\n"
    "ds_read_b64_tr_b16 v[52:53], v205 offset:24576\n"
    "v_mfma_f32_16x16x32_bf16 v[140:143], v[112:115], v[108:111], v[140:143]\n"
    "ds_read_b64_tr_b16 v[54:55], v205 offset:28672\n"
    "v_mfma_f32_16x16x32_bf16 v[144:147], v[116:119], v[96:99], v[144:147]\n"
    "ds_read_b64_tr_b16 v[56:57], v204 offset:25088\n"
    "v_mfma_f32_16x16x32_bf16 v[148:151], v[116:119], v[100:103], v[148:151]\n"
    "ds_read_b64_tr_b16 v[58:59], v204 offset:29184\n"
    "v_mfma_f32_16x16x32_bf16 v[152:155], v[116:119], v[104:107], v[152:155]\n"
    "ds_read_b64_tr_b16 v[60:61], v205 offset:25088\n"
    "v_mfma_f32_16x16x32_bf16 v[156:159], v[116:119], v[108:111], v[156:159]\n"
    "ds_read_b64_tr_b16 v[62:63], v205 offset:29184\n"
    "v_mfma_f32_16x16x32_bf16 v[160:163], v[120:123], v[96:99], v[160:163]\n"
    "v_mfma_f32_16x16x32_bf16 v[164:167], v[120:123], v[100:103], v[164:167]\n"
    "v_mfma_f32_16x16x32_bf16 v[168:171], v[120:123], v[104:107], v[168:171]\n"
    "v_mfma_f32_16x16x32_bf16 v[172:175], v[120:123], v[108:111], v[172:175]\n"
    "v_mfma_f32_16x16x32_bf16 v[176:179], v[124:127], v[96:99], v[176:179]\n"
    "v_mfma_f32_16x16x32_bf16 v[180:183], v[124:127], v[100:103], v[180:183]\n"
    "v_mfma_f32_16x16x32_bf16 v[184:187], v[124:127], v[104:107], v[184:187]\n"
    "v_mfma_f32_16x16x32_bf16 v[188:191], v[124:127], v[108:111], v[188:191]\n"
    "s_waitcnt lgkmcnt(0)\n"
    "s_barrier\n"
    "v_mfma_f32_16x16x32_bf16 v[128:131], v[16:19], v[0:3], v[128:131]\n"
    "ds_read_b128 v[64:67], v203 offset:32768\n"
    "v_mfma_f32_16x16x32_bf16 v[132:135], v[16:19], v[4:7], v[132:135]\n"
    "ds_read_b128 v[68:71], v203 offset:34816\n"
    "v_mfma_f32_16x16x32_bf16 v[136:139], v[16:19], v[8:11], v[136:139]\n"
    "ds_read_b128 v[72:75], v203 offset:36864\n"
    "v_mfma_f32_16x16x32_bf16 v[140:143], v[16:19], v[12:15], v[140:143]\n"
    "ds_read_b128 v[76:79], v203 offset:38912\n"
    "v_mfma_f32_16x16x32_bf16 v[144:147], v[20:23], v[0:3], v[144:147]\n"
    "ds_read_b64_tr_b16 v[80:81], v204 offset:49152\n"
    "v_mfma_f32_16x16x32_bf16 v[148:151], v[20:23], v[4:7], v[148:151]\n"
    "ds_read_b64_tr_b16 v[82:83], v204 offset:53248\n"
    "v_mfma_f32_16x16x32_bf16 v[152:155], v[20:23], v[8:11], v[152:155]\n"
    "ds_read_b64_tr_b16 v[84:85], v205 offset:49152\n"
    "v_mfma_f32_16x16x32_bf16 v[156:159], v[20:23], v[12:15], v[156:159]\n"
    "ds_read_b64_tr_b16 v[86:87], v205 offset:53248\n"
    "v_mfma_f32_16x16x32_bf16 v[160:163], v[24:27], v[0:3], v[160:163]\n"
    "ds_read_b64_tr_b16 v[88:89], v204 offset:49664\n"
    "v_mfma_f32_16x16x32_bf16 v[164:167], v[24:27], v[4:7], v[164:167]\n"
    "ds_read_b64_tr_b16 v[90:91], v204 offset:53760\n"
    "v_mfma_f32_16x16x32_bf16 v[168:171], v[24:27], v[8:11], v[168:171]\n"
    "ds_read_b64_tr_b16 v[92:93], v205 offset:49664\n"
    "v_mfma_f32_16x16x32_bf16 v[172:175], v[24:27], v[12:15], v[172:175]\n"
    "ds_read_b64_tr_b16 v[94:95], v205 offset:53760\n"
    "v_mfma_f32_16x16x32_bf16 v[176:179], v[28:31], v[0:3], v[176:179]\n"
    "ds_read_b128 v[96:99], v203 offset:33792\n"
    "v_mfma_f32_16x16x32_bf16 v[180:183], v[28:31], v[4:7], v[180:183]\n"
    "ds_read_b128 v[100:103], v203 offset:35840\n"
    "v_mfma_f32_16x16x32_bf16 v[184:187], v[28:31], v[8:11], v[184:187]\n"
    "ds_read_b128 v[104:107], v203 offset:37888\n"
    "v_mfma_f32_16x16x32_bf16 v[188:191], v[28:31], v[12:15], v[188:191]\n"
    "ds_read_b128 v[108:111], v203 offset:39936\n"
    "v_mfma_f32_16x16x32_bf16 v[128:131], v[48:51], v[32:35], v[128:131]\n"
    "ds_read_b64_tr_b16 v[112:113], v204 offset:57344\n"
    "v_mfma_f32_16x16x32_bf16 v[132:135], v[48:51], v[36:39], v[132:135]\n"
    "ds_read_b64_tr_b16 v[114:115], v204 offset:61440\n"
    "v_mfma_f32_16x16x32_bf16 v[136:139], v[48:51], v[40:43], v[136:139]\n"
    "ds_read_b64_tr_b16 v[116:117], v205 offset:57344\n"
    "v_mfma_f32_16x16x32_bf16 v[140:143], v[48:51], v[44:47], v[140:143]\n"
    "ds_read_b64_tr_b16 v[118:119], v205 offset:61440\n"
    "v_mfma_f32_16x16x32_bf16 v[144:147], v[52:55], v[32:35], v[144:147]\n"
    "ds_read_b64_tr_b16 v[120:121], v204 offset:57856\n"
    "v_mfma_f32_16x16x32_bf16 v[148:151], v[52:55], v[36:39], v[148:151]\n"
    "ds_read_b64_tr_b16 v[122:123], v204 offset:61952\n"
    "v_mfma_f32_16x16x32_bf16 v[152:155], v[52:55], v[40:43], v[152:155]\n"
    "ds_read_b64_tr_b16 v[124:125], v205 offset:57856\n"
    "v_mfma_f32_16x16x32_bf16 v[156:159], v[52:55], v[44:47], v[156:159]\n"
    "ds_read_b64_tr_b16 v[126:127], v205 offset:61952\n"
    "v_mfma_f32_16x16x32_bf16 v[160:163], v[56:59], v[32:35], v[160:163]\n"
    "v_mfma_f32_16x16x32_bf16 v[164:167], v[56:59], v[36:39], v[164:167]\n"
    "v_mfma_f32_16x16x32_bf16 v[168:171], v[56:59], v[40:43], v[168:171]\n"
    "v_mfma_f32_16x16x32_bf16 v[172:175], v[56:59], v[44:47], v[172:175]\n"
    "v_mfma_f32_16x16x32_bf16 v[176:179], v[60:63], v[32:35], v[176:179]\n"
    "v_mfma_f32_16x16x32_bf16 v[180:183], v[60:63], v[36:39], v[180:183]\n"
    "v_mfma_f32_16x16x32_bf16 v[184:187], v[60:63], v[40:43], v[184:187]\n"
    "v_mfma_f32_16x16x32_bf16 v[188:191], v[60:63], v[44:47], v[188:191]\n"
    "s_waitcnt lgkmcnt(0)\n"
    "s_barrier\n"
    "s_sub_u32 s57, s57, 1\n"
    "s_cmp_lg_u32 s57, 0\n"
    "s_cbranch_scc1 L_oloop_%=\n"
    "v_mfma_f32_16x16x32_bf16 v[128:131], v[80:83], v[64:67], v[128:131]\n"
    "ds_read_b128 v[0:3], v208 offset:0\n"
    "v_mfma_f32_16x16x32_bf16 v[132:135], v[80:83], v[68:71], v[132:135]\n"
    "ds_read_b128 v[4:7], v208 offset:2048\n"
    "v_mfma_f32_16x16x32_bf16 v[136:139], v[80:83], v[72:75], v[136:139]\n"
    "ds_read_b128 v[8:11], v208 offset:4096\n"
    "v_mfma_f32_16x16x32_bf16 v[140:143], v[80:83], v[76:79], v[140:143]\n"
    "ds_read_b128 v[12:15], v208 offset:6144\n"
    "v_mfma_f32_16x16x32_bf16 v[144:147], v[84:87], v[64:67], v[144:147]\n"
    "ds_read_b64_tr_b16 v[16:17], v209 offset:16384\n"
    "v_mfma_f32_16x16x32_bf16 v[148:151], v[84:87], v[68:71], v[148:151]\n"
    "ds_read_b64_tr_b16 v[18:19], v209 offset:20480\n"
    "v_mfma_f32_16x16x32_bf16 v[152:155], v[84:87], v[72:75], v[152:155]\n"
    "ds_read_b64_tr_b16 v[20:21], v210 offset:16384\n"
    "v_mfma_f32_16x16x32_bf16 v[156:159], v[84:87], v[76:79], v[156:159]\n"
    "ds_read_b64_tr_b16 v[22:23], v210 offset:20480\n"
    "v_mfma_f32_16x16x32_bf16 v[160:163], v[88:91], v[64:67], v[160:163]\n"
    "ds_read_b64_tr_b16 v[24:25], v209 offset:16896\n"
    "v_mfma_f32_16x16x32_bf16 v[164:167], v[88:91], v[68:71], v[164:167]\n"
    "ds_read_b64_tr_b16 v[26:27], v209 offset:20992\n"
    "v_mfma_f32_16x16x32_bf16 v[168:171], v[88:91], v[72:75], v[168:171]\n"
    "ds_read_b64_tr_b16 v[28:29], v210 offset:16896\n"
    "v_mfma_f32_16x16x32_bf16 v[172:175], v[88:91], v[76:79], v[172:175]\n"
    "ds_read_b64_tr_b16 v[30:31], v210 offset:20992\n"
    "v_mfma_f32_16x16x32_bf16 v[176:179], v[92:95], v[64:67], v[176:179]\n"
    "ds_read_b128 v[32:35], v208 offset:1024\n"
    "v_mfma_f32_16x16x32_bf16 v[180:183], v[92:95], v[68:71], v[180:183]\n"
    "ds_read_b128 v[36:39], v208 offset:3072\n"
    "v_mfma_f32_16x16x32_bf16 v[184:187], v[92:95], v[72:75], v[184:187]\n"
    "ds_read_b128 v[40:43], v208 offset:5120\n"
    "v_mfma_f32_16x16x32_bf16 v[188:191], v[92:95], v[76:79], v[188:191]\n"
    "ds_read_b128 v[44:47], v208 offset:7168\n"
    "v_mfma_f32_16x16x32_bf16 v[128:131], v[112:115], v[96:99], v[128:131]\n"
    "ds_read_b64_tr_b16 v[48:49], v209 offset:24576\n"
    "v_mfma_f32_16x16x32_bf16 v[132:135], v[112:115], v[100:103], v[132:135]\n"
    "ds_read_b64_tr_b16 v[50:51], v209 offset:28672\n"
    "v_mfma_f32_16x16x32_bf16 v[136:139], v[112:115], v[104:107], v[136:139]\n"
    "ds_read_b64_tr_b16 v[52:53], v210 offset:24576\n"
    "v_mfma_f32_16x16x32_bf16 v[140:143], v[112:115], v[108:111], v[140:143]\n"
    "ds_read_b64_tr_b16 v[54:55], v210 offset:28672\n"
    "v_mfma_f32_16x16x32_bf16 v[144:147], v[116:119], v[96:99], v[144:147]\n"
    "ds_read_b64_tr_b16 v[56:57], v209 offset:25088\n"
    "v_mfma_f32_16x16x32_bf16 v[148:151], v[116:119], v[100:103], v[148:151]\n"
    "ds_read_b64_tr_b16 v[58:59], v209 offset:29184\n"
    "v_mfma_f32_16x16x32_bf16 v[152:155], v[116:119], v[104:107], v[152:155]\n"
    "ds_read_b64_tr_b16 v[60:61], v210 offset:25088\n"
    "v_mfma_f32_16x16x32_bf16 v[156:159], v[116:119], v[108:111], v[156:159]\n"
    "ds_read_b64_tr_b16 v[62:63], v210 offset:29184\n"
    "v_mfma_f32_16x16x32_bf16 v[160:163], v[120:123], v[96:99], v[160:163]\n"
    "v_mfma_f32_16x16x32_bf16 v[164:167], v[120:123], v[100:103], v[164:167]\n"
    "v_mfma_f32_16x16x32_bf16 v[168:171], v[120:123], v[104:107], v[168:171]\n"
    "v_mfma_f32_16x16x32_bf16 v[172:175], v[120:123], v[108:111], v[172:175]\n"
    "v_mfma_f32_16x16x32_bf16 v[176:179], v[124:127], v[96:99], v[176:179]\n"
    "v_mfma_f32_16x16x32_bf16 v[180:183], v[124:127], v[100:103], v[180:183]\n"
    "v_mfma_f32_16x16x32_bf16 v[184:187], v[124:127], v[104:107], v[184:187]\n"
    "v_mfma_f32_16x16x32_bf16 v[188:191], v[124:127], v[108:111], v[188:191]\n"
    "s_waitcnt lgkmcnt(0)\n"
    "s_barrier\n"
    "v_mfma_f32_16x16x32_bf16 v[128:131], v[16:19], v[0:3], v[128:131]\n"
    "ds_read_b128 v[64:67], v208 offset:32768\n"
    "v_mfma_f32_16x16x32_bf16 v[132:135], v[16:19], v[4:7], v[132:135]\n"
    "ds_read_b128 v[68:71], v208 offset:34816\n"
    "v_mfma_f32_16x16x32_bf16 v[136:139], v[16:19], v[8:11], v[136:139]\n"
    "ds_read_b128 v[72:75], v208 offset:36864\n"
    "v_mfma_f32_16x16x32_bf16 v[140:143], v[16:19], v[12:15], v[140:143]\n"
    "ds_read_b128 v[76:79], v208 offset:38912\n"
    "v_mfma_f32_16x16x32_bf16 v[144:147], v[20:23], v[0:3], v[144:147]\n"
    "ds_read_b64_tr_b16 v[80:81], v209 offset:49152\n"
    "v_mfma_f32_16x16x32_bf16 v[148:151], v[20:23], v[4:7], v[148:151]\n"
    "ds_read_b64_tr_b16 v[82:83], v209 offset:53248\n"
    "v_mfma_f32_16x16x32_bf16 v[152:155], v[20:23], v[8:11], v[152:155]\n"
    "ds_read_b64_tr_b16 v[84:85], v210 offset:49152\n"
    "v_mfma_f32_16x16x32_bf16 v[156:159], v[20:23], v[12:15], v[156:159]\n"
    "ds_read_b64_tr_b16 v[86:87], v210 offset:53248\n"
    "v_mfma_f32_16x16x32_bf16 v[160:163], v[24:27], v[0:3], v[160:163]\n"
    "ds_read_b64_tr_b16 v[88:89], v209 offset:49664\n"
    "v_mfma_f32_16x16x32_bf16 v[164:167], v[24:27], v[4:7], v[164:167]\n"
    "ds_read_b64_tr_b16 v[90:91], v209 offset:53760\n"
    "v_mfma_f32_16x16x32_bf16 v[168:171], v[24:27], v[8:11], v[168:171]\n"
    "ds_read_b64_tr_b16 v[92:93], v210 offset:49664\n"
    "v_mfma_f32_16x16x32_bf16 v[172:175], v[24:27], v[12:15], v[172:175]\n"
    "ds_read_b64_tr_b16 v[94:95], v210 offset:53760\n"
    "v_mfma_f32_16x16x32_bf16 v[176:179], v[28:31], v[0:3], v[176:179]\n"
    "ds_read_b128 v[96:99], v208 offset:33792\n"
    "v_mfma_f32_16x16x32_bf16 v[180:183], v[28:31], v[4:7], v[180:183]\n"
    "ds_read_b128 v[100:103], v208 offset:35840\n"
    "v_mfma_f32_16x16x32_bf16 v[184:187], v[28:31], v[8:11], v[184:187]\n"
    "ds_read_b128 v[104:107], v208 offset:37888\n"
    "v_mfma_f32_16x16x32_bf16 v[188:191], v[28:31], v[12:15], v[188:191]\n"
    "ds_read_b128 v[108:111], v208 offset:39936\n"
    "v_mfma_f32_16x16x32_bf16 v[128:131], v[48:51], v[32:35], v[128:131]\n"
    "ds_read_b64_tr_b16 v[112:113], v209 offset:57344\n"
    "v_mfma_f32_16x16x32_bf16 v[132:135], v[48:51], v[36:39], v[132:135]\n"
    "ds_read_b64_tr_b16 v[114:115], v209 offset:61440\n"
    "v_mfma_f32_16x16x32_bf16 v[136:139], v[48:51], v[40:43], v[136:139]\n"
    "ds_read_b64_tr_b16 v[116:117], v210 offset:57344\n"
    "v_mfma_f32_16x16x32_bf16 v[140:143], v[48:51], v[44:47], v[140:143]\n"
    "ds_read_b64_tr_b16 v[118:119], v210 offset:61440\n"
    "v_mfma_f32_16x16x32_bf16 v[144:147], v[52:55], v[32:35], v[144:147]\n"
    "ds_read_b64_tr_b16 v[120:121], v209 offset:57856\n"
    "v_mfma_f32_16x16x32_bf16 v[148:151], v[52:55], v[36:39], v[148:151]\n"
    "ds_read_b64_tr_b16 v[122:123], v209 offset:61952\n"
    "v_mfma_f32_16x16x32_bf16 v[152:155], v[52:55], v[40:43], v[152:155]\n"
    "ds_read_b64_tr_b16 v[124:125], v210 offset:57856\n"
    "v_mfma_f32_16x16x32_bf16 v[156:159], v[52:55], v[44:47], v[156:159]\n"
    "ds_read_b64_tr_b16 v[126:127], v210 offset:61952\n"
    "v_mfma_f32_16x16x32_bf16 v[160:163], v[56:59], v[32:35], v[160:163]\n"
    "v_mfma_f32_16x16x32_bf16 v[164:167], v[56:59], v[36:39], v[164:167]\n"
    "v_mfma_f32_16x16x32_bf16 v[168:171], v[56:59], v[40:43], v[168:171]\n"
    "v_mfma_f32_16x16x32_bf16 v[172:175], v[56:59], v[44:47], v[172:175]\n"
    "v_mfma_f32_16x16x32_bf16 v[176:179], v[60:63], v[32:35], v[176:179]\n"
    "v_mfma_f32_16x16x32_bf16 v[180:183], v[60:63], v[36:39], v[180:183]\n"
    "v_mfma_f32_16x16x32_bf16 v[184:187], v[60:63], v[40:43], v[184:187]\n"
    "v_mfma_f32_16x16x32_bf16 v[188:191], v[60:63], v[44:47], v[188:191]\n"
    "s_waitcnt lgkmcnt(0)\n"
    "s_barrier\n"
    "v_mfma_f32_16x16x32_bf16 v[128:131], v[80:83], v[64:67], v[128:131]\n"
    "ds_read_b128 v[0:3], v203 offset:0\n"
    "v_mfma_f32_16x16x32_bf16 v[132:135], v[80:83], v[68:71], v[132:135]\n"
    "ds_read_b128 v[4:7], v203 offset:2048\n"
    "v_mfma_f32_16x16x32_bf16 v[136:139], v[80:83], v[72:75], v[136:139]\n"
    "ds_read_b128 v[8:11], v203 offset:4096\n"
    "v_mfma_f32_16x16x32_bf16 v[140:143], v[80:83], v[76:79], v[140:143]\n"
    "ds_read_b128 v[12:15], v203 offset:6144\n"
    "v_mfma_f32_16x16x32_bf16 v[144:147], v[84:87], v[64:67], v[144:147]\n"
    "ds_read_b64_tr_b16 v[16:17], v204 offset:16384\n"
    "v_mfma_f32_16x16x32_bf16 v[148:151], v[84:87], v[68:71], v[148:151]\n"
    "ds_read_b64_tr_b16 v[18:19], v204 offset:20480\n"
    "v_mfma_f32_16x16x32_bf16 v[152:155], v[84:87], v[72:75], v[152:155]\n"
    "ds_read_b64_tr_b16 v[20:21], v205 offset:16384\n"
    "v_mfma_f32_16x16x32_bf16 v[156:159], v[84:87], v[76:79], v[156:159]\n"
    "ds_read_b64_tr_b16 v[22:23], v205 offset:20480\n"
    "v_mfma_f32_16x16x32_bf16 v[160:163], v[88:91], v[64:67], v[160:163]\n"
    "ds_read_b64_tr_b16 v[24:25], v204 offset:16896\n"
    "v_mfma_f32_16x16x32_bf16 v[164:167], v[88:91], v[68:71], v[164:167]\n"
    "ds_read_b64_tr_b16 v[26:27], v204 offset:20992\n"
    "v_mfma_f32_16x16x32_bf16 v[168:171], v[88:91], v[72:75], v[168:171]\n"
    "ds_read_b64_tr_b16 v[28:29], v205 offset:16896\n"
    "v_mfma_f32_16x16x32_bf16 v[172:175], v[88:91], v[76:79], v[172:175]\n"
    "ds_read_b64_tr_b16 v[30:31], v205 offset:20992\n"
    "v_mfma_f32_16x16x32_bf16 v[176:179], v[92:95], v[64:67], v[176:179]\n"
    "ds_read_b128 v[32:35], v203 offset:1024\n"
    "v_mfma_f32_16x16x32_bf16 v[180:183], v[92:95], v[68:71], v[180:183]\n"
    "ds_read_b128 v[36:39], v203 offset:3072\n"
    "v_mfma_f32_16x16x32_bf16 v[184:187], v[92:95], v[72:75], v[184:187]\n"
    "ds_read_b128 v[40:43], v203 offset:5120\n"
    "v_mfma_f32_16x16x32_bf16 v[188:191], v[92:95], v[76:79], v[188:191]\n"
    "ds_read_b128 v[44:47], v203 offset:7168\n"
    "v_mfma_f32_16x16x32_bf16 v[128:131], v[112:115], v[96:99], v[128:131]\n"
    "ds_read_b64_tr_b16 v[48:49], v204 offset:24576\n"
    "v_mfma_f32_16x16x32_bf16 v[132:135], v[112:115], v[100:103], v[132:135]\n"
    "ds_read_b64_tr_b16 v[50:51], v204 offset:28672\n"
    "v_mfma_f32_16x16x32_bf16 v[136:139], v[112:115], v[104:107], v[136:139]\n"
    "ds_read_b64_tr_b16 v[52:53], v205 offset:24576\n"
    "v_mfma_f32_16x16x32_bf16 v[140:143], v[112:115], v[108:111], v[140:143]\n"
    "ds_read_b64_tr_b16 v[54:55], v205 offset:28672\n"
    "v_mfma_f32_16x16x32_bf16 v[144:147], v[116:119], v[96:99], v[144:147]\n"
    "ds_read_b64_tr_b16 v[56:57], v204 offset:25088\n"
    "v_mfma_f32_16x16x32_bf16 v[148:151], v[116:119], v[100:103], v[148:151]\n"
    "ds_read_b64_tr_b16 v[58:59], v204 offset:29184\n"
    "v_mfma_f32_16x16x32_bf16 v[152:155], v[116:119], v[104:107], v[152:155]\n"
    "ds_read_b64_tr_b16 v[60:61], v205 offset:25088\n"
    "v_mfma_f32_16x16x32_bf16 v[156:159], v[116:119], v[108:111], v[156:159]\n"
    "ds_read_b64_tr_b16 v[62:63], v205 offset:29184\n"
    "v_mfma_f32_16x16x32_bf16 v[160:163], v[120:123], v[96:99], v[160:163]\n"
    "v_mfma_f32_16x16x32_bf16 v[164:167], v[120:123], v[100:103], v[164:167]\n"
    "v_mfma_f32_16x16x32_bf16 v[168:171], v[120:123], v[104:107], v[168:171]\n"
    "v_mfma_f32_16x16x32_bf16 v[172:175], v[120:123], v[108:111], v[172:175]\n"
    "v_mfma_f32_16x16x32_bf16 v[176:179], v[124:127], v[96:99], v[176:179]\n"
    "v_mfma_f32_16x16x32_bf16 v[180:183], v[124:127], v[100:103], v[180:183]\n"
    "v_mfma_f32_16x16x32_bf16 v[184:187], v[124:127], v[104:107], v[184:187]\n"
    "v_mfma_f32_16x16x32_bf16 v[188:191], v[124:127], v[108:111], v[188:191]\n"
    "s_waitcnt lgkmcnt(0)\n"
    "s_barrier\n"
    "v_mfma_f32_16x16x32_bf16 v[128:131], v[16:19], v[0:3], v[128:131]\n"
    "ds_read_b128 v[64:67], v203 offset:32768\n"
    "v_mfma_f32_16x16x32_bf16 v[132:135], v[16:19], v[4:7], v[132:135]\n"
    "ds_read_b128 v[68:71], v203 offset:34816\n"
    "v_mfma_f32_16x16x32_bf16 v[136:139], v[16:19], v[8:11], v[136:139]\n"
    "ds_read_b128 v[72:75], v203 offset:36864\n"
    "v_mfma_f32_16x16x32_bf16 v[140:143], v[16:19], v[12:15], v[140:143]\n"
    "ds_read_b128 v[76:79], v203 offset:38912\n"
    "v_mfma_f32_16x16x32_bf16 v[144:147], v[20:23], v[0:3], v[144:147]\n"
    "ds_read_b64_tr_b16 v[80:81], v204 offset:49152\n"
    "v_mfma_f32_16x16x32_bf16 v[148:151], v[20:23], v[4:7], v[148:151]\n"
    "ds_read_b64_tr_b16 v[82:83], v204 offset:53248\n"
    "v_mfma_f32_16x16x32_bf16 v[152:155], v[20:23], v[8:11], v[152:155]\n"
    "ds_read_b64_tr_b16 v[84:85], v205 offset:49152\n"
    "v_mfma_f32_16x16x32_bf16 v[156:159], v[20:23], v[12:15], v[156:159]\n"
    "ds_read_b64_tr_b16 v[86:87], v205 offset:53248\n"
    "v_mfma_f32_16x16x32_bf16 v[160:163], v[24:27], v[0:3], v[160:163]\n"
    "ds_read_b64_tr_b16 v[88:89], v204 offset:49664\n"
    "v_mfma_f32_16x16x32_bf16 v[164:167], v[24:27], v[4:7], v[164:167]\n"
    "ds_read_b64_tr_b16 v[90:91], v204 offset:53760\n"
    "v_mfma_f32_16x16x32_bf16 v[168:171], v[24:27], v[8:11], v[168:171]\n"
    "ds_read_b64_tr_b16 v[92:93], v205 offset:49664\n"
    "v_mfma_f32_16x16x32_bf16 v[172:175], v[24:27], v[12:15], v[172:175]\n"
    "ds_read_b64_tr_b16 v[94:95], v205 offset:53760\n"
    "v_mfma_f32_16x16x32_bf16 v[176:179], v[28:31], v[0:3], v[176:179]\n"
    "ds_read_b128 v[96:99], v203 offset:33792\n"
    "v_mfma_f32_16x16x32_bf16 v[180:183], v[28:31], v[4:7], v[180:183]\n"
    "ds_read_b128 v[100:103], v203 offset:35840\n"
    "v_mfma_f32_16x16x32_bf16 v[184:187], v[28:31], v[8:11], v[184:187]\n"
    "ds_read_b128 v[104:107], v203 offset:37888\n"
    "v_mfma_f32_16x16x32_bf16 v[188:191], v[28:31], v[12:15], v[188:191]\n"
    "ds_read_b128 v[108:111], v203 offset:39936\n"
    "v_mfma_f32_16x16x32_bf16 v[128:131], v[48:51], v[32:35], v[128:131]\n"
    "ds_read_b64_tr_b16 v[112:113], v204 offset:57344\n"
    "v_mfma_f32_16x16x32_bf16 v[132:135], v[48:51], v[36:39], v[132:135]\n"
    "ds_read_b64_tr_b16 v[114:115], v204 offset:61440\n"
    "v_mfma_f32_16x16x32_bf16 v[136:139], v[48:51], v[40:43], v[136:139]\n"
    "ds_read_b64_tr_b16 v[116:117], v205 offset:57344\n"
    "v_mfma_f32_16x16x32_bf16 v[140:143], v[48:51], v[44:47], v[140:143]\n"
    "ds_read_b64_tr_b16 v[118:119], v205 offset:61440\n"
    "v_mfma_f32_16x16x32_bf16 v[144:147], v[52:55], v[32:35], v[144:147]\n"
    "ds_read_b64_tr_b16 v[120:121], v204 offset:57856\n"
    "v_mfma_f32_16x16x32_bf16 v[148:151], v[52:55], v[36:39], v[148:151]\n"
    "ds_read_b64_tr_b16 v[122:123], v204 offset:61952\n"
    "v_mfma_f32_16x16x32_bf16 v[152:155], v[52:55], v[40:43], v[152:155]\n"
    "ds_read_b64_tr_b16 v[124:125], v205 offset:57856\n"
    "v_mfma_f32_16x16x32_bf16 v[156:159], v[52:55], v[44:47], v[156:159]\n"
    "ds_read_b64_tr_b16 v[126:127], v205 offset:61952\n"
    "v_mfma_f32_16x16x32_bf16 v[160:163], v[56:59], v[32:35], v[160:163]\n"
    "v_mfma_f32_16x16x32_bf16 v[164:167], v[56:59], v[36:39], v[164:167]\n"
    "v_mfma_f32_16x16x32_bf16 v[168:171], v[56:59], v[40:43], v[168:171]\n"
    "v_mfma_f32_16x16x32_bf16 v[172:175], v[56:59], v[44:47], v[172:175]\n"
    "v_mfma_f32_16x16x32_bf16 v[176:179], v[60:63], v[32:35], v[176:179]\n"
    "v_mfma_f32_16x16x32_bf16 v[180:183], v[60:63], v[36:39], v[180:183]\n"
    "v_mfma_f32_16x16x32_bf16 v[184:187], v[60:63], v[40:43], v[184:187]\n"
    "v_mfma_f32_16x16x32_bf16 v[188:191], v[60:63], v[44:47], v[188:191]\n"
    "s_waitcnt lgkmcnt(0)\n"
    "s_barrier\n"
    "v_mfma_f32_16x16x32_bf16 v[128:131], v[80:83], v[64:67], v[128:131]\n"
    "ds_read_b128 v[0:3], v208 offset:0\n"
    "v_mfma_f32_16x16x32_bf16 v[132:135], v[80:83], v[68:71], v[132:135]\n"
    "ds_read_b128 v[4:7], v208 offset:2048\n"
    "v_mfma_f32_16x16x32_bf16 v[136:139], v[80:83], v[72:75], v[136:139]\n"
    "ds_read_b128 v[8:11], v208 offset:4096\n"
    "v_mfma_f32_16x16x32_bf16 v[140:143], v[80:83], v[76:79], v[140:143]\n"
    "ds_read_b128 v[12:15], v208 offset:6144\n"
    "v_mfma_f32_16x16x32_bf16 v[144:147], v[84:87], v[64:67], v[144:147]\n"
    "ds_read_b64_tr_b16 v[16:17], v209 offset:16384\n"
    "v_mfma_f32_16x16x32_bf16 v[148:151], v[84:87], v[68:71], v[148:151]\n"
    "ds_read_b64_tr_b16 v[18:19], v209 offset:20480\n"
    "v_mfma_f32_16x16x32_bf16 v[152:155], v[84:87], v[72:75], v[152:155]\n"
    "ds_read_b64_tr_b16 v[20:21], v210 offset:16384\n"
    "v_mfma_f32_16x16x32_bf16 v[156:159], v[84:87], v[76:79], v[156:159]\n"
    "ds_read_b64_tr_b16 v[22:23], v210 offset:20480\n"
    "v_mfma_f32_16x16x32_bf16 v[160:163], v[88:91], v[64:67], v[160:163]\n"
    "ds_read_b64_tr_b16 v[24:25], v209 offset:16896\n"
    "v_mfma_f32_16x16x32_bf16 v[164:167], v[88:91], v[68:71], v[164:167]\n"
    "ds_read_b64_tr_b16 v[26:27], v209 offset:20992\n"
    "v_mfma_f32_16x16x32_bf16 v[168:171], v[88:91], v[72:75], v[168:171]\n"
    "ds_read_b64_tr_b16 v[28:29], v210 offset:16896\n"
    "v_mfma_f32_16x16x32_bf16 v[172:175], v[88:91], v[76:79], v[172:175]\n"
    "ds_read_b64_tr_b16 v[30:31], v210 offset:20992\n"
    "v_mfma_f32_16x16x32_bf16 v[176:179], v[92:95], v[64:67], v[176:179]\n"
    "ds_read_b128 v[32:35], v208 offset:1024\n"
    "v_mfma_f32_16x16x32_bf16 v[180:183], v[92:95], v[68:71], v[180:183]\n"
    "ds_read_b128 v[36:39], v208 offset:3072\n"
    "v_mfma_f32_16x16x32_bf16 v[184:187], v[92:95], v[72:75], v[184:187]\n"
    "ds_read_b128 v[40:43], v208 offset:5120\n"
    "v_mfma_f32_16x16x32_bf16 v[188:191], v[92:95], v[76:79], v[188:191]\n"
    "ds_read_b128 v[44:47], v208 offset:7168\n"
    "v_mfma_f32_16x16x32_bf16 v[128:131], v[112:115], v[96:99], v[128:131]\n"
    "ds_read_b64_tr_b16 v[48:49], v209 offset:24576\n"
    "v_mfma_f32_16x16x32_bf16 v[132:135], v[112:115], v[100:103], v[132:135]\n"
    "ds_read_b64_tr_b16 v[50:51], v209 offset:28672\n"
    "v_mfma_f32_16x16x32_bf16 v[136:139], v[112:115], v[104:107], v[136:139]\n"
    "ds_read_b64_tr_b16 v[52:53], v210 offset:24576\n"
    "v_mfma_f32_16x16x32_bf16 v[140:143], v[112:115], v[108:111], v[140:143]\n"
    "ds_read_b64_tr_b16 v[54:55], v210 offset:28672\n"
    "v_mfma_f32_16x16x32_bf16 v[144:147], v[116:119], v[96:99], v[144:147]\n"
    "ds_read_b64_tr_b16 v[56:57], v209 offset:25088\n"
    "v_mfma_f32_16x16x32_bf16 v[148:151], v[116:119], v[100:103], v[148:151]\n"
    "ds_read_b64_tr_b16 v[58:59], v209 offset:29184\n"
    "v_mfma_f32_16x16x32_bf16 v[152:155], v[116:119], v[104:107], v[152:155]\n"
    "ds_read_b64_tr_b16 v[60:61], v210 offset:25088\n"
    "v_mfma_f32_16x16x32_bf16 v[156:159], v[116:119], v[108:111], v[156:159]\n"
    "ds_read_b64_tr_b16 v[62:63], v210 offset:29184\n"
    "v_mfma_f32_16x16x32_bf16 v[160:163], v[120:123], v[96:99], v[160:163]\n"
    "v_mfma_f32_16x16x32_bf16 v[164:167], v[120:123], v[100:103], v[164:167]\n"
    "v_mfma_f32_16x16x32_bf16 v[168:171], v[120:123], v[104:107], v[168:171]\n"
    "v_mfma_f32_16x16x32_bf16 v[172:175], v[120:123], v[108:111], v[172:175]\n"
    "v_mfma_f32_16x16x32_bf16 v[176:179], v[124:127], v[96:99], v[176:179]\n"
    "v_mfma_f32_16x16x32_bf16 v[180:183], v[124:127], v[100:103], v[180:183]\n"
    "v_mfma_f32_16x16x32_bf16 v[184:187], v[124:127], v[104:107], v[184:187]\n"
    "v_mfma_f32_16x16x32_bf16 v[188:191], v[124:127], v[108:111], v[188:191]\n"
    "s_waitcnt lgkmcnt(0)\n"
    "s_barrier\n"
    "v_mfma_f32_16x16x32_bf16 v[128:131], v[16:19], v[0:3], v[128:131]\n"
    "ds_read_b128 v[64:67], v208 offset:32768\n"
    "v_mfma_f32_16x16x32_bf16 v[132:135], v[16:19], v[4:7], v[132:135]\n"
    "ds_read_b128 v[68:71], v208 offset:34816\n"
    "v_mfma_f32_16x16x32_bf16 v[136:139], v[16:19], v[8:11], v[136:139]\n"
    "ds_read_b128 v[72:75], v208 offset:36864\n"
    "v_mfma_f32_16x16x32_bf16 v[140:143], v[16:19], v[12:15], v[140:143]\n"
    "ds_read_b128 v[76:79], v208 offset:38912\n"
    "v_mfma_f32_16x16x32_bf16 v[144:147], v[20:23], v[0:3], v[144:147]\n"
    "ds_read_b64_tr_b16 v[80:81], v209 offset:49152\n"
    "v_mfma_f32_16x16x32_bf16 v[148:151], v[20:23], v[4:7], v[148:151]\n"
    "ds_read_b64_tr_b16 v[82:83], v209 offset:53248\n"
    "v_mfma_f32_16x16x32_bf16 v[152:155], v[20:23], v[8:11], v[152:155]\n"
    "ds_read_b64_tr_b16 v[84:85], v210 offset:49152\n"
    "v_mfma_f32_16x16x32_bf16 v[156:159], v[20:23], v[12:15], v[156:159]\n"
    "ds_read_b64_tr_b16 v[86:87], v210 offset:53248\n"
    "v_mfma_f32_16x16x32_bf16 v[160:163], v[24:27], v[0:3], v[160:163]\n"
    "ds_read_b64_tr_b16 v[88:89], v209 offset:49664\n"
    "v_mfma_f32_16x16x32_bf16 v[164:167], v[24:27], v[4:7], v[164:167]\n"
    "ds_read_b64_tr_b16 v[90:91], v209 offset:53760\n"
    "v_mfma_f32_16x16x32_bf16 v[168:171], v[24:27], v[8:11], v[168:171]\n"
    "ds_read_b64_tr_b16 v[92:93], v210 offset:49664\n"
    "v_mfma_f32_16x16x32_bf16 v[172:175], v[24:27], v[12:15], v[172:175]\n"
    "ds_read_b64_tr_b16 v[94:95], v210 offset:53760\n"
    "v_mfma_f32_16x16x32_bf16 v[176:179], v[28:31], v[0:3], v[176:179]\n"
    "ds_read_b128 v[96:99], v208 offset:33792\n"
    "v_mfma_f32_16x16x32_bf16 v[180:183], v[28:31], v[4:7], v[180:183]\n"
    "ds_read_b128 v[100:103], v208 offset:35840\n"
    "v_mfma_f32_16x16x32_bf16 v[184:187], v[28:31], v[8:11], v[184:187]\n"
    "ds_read_b128 v[104:107], v208 offset:37888\n"
    "v_mfma_f32_16x16x32_bf16 v[188:191], v[28:31], v[12:15], v[188:191]\n"
    "ds_read_b128 v[108:111], v208 offset:39936\n"
    "v_mfma_f32_16x16x32_bf16 v[128:131], v[48:51], v[32:35], v[128:131]\n"
    "ds_read_b64_tr_b16 v[112:113], v209 offset:57344\n"
    "v_mfma_f32_16x16x32_bf16 v[132:135], v[48:51], v[36:39], v[132:135]\n"
    "ds_read_b64_tr_b16 v[114:115], v209 offset:61440\n"
    "v_mfma_f32_16x16x32_bf16 v[136:139], v[48:51], v[40:43], v[136:139]\n"
    "ds_read_b64_tr_b16 v[116:117], v210 offset:57344\n"
    "v_mfma_f32_16x16x32_bf16 v[140:143], v[48:51], v[44:47], v[140:143]\n"
    "ds_read_b64_tr_b16 v[118:119], v210 offset:61440\n"
    "v_mfma_f32_16x16x32_bf16 v[144:147], v[52:55], v[32:35], v[144:147]\n"
    "ds_read_b64_tr_b16 v[120:121], v209 offset:57856\n"
    "v_mfma_f32_16x16x32_bf16 v[148:151], v[52:55], v[36:39], v[148:151]\n"
    "ds_read_b64_tr_b16 v[122:123], v209 offset:61952\n"
    "v_mfma_f32_16x16x32_bf16 v[152:155], v[52:55], v[40:43], v[152:155]\n"
    "ds_read_b64_tr_b16 v[124:125], v210 offset:57856\n"
    "v_mfma_f32_16x16x32_bf16 v[156:159], v[52:55], v[44:47], v[156:159]\n"
    "ds_read_b64_tr_b16 v[126:127], v210 offset:61952\n"
    "v_mfma_f32_16x16x32_bf16 v[160:163], v[56:59], v[32:35], v[160:163]\n"
    "v_mfma_f32_16x16x32_bf16 v[164:167], v[56:59], v[36:39], v[164:167]\n"
    "v_mfma_f32_16x16x32_bf16 v[168:171], v[56:59], v[40:43], v[168:171]\n"
    "v_mfma_f32_16x16x32_bf16 v[172:175], v[56:59], v[44:47], v[172:175]\n"
    "v_mfma_f32_16x16x32_bf16 v[176:179], v[60:63], v[32:35], v[176:179]\n"
    "v_mfma_f32_16x16x32_bf16 v[180:183], v[60:63], v[36:39], v[180:183]\n"
    "v_mfma_f32_16x16x32_bf16 v[184:187], v[60:63], v[40:43], v[184:187]\n"
    "v_mfma_f32_16x16x32_bf16 v[188:191], v[60:63], v[44:47], v[188:191]\n"
    "s_waitcnt lgkmcnt(0)\n"
    "s_barrier\n"
    "v_mfma_f32_16x16x32_bf16 v[128:131], v[80:83], v[64:67], v[128:131]\n"
    "v_mfma_f32_16x16x32_bf16 v[132:135], v[80:83], v[68:71], v[132:135]\n"
    "v_mfma_f32_16x16x32_bf16 v[136:139], v[80:83], v[72:75], v[136:139]\n"
    "v_mfma_f32_16x16x32_bf16 v[140:143], v[80:83], v[76:79], v[140:143]\n"
    "v_mfma_f32_16x16x32_bf16 v[144:147], v[84:87], v[64:67], v[144:147]\n"
    "v_mfma_f32_16x16x32_bf16 v[148:151], v[84:87], v[68:71], v[148:151]\n"
    "v_mfma_f32_16x16x32_bf16 v[152:155], v[84:87], v[72:75], v[152:155]\n"
    "v_mfma_f32_16x16x32_bf16 v[156:159], v[84:87], v[76:79], v[156:159]\n"
    "v_mfma_f32_16x16x32_bf16 v[160:163], v[88:91], v[64:67], v[160:163]\n"
    "v_mfma_f32_16x16x32_bf16 v[164:167], v[88:91], v[68:71], v[164:167]\n"
    "v_mfma_f32_16x16x32_bf16 v[168:171], v[88:91], v[72:75], v[168:171]\n"
    "v_mfma_f32_16x16x32_bf16 v[172:175], v[88:91], v[76:79], v[172:175]\n"
    "v_mfma_f32_16x16x32_bf16 v[176:179], v[92:95], v[64:67], v[176:179]\n"
    "v_mfma_f32_16x16x32_bf16 v[180:183], v[92:95], v[68:71], v[180:183]\n"
    "v_mfma_f32_16x16x32_bf16 v[184:187], v[92:95], v[72:75], v[184:187]\n"
    "v_mfma_f32_16x16x32_bf16 v[188:191], v[92:95], v[76:79], v[188:191]\n"
    "v_mfma_f32_16x16x32_bf16 v[128:131], v[112:115], v[96:99], v[128:131]\n"
    "v_mfma_f32_16x16x32_bf16 v[132:135], v[112:115], v[100:103], v[132:135]\n"
    "v_mfma_f32_16x16x32_bf16 v[136:139], v[112:115], v[104:107], v[136:139]\n"
    "v_mfma_f32_16x16x32_bf16 v[140:143], v[112:115], v[108:111], v[140:143]\n"
    "v_mfma_f32_16x16x32_bf16 v[144:147], v[116:119], v[96:99], v[144:147]\n"
    "v_mfma_f32_16x16x32_bf16 v[148:151], v[116:119], v[100:103], v[148:151]\n"
    "v_mfma_f32_16x16x32_bf16 v[152:155], v[116:119], v[104:107], v[152:155]\n"
    "v_mfma_f32_16x16x32_bf16 v[156:159], v[116:119], v[108:111], v[156:159]\n"
    "v_mfma_f32_16x16x32_bf16 v[160:163], v[120:123], v[96:99], v[160:163]\n"
    "v_mfma_f32_16x16x32_bf16 v[164:167], v[120:123], v[100:103], v[164:167]\n"
    "v_mfma_f32_16x16x32_bf16 v[168:171], v[120:123], v[104:107], v[168:171]\n"
    "v_mfma_f32_16x16x32_bf16 v[172:175], v[120:123], v[108:111], v[172:175]\n"
    "v_mfma_f32_16x16x32_bf16 v[176:179], v[124:127], v[96:99], v[176:179]\n"
    "v_mfma_f32_16x16x32_bf16 v[180:183], v[124:127], v[100:103], v[180:183]\n"
    "v_mfma_f32_16x16x32_bf16 v[184:187], v[124:127], v[104:107], v[184:187]\n"
    "v_mfma_f32_16x16x32_bf16 v[188:191], v[124:127], v[108:111], v[188:191]\n"
    "s_waitcnt lgkmcnt(0)\n"
    "s_nop 7\n"
    "v_and_b32 v211, 15, v199\n"
    "v_lshrrev_b32 v212, 4, v199\n"
    "s_lshr_b32 s55, %[wv], 1\n"
    "s_lshl_b32 s55, s55, 14\n"
    "s_add_u32 s55, s55, %[lds]\n"
    "s_and_b32 s56, %[wv], 1\n"
    "s_lshl_b32 s56, s56, 3\n"
    "v_lshrrev_b32 v213, 1, v212\n"
    "v_add_u32 v213, s56, v213\n"
    "v_and_b32 v214, 1, v212\n"
    "v_lshlrev_b32 v214, 3, v214\n"
    "v_lshl_add_u32 v214, v211, 8, v214\n"
    "v_add_u32 v214, s55, v214\n"
    "v_add_u32 v215, 0, v213\n"
    "v_xor_b32 v215, v215, v211\n"
    "v_lshl_add_u32 v216, v215, 4, v214\n"
    "v_add_u32 v215, 2, v213\n"
    "v_xor_b32 v215, v215, v211\n"
    "v_lshl_add_u32 v217, v215, 4, v214\n"
    "v_add_u32 v215, 4, v213\n"
    "v_xor_b32 v215, v215, v211\n"
    "v_lshl_add_u32 v218, v215, 4, v214\n"
    "v_add_u32 v215, 6, v213\n"
    "v_xor_b32 v215, v215, v211\n"
    "v_lshl_add_u32 v219, v215, 4, v214\n"
    "v_cvt_pk_bf16_f32 v0, v128, v129\n"
    "v_cvt_pk_bf16_f32 v1, v130, v131\n"
    "ds_write_b64 v216, v[0:1] offset:0\n"
    "v_cvt_pk_bf16_f32 v2, v132, v133\n"
    "v_cvt_pk_bf16_f32 v3, v134, v135\n"
    "ds_write_b64 v216, v[2:3] offset:4096\n"
    "v_cvt_pk_bf16_f32 v4, v136, v137\n"
    "v_cvt_pk_bf16_f32 v5, v138, v139\n"
    "ds_write_b64 v216, v[4:5] offset:8192\n"
    "v_cvt_pk_bf16_f32 v6, v140, v141\n"
    "v_cvt_pk_bf16_f32 v7, v142, v143\n"
    "ds_write_b64 v216, v[6:7] offset:12288\n"
    "v_cvt_pk_bf16_f32 v8, v144, v145\n"
    "v_cvt_pk_bf16_f32 v9, v146, v147\n"
    "ds_write_b64 v217, v[8:9] offset:0\n"
    "v_cvt_pk_bf16_f32 v10, v148, v149\n"
    "v_cvt_pk_bf16_f32 v11, v150, v151\n"
    "ds_write_b64 v217, v[10:11] offset:4096\n"
    "v_cvt_pk_bf16_f32 v12, v152, v153\n"
    "v_cvt_pk_bf16_f32 v13, v154, v155\n"
    "ds_write_b64 v217, v[12:13] offset:8192\n"
    "v_cvt_pk_bf16_f32 v14, v156, v157\n"
    "v_cvt_pk_bf16_f32 v15, v158, v159\n"
    "ds_write_b64 v217, v[14:15] offset:12288\n"
    "v_cvt_pk_bf16_f32 v16, v160, v161\n"
    "v_cvt_pk_bf16_f32 v17, v162, v163\n"
    "ds_write_b64 v218, v[16:17] offset:0\n"
    "v_cvt_pk_bf16_f32 v18, v164, v165\n"
    "v_cvt_pk_bf16_f32 v19, v166, v167\n"
    "ds_write_b64 v218, v[18:19] offset:4096\n"
    "v_cvt_pk_bf16_f32 v20, v168, v169\n"
    "v_cvt_pk_bf16_f32 v21, v170, v171\n"
    "ds_write_b64 v218, v[20:21] offset:8192\n"
    "v_cvt_pk_bf16_f32 v22, v172, v173\n"
    "v_cvt_pk_bf16_f32 v23, v174, v175\n"
    "ds_write_b64 v218, v[22:23] offset:12288\n"
    "v_cvt_pk_bf16_f32 v24, v176, v177\n"
    "v_cvt_pk_bf16_f32 v25, v178, v179\n"
    "ds_write_b64 v219, v[24:25] offset:0\n"
    "v_cvt_pk_bf16_f32 v26, v180, v181\n"
    "v_cvt_pk_bf16_f32 v27, v182, v183\n"
    "ds_write_b64 v219, v[26:27] offset:4096\n"
    "v_cvt_pk_bf16_f32 v28, v184, v185\n"
    "v_cvt_pk_bf16_f32 v29, v186, v187\n"
    "ds_write_b64 v219, v[28:29] offset:8192\n"
    "v_cvt_pk_bf16_f32 v30, v188, v189\n"
    "v_cvt_pk_bf16_f32 v31, v190, v191\n"
    "ds_write_b64 v219, v[30:31] offset:12288\n"
    "s_waitcnt lgkmcnt(0)\n"
    "s_barrier\n"
    "v_lshrrev_b32 v212, 4, v199\n"
    "s_lshl_b32 s55, %[wv], 13\n"
    "s_add_u32 s56, s55, %[lds]\n"
    "v_add_u32 v213, 0, v212\n"
    "v_xor_b32 v213, v213, v211\n"
    "v_lshlrev_b32 v213, 4, v213\n"
    "v_lshl_add_u32 v213, v212, 8, v213\n"
    "v_add_u32 v220, s56, v213\n"
    "v_add_u32 v213, 4, v212\n"
    "v_xor_b32 v213, v213, v211\n"
    "v_lshlrev_b32 v213, 4, v213\n"
    "v_lshl_add_u32 v213, v212, 8, v213\n"
    "v_add_u32 v221, s56, v213\n"
    "v_add_u32 v213, 8, v212\n"
    "v_xor_b32 v213, v213, v211\n"
    "v_lshlrev_b32 v213, 4, v213\n"
    "v_lshl_add_u32 v213, v212, 8, v213\n"
    "v_add_u32 v222, s56, v213\n"
    "v_add_u32 v213, 12, v212\n"
    "v_xor_b32 v213, v213, v211\n"
    "v_lshlrev_b32 v213, 4, v213\n"
    "v_lshl_add_u32 v213, v212, 8, v213\n"
    "v_add_u32 v223, s56, v213\n"
    "v_add_u32 v213, 0, v212\n"
    "v_xor_b32 v213, v213, v211\n"
    "v_lshlrev_b32 v213, 4, v213\n"
    "v_lshl_add_u32 v213, v212, 8, v213\n"
    "v_add_u32 v224, s56, v213\n"
    "v_add_u32 v213, 4, v212\n"
    "v_xor_b32 v213, v213, v211\n"
    "v_lshlrev_b32 v213, 4, v213\n"
    "v_lshl_add_u32 v213, v212, 8, v213\n"
    "v_add_u32 v225, s56, v213\n"
    "v_add_u32 v213, 8, v212\n"
    "v_xor_b32 v213, v213, v211\n"
    "v_lshlrev_b32 v213, 4, v213\n"
    "v_lshl_add_u32 v213, v212, 8, v213\n"
    "v_add_u32 v226, s56, v213\n"
    "v_add_u32 v213, 12, v212\n"
    "v_xor_b32 v213, v213, v211\n"
    "v_lshlrev_b32 v213, 4, v213\n"
    "v_lshl_add_u32 v213, v212, 8, v213\n"
    "v_add_u32 v227, s56, v213\n"
    "ds_read_b128 v[64:67], v220 offset:0\n"
    "ds_read_b128 v[68:71], v221 offset:1024\n"
    "ds_read_b128 v[72:75], v222 offset:2048\n"
    "ds_read_b128 v[76:79], v223 offset:3072\n"
    "ds_read_b128 v[80:83], v224 offset:4096\n"
    "ds_read_b128 v[84:87], v225 offset:5120\n"
    "ds_read_b128 v[88:91], v226 offset:6144\n"
    "ds_read_b128 v[92:95], v227 offset:7168\n"
    "v_lshlrev_b32 v213, 4, v211\n"
    "v_lshl_add_u32 v213, v212, 10, v213\n"
    "s_lshl_b32 s55, %[wv], 15\n"
    "s_waitcnt lgkmcnt(7)\n"
    "s_add_u32 s56, s55, 0\n"
    "buffer_store_dwordx4 v[64:67], v213, s[48:51], s56 offen sc1\n"
    "s_waitcnt lgkmcnt(6)\n"
    "s_add_u32 s56, s55, 4096\n"
    "buffer_store_dwordx4 v[68:71], v213, s[48:51], s56 offen sc1\n"
    "s_waitcnt lgkmcnt(5)\n"
    "s_add_u32 s56, s55, 8192\n"
    "buffer_store_dwordx4 v[72:75], v213, s[48:51], s56 offen sc1\n"
    "s_waitcnt lgkmcnt(4)\n"
    "s_add_u32 s56, s55, 12288\n"
    "buffer_store_dwordx4 v[76:79], v213, s[48:51], s56 offen sc1\n"
    "s_waitcnt lgkmcnt(3)\n"
    "s_add_u32 s56, s55, 16384\n"
    "buffer_store_dwordx4 v[80:83], v213, s[48:51], s56 offen sc1\n"
    "s_waitcnt lgkmcnt(2)\n"
    "s_add_u32 s56, s55, 20480\n"
    "buffer_store_dwordx4 v[84:87], v213, s[48:51], s56 offen sc1\n"
    "s_waitcnt lgkmcnt(1)\n"
    "s_add_u32 s56, s55, 24576\n"
    "buffer_store_dwordx4 v[88:91], v213, s[48:51], s56 offen sc1\n"
    "s_waitcnt lgkmcnt(0)\n"
    "s_add_u32 s56, s55, 28672\n"
    "buffer_store_dwordx4 v[92:95], v213, s[48:51], s56 offen sc1\n"
    "s_branch L_oend_%=\n"
    "L_oload_%=:\n"
    "s_mov_b64 s[40:41], %[of]\n"
    "s_and_b32 s41, s41, 0xffff\n"
    "s_mov_b32 s42, 1048576\n"
    "s_mov_b32 s43, 0x20000\n"
    "s_mov_b64 s[44:45], %[wo]\n"
    "s_and_b32 s45, s45, 0xffff\n"
    "s_mov_b32 s46, 1048576\n"
    "s_mov_b32 s47, 0x20000\n"
    "s_mov_b64 s[48:49], %[pp]\n"
    "s_and_b32 s49, s49, 0xffff\n"
    "s_mov_b32 s50, 131072\n"
    "s_mov_b32 s51, 0x20000\n"
    "s_lshl_b32 s55, %[wv], 12\n"
    "s_add_u32 s52, %[lds], s55\n"
    "s_lshl_b32 s53, %[wv], 18\n"
    "s_lshl_b32 s54, %[wv], 14\n"
    "v_mbcnt_lo_u32_b32 v199, -1, 0\n"
    "v_mbcnt_hi_u32_b32 v199, -1, v199\n"
    "v_lshlrev_b32 v200, 4, v199\n"
    "v_bfe_u32 v211, v199, 2, 3\n"
    "v_lshrrev_b32 v212, 5, v199\n"
    "v_bfe_u32 v213, v199, 1, 1\n"
    "v_bfe_u32 v214, v199, 4, 1\n"
    "v_xor_b32 v213, v213, v214\n"
    "v_and_b32 v214, 1, v199\n"
    "v_lshlrev_b32 v215, 6, v212\n"
    "v_lshl_add_u32 v215, v213, 5, v215\n"
    "v_lshl_add_u32 v215, v214, 4, v215\n"
    "v_lshl_add_u32 v202, v211, 10, v215\n"
    "s_add_u32 s55, s53, 0\n"
    "s_add_u32 m0, s52, 0\n"
    "s_nop 0\n"
    "buffer_load_dwordx4 v200, s[40:43], s55 offen lds\n"
    "s_nop 0\n"
    "buffer_load_dwordx4 v200, s[40:43], s55 offen offset:1024 lds\n"
    "s_add_u32 s55, s53, 131072\n"
    "s_add_u32 m0, s52, 2048\n"
    "s_nop 0\n"
    "buffer_load_dwordx4 v200, s[40:43], s55 offen lds\n"
    "s_nop 0\n"
    "buffer_load_dwordx4 v200, s[40:43], s55 offen offset:1024 lds\n"
    "s_add_u32 s56, s54, 0\n"
    "s_add_u32 m0, s52, 16384\n"
    "s_nop 0\n"
    "buffer_load_dwordx4 v202, s[44:47], s56 offen lds\n"
    "s_add_u32 m0, s52, 17280\n"
    "s_nop 0\n"
    "buffer_load_dwordx4 v202, s[44:47], s56 offen offset:128 lds\n"
    "s_add_u32 s56, s54, 8192\n"
    "s_add_u32 m0, s52, 18432\n"
    "s_nop 0\n"
    "buffer_load_dwordx4 v202, s[44:47], s56 offen lds\n"
    "s_add_u32 m0, s52, 19328\n"
    "s_nop 0\n"
    "buffer_load_dwordx4 v202, s[44:47], s56 offen offset:128 lds\n"
    "s_add_u32 s53, s53, 2048\n"
    "s_add_u32 s54, s54, 65536\n"
    "s_add_u32 s55, s53, 0\n"
    "s_add_u32 m0, s52, 32768\n"
    "s_nop 0\n"
    "buffer_load_dwordx4 v200, s[40:43], s55 offen lds\n"
    "s_nop 0\n"
    "buffer_load_dwordx4 v200, s[40:43], s55 offen offset:1024 lds\n"
    "s_add_u32 s55, s53, 131072\n"
    "s_add_u32 m0, s52, 34816\n"
    "s_nop 0\n"
    "buffer_load_dwordx4 v200, s[40:43], s55 offen lds\n"
    "s_nop 0\n"
    "buffer_load_dwordx4 v200, s[40:43], s55 offen offset:1024 lds\n"
    "s_add_u32 s56, s54, 0\n"
    "s_add_u32 m0, s52, 49152\n"
    "s_nop 0\n"
    "buffer_load_dwordx4 v202, s[44:47], s56 offen lds\n"
    "s_add_u32 m0, s52, 50048\n"
    "s_nop 0\n"
    "buffer_load_dwordx4 v202, s[44:47], s56 offen offset:128 lds\n"
    "s_add_u32 s56, s54, 8192\n"
    "s_add_u32 m0, s52, 51200\n"
    "s_nop 0\n"
    "buffer_load_dwordx4 v202, s[44:47], s56 offen lds\n"
    "s_add_u32 m0, s52, 52096\n"
    "s_nop 0\n"
    "buffer_load_dwordx4 v202, s[44:47], s56 offen offset:128 lds\n"
    "s_add_u32 s53, s53, 2048\n"
    "s_add_u32 s54, s54, 65536\n"
    "s_waitcnt vmcnt(8)\n"
    "s_barrier\n"
    "s_add_u32 s55, s53, 0\n"
    "s_add_u32 m0, s52, 65536\n"
    "s_nop 0\n"
    "buffer_load_dwordx4 v200, s[40:43], s55 offen lds\n"
    "s_nop 0\n"
    "buffer_load_dwordx4 v200, s[40:43], s55 offen offset:1024 lds\n"
    "s_add_u32 s55, s53, 131072\n"
    "s_add_u32 m0, s52, 67584\n"
    "s_nop 0\n"
    "buffer_load_dwordx4 v200, s[40:43], s55 offen lds\n"
    "s_nop 0\n"
    "buffer_load_dwordx4 v200, s[40:43], s55 offen offset:1024 lds\n"
    "s_add_u32 s56, s54, 0\n"
    "s_add_u32 m0, s52, 81920\n"
    "s_nop 0\n"
    "buffer_load_dwordx4 v202, s[44:47], s56 offen lds\n"
    "s_add_u32 m0, s52, 82816\n"
    "s_nop 0\n"
    "buffer_load_dwordx4 v202, s[44:47], s56 offen offset:128 lds\n"
    "s_add_u32 s56, s54, 8192\n"
    "s_add_u32 m0, s52, 83968\n"
    "s_nop 0\n"
    "buffer_load_dwordx4 v202, s[44:47], s56 offen lds\n"
    "s_add_u32 m0, s52, 84864\n"
    "s_nop 0\n"
    "buffer_load_dwordx4 v202, s[44:47], s56 offen offset:128 lds\n"
    "s_add_u32 s53, s53, 2048\n"
    "s_add_u32 s54, s54, 65536\n"
    "s_waitcnt vmcnt(8)\n"
    "s_barrier\n"
    "s_add_u32 s55, s53, 0\n"
    "s_add_u32 m0, s52, 98304\n"
    "s_nop 0\n"
    "buffer_load_dwordx4 v200, s[40:43], s55 offen lds\n"
    "s_nop 0\n"
    "buffer_load_dwordx4 v200, s[40:43], s55 offen offset:1024 lds\n"
    "s_add_u32 s55, s53, 131072\n"
    "s_add_u32 m0, s52, 100352\n"
    "s_nop 0\n"
    "buffer_load_dwordx4 v200, s[40:43], s55 offen lds\n"
    "s_nop 0\n"
    "buffer_load_dwordx4 v200, s[40:43], s55 offen offset:1024 lds\n"
    "s_add_u32 s56, s54, 0\n"
    "s_add_u32 m0, s52, 114688\n"
    "s_nop 0\n"
    "buffer_load_dwordx4 v202, s[44:47], s56 offen lds\n"
    "s_add_u32 m0, s52, 115584\n"
    "s_nop 0\n"
    "buffer_load_dwordx4 v202, s[44:47], s56 offen offset:128 lds\n"
    "s_add_u32 s56, s54, 8192\n"
    "s_add_u32 m0, s52, 116736\n"
    "s_nop 0\n"
    "buffer_load_dwordx4 v202, s[44:47], s56 offen lds\n"
    "s_add_u32 m0, s52, 117632\n"
    "s_nop 0\n"
    "buffer_load_dwordx4 v202, s[44:47], s56 offen offset:128 lds\n"
    "s_add_u32 s53, s53, 2048\n"
    "s_add_u32 s54, s54, 65536\n"
    "s_waitcnt vmcnt(8)\n"
    "s_barrier\n"
    "s_mov_b32 s57, 2\n"
    "L_olloop_%=:\n"
    "s_add_u32 s55, s53, 0\n"
    "s_add_u32 m0, s52, 0\n"
    "s_nop 0\n"
    "buffer_load_dwordx4 v200, s[40:43], s55 offen lds\n"
    "s_nop 0\n"
    "buffer_load_dwordx4 v200, s[40:43], s55 offen offset:1024 lds\n"
    "s_add_u32 s55, s53, 131072\n"
    "s_add_u32 m0, s52, 2048\n"
    "s_nop 0\n"
    "buffer_load_dwordx4 v200, s[40:43], s55 offen lds\n"
    "s_nop 0\n"
    "buffer_load_dwordx4 v200, s[40:43], s55 offen offset:1024 lds\n"
    "s_add_u32 s56, s54, 0\n"
    "s_add_u32 m0, s52, 16384\n"
    "s_nop 0\n"
    "buffer_load_dwordx4 v202, s[44:47], s56 offen lds\n"
    "s_add_u32 m0, s52, 17280\n"
    "s_nop 0\n"
    "buffer_load_dwordx4 v202, s[44:47], s56 offen offset:128 lds\n"
    "s_add_u32 s56, s54, 8192\n"
    "s_add_u32 m0, s52, 18432\n"
    "s_nop 0\n"
    "buffer_load_dwordx4 v202, s[44:47], s56 offen lds\n"
    "s_add_u32 m0, s52, 19328\n"
    "s_nop 0\n"
    "buffer_load_dwordx4 v202, s[44:47], s56 offen offset:128 lds\n"
    "s_add_u32 s53, s53, 2048\n"
    "s_add_u32 s54, s54, 65536\n"
    "s_waitcnt vmcnt(8)\n"
    "s_barrier\n"
    "s_add_u32 s55, s53, 0\n"
    "s_add_u32 m0, s52, 32768\n"
    "s_nop 0\n"
    "buffer_load_dwordx4 v200, s[40:43], s55 offen lds\n"
    "s_nop 0\n"
    "buffer_load_dwordx4 v200, s[40:43], s55 offen offset:1024 lds\n"
    "s_add_u32 s55, s53, 131072\n"
    "s_add_u32 m0, s52, 34816\n"
    "s_nop 0\n"
    "buffer_load_dwordx4 v200, s[40:43], s55 offen lds\n"
    "s_nop 0\n"
    "buffer_load_dwordx4 v200, s[40:43], s55 offen offset:1024 lds\n"
    "s_add_u32 s56, s54, 0\n"
    "s_add_u32 m0, s52, 49152\n"
    "s_nop 0\n"
    "buffer_load_dwordx4 v202, s[44:47], s56 offen lds\n"
    "s_add_u32 m0, s52, 50048\n"
    "s_nop 0\n"
    "buffer_load_dwordx4 v202, s[44:47], s56 offen offset:128 lds\n"
    "s_add_u32 s56, s54, 8192\n"
    "s_add_u32 m0, s52, 51200\n"
    "s_nop 0\n"
    "buffer_load_dwordx4 v202, s[44:47], s56 offen lds\n"
    "s_add_u32 m0, s52, 52096\n"
    "s_nop 0\n"
    "buffer_load_dwordx4 v202, s[44:47], s56 offen offset:128 lds\n"
    "s_add_u32 s53, s53, 2048\n"
    "s_add_u32 s54, s54, 65536\n"
    "s_waitcnt vmcnt(8)\n"
    "s_barrier\n"
    "s_add_u32 s55, s53, 0\n"
    "s_add_u32 m0, s52, 65536\n"
    "s_nop 0\n"
    "buffer_load_dwordx4 v200, s[40:43], s55 offen lds\n"
    "s_nop 0\n"
    "buffer_load_dwordx4 v200, s[40:43], s55 offen offset:1024 lds\n"
    "s_add_u32 s55, s53, 131072\n"
    "s_add_u32 m0, s52, 67584\n"
    "s_nop 0\n"
    "buffer_load_dwordx4 v200, s[40:43], s55 offen lds\n"
    "s_nop 0\n"
    "buffer_load_dwordx4 v200, s[40:43], s55 offen offset:1024 lds\n"
    "s_add_u32 s56, s54, 0\n"
    "s_add_u32 m0, s52, 81920\n"
    "s_nop 0\n"
    "buffer_load_dwordx4 v202, s[44:47], s56 offen lds\n"
    "s_add_u32 m0, s52, 82816\n"
    "s_nop 0\n"
    "buffer_load_dwordx4 v202, s[44:47], s56 offen offset:128 lds\n"
    "s_add_u32 s56, s54, 8192\n"
    "s_add_u32 m0, s52, 83968\n"
    "s_nop 0\n"
    "buffer_load_dwordx4 v202, s[44:47], s56 offen lds\n"
    "s_add_u32 m0, s52, 84864\n"
    "s_nop 0\n"
    "buffer_load_dwordx4 v202, s[44:47], s56 offen offset:128 lds\n"
    "s_add_u32 s53, s53, 2048\n"
    "s_add_u32 s54, s54, 65536\n"
    "s_waitcnt vmcnt(8)\n"
    "s_barrier\n"
    "s_add_u32 s55, s53, 0\n"
    "s_add_u32 m0, s52, 98304\n"
    "s_nop 0\n"
    "buffer_load_dwordx4 v200, s[40:43], s55 offen lds\n"
    "s_nop 0\n"
    "buffer_load_dwordx4 v200, s[40:43], s55 offen offset:1024 lds\n"
    "s_add_u32 s55, s53, 131072\n"
    "s_add_u32 m0, s52, 100352\n"
    "s_nop 0\n"
    "buffer_load_dwordx4 v200, s[40:43], s55 offen lds\n"
    "s_nop 0\n"
    "buffer_load_dwordx4 v200, s[40:43], s55 offen offset:1024 lds\n"
    "s_add_u32 s56, s54, 0\n"
    "s_add_u32 m0, s52, 114688\n"
    "s_nop 0\n"
    "buffer_load_dwordx4 v202, s[44:47], s56 offen lds\n"
    "s_add_u32 m0, s52, 115584\n"
    "s_nop 0\n"
    "buffer_load_dwordx4 v202, s[44:47], s56 offen offset:128 lds\n"
    "s_add_u32 s56, s54, 8192\n"
    "s_add_u32 m0, s52, 116736\n"
    "s_nop 0\n"
    "buffer_load_dwordx4 v202, s[44:47], s56 offen lds\n"
    "s_add_u32 m0, s52, 117632\n"
    "s_nop 0\n"
    "buffer_load_dwordx4 v202, s[44:47], s56 offen offset:128 lds\n"
    "s_add_u32 s53, s53, 2048\n"
    "s_add_u32 s54, s54, 65536\n"
    "s_waitcnt vmcnt(8)\n"
    "s_barrier\n"
    "s_sub_u32 s57, s57, 1\n"
    "s_cmp_lg_u32 s57, 0\n"
    "s_cbranch_scc1 L_olloop_%=\n"
    "s_add_u32 s55, s53, 0\n"
    "s_add_u32 m0, s52, 0\n"
    "s_nop 0\n"
    "buffer_load_dwordx4 v200, s[40:43], s55 offen lds\n"
    "s_nop 0\n"
    "buffer_load_dwordx4 v200, s[40:43], s55 offen offset:1024 lds\n"
    "s_add_u32 s55, s53, 131072\n"
    "s_add_u32 m0, s52, 2048\n"
    "s_nop 0\n"
    "buffer_load_dwordx4 v200, s[40:43], s55 offen lds\n"
    "s_nop 0\n"
    "buffer_load_dwordx4 v200, s[40:43], s55 offen offset:1024 lds\n"
    "s_add_u32 s56, s54, 0\n"
    "s_add_u32 m0, s52, 16384\n"
    "s_nop 0\n"
    "buffer_load_dwordx4 v202, s[44:47], s56 offen lds\n"
    "s_add_u32 m0, s52, 17280\n"
    "s_nop 0\n"
    "buffer_load_dwordx4 v202, s[44:47], s56 offen offset:128 lds\n"
    "s_add_u32 s56, s54, 8192\n"
    "s_add_u32 m0, s52, 18432\n"
    "s_nop 0\n"
    "buffer_load_dwordx4 v202, s[44:47], s56 offen lds\n"
    "s_add_u32 m0, s52, 19328\n"
    "s_nop 0\n"
    "buffer_load_dwordx4 v202, s[44:47], s56 offen offset:128 lds\n"
    "s_add_u32 s53, s53, 2048\n"
    "s_add_u32 s54, s54, 65536\n"
    "s_waitcnt vmcnt(8)\n"
    "s_barrier\n"
    "s_add_u32 s55, s53, 0\n"
    "s_add_u32 m0, s52, 32768\n"
    "s_nop 0\n"
    "buffer_load_dwordx4 v200, s[40:43], s55 offen lds\n"
    "s_nop 0\n"
    "buffer_load_dwordx4 v200, s[40:43], s55 offen offset:1024 lds\n"
    "s_add_u32 s55, s53, 131072\n"
    "s_add_u32 m0, s52, 34816\n"
    "s_nop 0\n"
    "buffer_load_dwordx4 v200, s[40:43], s55 offen lds\n"
    "s_nop 0\n"
    "buffer_load_dwordx4 v200, s[40:43], s55 offen offset:1024 lds\n"
    "s_add_u32 s56, s54, 0\n"
    "s_add_u32 m0, s52, 49152\n"
    "s_nop 0\n"
    "buffer_load_dwordx4 v202, s[44:47], s56 offen lds\n"
    "s_add_u32 m0, s52, 50048\n"
    "s_nop 0\n"
    "buffer_load_dwordx4 v202, s[44:47], s56 offen offset:128 lds\n"
    "s_add_u32 s56, s54, 8192\n"
    "s_add_u32 m0, s52, 51200\n"
    "s_nop 0\n"
    "buffer_load_dwordx4 v202, s[44:47], s56 offen lds\n"
    "s_add_u32 m0, s52, 52096\n"
    "s_nop 0\n"
    "buffer_load_dwordx4 v202, s[44:47], s56 offen offset:128 lds\n"
    "s_add_u32 s53, s53, 2048\n"
    "s_add_u32 s54, s54, 65536\n"
    "s_waitcnt vmcnt(8)\n"
    "s_barrier\n"
    "s_add_u32 s55, s53, 0\n"
    "s_add_u32 m0, s52, 65536\n"
    "s_nop 0\n"
    "buffer_load_dwordx4 v200, s[40:43], s55 offen lds\n"
    "s_nop 0\n"
    "buffer_load_dwordx4 v200, s[40:43], s55 offen offset:1024 lds\n"
    "s_add_u32 s55, s53, 131072\n"
    "s_add_u32 m0, s52, 67584\n"
    "s_nop 0\n"
    "buffer_load_dwordx4 v200, s[40:43], s55 offen lds\n"
    "s_nop 0\n"
    "buffer_load_dwordx4 v200, s[40:43], s55 offen offset:1024 lds\n"
    "s_add_u32 s56, s54, 0\n"
    "s_add_u32 m0, s52, 81920\n"
    "s_nop 0\n"
    "buffer_load_dwordx4 v202, s[44:47], s56 offen lds\n"
    "s_add_u32 m0, s52, 82816\n"
    "s_nop 0\n"
    "buffer_load_dwordx4 v202, s[44:47], s56 offen offset:128 lds\n"
    "s_add_u32 s56, s54, 8192\n"
    "s_add_u32 m0, s52, 83968\n"
    "s_nop 0\n"
    "buffer_load_dwordx4 v202, s[44:47], s56 offen lds\n"
    "s_add_u32 m0, s52, 84864\n"
    "s_nop 0\n"
    "buffer_load_dwordx4 v202, s[44:47], s56 offen offset:128 lds\n"
    "s_add_u32 s53, s53, 2048\n"
    "s_add_u32 s54, s54, 65536\n"
    "s_waitcnt vmcnt(8)\n"
    "s_barrier\n"
    "s_add_u32 s55, s53, 0\n"
    "s_add_u32 m0, s52, 98304\n"
    "s_nop 0\n"
    "buffer_load_dwordx4 v200, s[40:43], s55 offen lds\n"
    "s_nop 0\n"
    "buffer_load_dwordx4 v200, s[40:43], s55 offen offset:1024 lds\n"
    "s_add_u32 s55, s53, 131072\n"
    "s_add_u32 m0, s52, 100352\n"
    "s_nop 0\n"
    "buffer_load_dwordx4 v200, s[40:43], s55 offen lds\n"
    "s_nop 0\n"
    "buffer_load_dwordx4 v200, s[40:43], s55 offen offset:1024 lds\n"
    "s_add_u32 s56, s54, 0\n"
    "s_add_u32 m0, s52, 114688\n"
    "s_nop 0\n"
    "buffer_load_dwordx4 v202, s[44:47], s56 offen lds\n"
    "s_add_u32 m0, s52, 115584\n"
    "s_nop 0\n"
    "buffer_load_dwordx4 v202, s[44:47], s56 offen offset:128 lds\n"
    "s_add_u32 s56, s54, 8192\n"
    "s_add_u32 m0, s52, 116736\n"
    "s_nop 0\n"
    "buffer_load_dwordx4 v202, s[44:47], s56 offen lds\n"
    "s_add_u32 m0, s52, 117632\n"
    "s_nop 0\n"
    "buffer_load_dwordx4 v202, s[44:47], s56 offen offset:128 lds\n"
    "s_add_u32 s53, s53, 2048\n"
    "s_add_u32 s54, s54, 65536\n"
    "s_waitcnt vmcnt(8)\n"
    "s_barrier\n"
    "s_waitcnt vmcnt(0)\n"
    "s_barrier\n"
    "s_barrier\n"
    "s_barrier\n"
    "L_oend_%=:\n"

    :
    : [of] "s"(p_of), [wo] "s"(p_wo), [pp] "s"(p_pp), [wv] "s"(wv), [wq8] "s"(wq8), [lds] "s"(ldsb)
    : "memory", "vcc", "scc",
      "v0","v1","v2","v3","v4","v5","v6","v7","v8","v9","v10","v11","v12","v13","v14","v15","v16","v17","v18","v19","v20","v21","v22","v23","v24","v25","v26","v27","v28","v29","v30","v31","v32","v33","v34","v35","v36","v37","v38","v39","v40","v41","v42","v43","v44","v45","v46","v47","v48","v49","v50","v51","v52","v53","v54","v55","v56","v57","v58","v59","v60","v61","v62","v63","v64","v65","v66","v67","v68","v69","v70","v71","v72","v73","v74","v75","v76","v77","v78","v79","v80","v81","v82","v83","v84","v85","v86","v87","v88","v89","v90","v91","v92","v93","v94","v95","v96","v97","v98","v99","v100","v101","v102","v103","v104","v105","v106","v107","v108","v109","v110","v111","v112","v113","v114","v115","v116","v117","v118","v119","v120","v121","v122","v123","v124","v125","v126","v127","v128","v129","v130","v131","v132","v133","v134","v135","v136","v137","v138","v139","v140","v141","v142","v143","v144","v145","v146","v147","v148","v149","v150","v151","v152","v153","v154","v155","v156","v157","v158","v159","v160","v161","v162","v163","v164","v165","v166","v167","v168","v169","v170","v171","v172","v173","v174","v175","v176","v177","v178","v179","v180","v181","v182","v183","v184","v185","v186","v187","v188","v189","v190","v191","v192","v193","v194","v195","v196","v197","v198","v199","v200","v201","v202","v203","v204","v205","v206","v207","v208","v209","v210","v211","v212","v213","v214","v215","v216","v217","v218","v219","v220","v221","v222","v223","v224","v225","v226","v227","v228","v229","v230","v231",
      "s40","s41","s42","s43","s44","s45","s46","s47","s48","s49","s50","s51","s52","s53","s54","s55","s56","s57","s58","s59");
}

DI void x2_load8(const u16* __restrict__ x, const u16* __restrict__ pp, const u32x4 gaw, int row, int col, float* v) {
  const u32x4 xw = *(const u32x4*)(x + (size_t)row * DM + col);
  u32x4 p[4];
#pragma unroll
  for (int s = 0; s < 4; ++s) p[s] = *(const u32x4*)(pp + ((size_t)s * SEQ + row) * DM + col);
#pragma unroll
  for (int e = 0; e < 4; ++e) {
    const float a0 = (lo16(p[0][e]) + lo16(p[1][e])) + (lo16(p[2][e]) + lo16(p[3][e])), a1 = (hi16(p[0][e]) + hi16(p[1][e])) + (hi16(p[2][e]) + hi16(p[3][e]));
    v[2 * e] = lo16(xw[e]) + lo16(gaw[e]) * a0; v[2 * e + 1] = hi16(xw[e]) + hi16(gaw[e]) * a1;
  }
}
typedef __attribute__((address_space(1))) unsigned long long gu64;
#define RLX_AGENT __ATOMIC_RELAXED, __HIP_MEMORY_SCOPE_AGENT
constexpr unsigned SPIN_CAP = 1u << 18;
DI float wave_max(float x) {
  x = fmaxf(x, __builtin_bit_cast(float, __builtin_amdgcn_update_dpp(0, __builtin_bit_cast(int, x), 0x128, 0xf, 0xf, true)));
  x = fmaxf(x, __builtin_bit_cast(float, __builtin_amdgcn_update_dpp(0, __builtin_bit_cast(int, x), 0x124, 0xf, 0xf, true)));
  x = fmaxf(x, __builtin_bit_cast(float, __builtin_amdgcn_update_dpp(0, __builtin_bit_cast(int, x), 0x122, 0xf, 0xf, true)));
  x = fmaxf(x, __builtin_bit_cast(float, __builtin_amdgcn_update_dpp(0, __builtin_bit_cast(int, x), 0x121, 0xf, 0xf, true)));
  auto a = __builtin_amdgcn_permlane16_swap(__float_as_uint(x), __float_as_uint(x), false, false); x = fmaxf(__uint_as_float(a[0]), __uint_as_float(a[1]));
  auto b = __builtin_amdgcn_permlane32_swap(__float_as_uint(x), __float_as_uint(x), false, false); return fmaxf(__uint_as_float(b[0]), __uint_as_float(b[1]));
}
constexpr int NROWB = 228, NMOE = 28;
constexpr int POLL_SLEEP = 2;
DI void row_finish(const float* v, const float* cb, const float* sb, const float* gb, const float* wv, float* h, float* rdo, unsigned* ow) {
  float s = 0;
#pragma unroll
  for (int e = 0; e < 8; ++e) s += v[e];
  s = wave_sum(s);
  const float mu = s * (1.f / DM); float q = 0;
#pragma unroll
  for (int e = 0; e < 8; ++e) { const float d = v[e] - mu; q += d * d; }
  q = wave_sum(q);
  const float rs = rsqrtf(q * (1.f / DM) + 1e-5f);
  float rd = 0, o[8];
#pragma unroll
  for (int e = 0; e < 8; ++e) { h[e] = (v[e] - mu) * rs * cb[e] + sb[e]; rd += h[e] * wv[e]; o[e] = v[e] + gb[e] * h[e]; }
  *rdo = wave_sum(rd);
#pragma unroll
  for (int e = 0; e < 4; ++e) ow[e] = cvtpk(o[2 * e], o[2 * e + 1]);
}
extern "C" __global__ __launch_bounds__(512, 2) void adaln_tail(const u16* __restrict__ x, const u16* __restrict__ pp, const u16* __restrict__ mod, const u16* __restrict__ wr,
                                                                unsigned long long* cand, float* ch, u16* out, const u16* __restrict__ wg, const u16* __restrict__ wu, const u16* __restrict__ wd,
                                                                unsigned long long* yg) {
  extern __shared__ __attribute__((aligned(16))) float sm[];
  const int tid = threadIdx.x, lane = tid & 63, b = blockIdx.x;
  const int w = __builtin_amdgcn_readfirstlane(tid >> 6);
  if (b >= NMOE) {
    const int rb = b - NMOE, row0 = rb * 9 + w, row1 = rb * 9 + 8;
    const bool ok0 = row0 < SEQ, ok1 = w == 0 && row1 < SEQ;
    float v0[8], v1[8];
    const u32x4 gaw = *(const u32x4*)(mod + lane * 8), sbw = *(const u32x4*)(mod + 512 + lane * 8), cbw = *(const u32x4*)(mod + 1024 + lane * 8), gbw = *(const u32x4*)(mod + 1536 + lane * 8);
    const u32x4 ww = *(const u32x4*)(wr + lane * 8);
    x2_load8(x, pp, gaw, ok0 ? row0 : 0, lane * 8, v0);
    if (w == 0) x2_load8(x, pp, gaw, ok1 ? row1 : 0, lane * 8, v1);
    float cb[8], sb[8], gb[8], wv[8];
#pragma unroll
    for (int e = 0; e < 4; ++e) { cb[2 * e] = lo16(cbw[e]); cb[2 * e + 1] = hi16(cbw[e]); sb[2 * e] = lo16(sbw[e]); sb[2 * e + 1] = hi16(sbw[e]); gb[2 * e] = lo16(gbw[e]); gb[2 * e + 1] = hi16(gbw[e]); wv[2 * e] = lo16(ww[e]); wv[2 * e + 1] = hi16(ww[e]); }
    const __amdgpu_buffer_rsrc_t ro = __builtin_amdgcn_make_buffer_rsrc((void*)out, (short)0, SEQ * DM * 2, 0x00020000);
    const __amdgpu_buffer_rsrc_t rc = __builtin_amdgcn_make_buffer_rsrc((void*)ch, (short)0, 256 * 1024 * 4, 0x00020000);
    float h0[8], h1[8], r0, r1 = -3.4e38f; u32x4 ow;
    row_finish(v0, cb, sb, gb, wv, h0, &r0, (unsigned*)&ow);
    if (ok0) __builtin_amdgcn_raw_buffer_store_b128(ow, ro, (row0 * DM + lane * 8) * 2, 0, 16);
    if (w == 0) {
      row_finish(v1, cb, sb, gb, wv, h1, &r1, (unsigned*)&ow);
      if (ok1) __builtin_amdgcn_raw_buffer_store_b128(ow, ro, (row1 * DM + lane * 8) * 2, 0, 16);
    }
    if (!ok0 || !(r0 == r0)) r0 = -3.4e38f;
    if (!ok1 || !(r1 == r1)) r1 = -3.4e38f;
    if (lane == 0) { sm[w] = r0; if (w == 0) sm[8] = r1; }
    __syncthreads();
    int bi = 0; float bv = sm[0];
#pragma unroll
    for (int k = 1; k < 9; ++k) if (sm[k] > bv) { bv = sm[k]; bi = k; }
    if ((bi & 7) == w) {
      const bool second = bi == 8;
      u32x4 a0, a1, b0, b1;
#pragma unroll
      for (int e = 0; e < 4; ++e) { a0[e] = __float_as_uint(second ? h1[e] : h0[e]); a1[e] = __float_as_uint(second ? h1[4 + e] : h0[4 + e]); b0[e] = __float_as_uint(second ? v1[e] : v0[e]); b1[e] = __float_as_uint(second ? v1[4 + e] : v0[4 + e]); }
      const int o = (rb * 1024 + lane * 8) * 4;
      __builtin_amdgcn_raw_buffer_store_b128(a0, rc, o, 0, 16); __builtin_amdgcn_raw_buffer_store_b128(a1, rc, o + 16, 0, 16);
      __builtin_amdgcn_raw_buffer_store_b128(b0, rc, o + 2048, 0, 16); __builtin_amdgcn_raw_buffer_store_b128(b1, rc, o + 2064, 0, 16);
    }
    asm volatile("s_waitcnt vmcnt(0)" ::: "memory");
    __syncthreads();
    if (tid == 0) __hip_atomic_store((gu64*)cand + rb * 16, ((unsigned long long)(unsigned)(rb * 9 + bi) << 32) | __float_as_uint(bv), RLX_AGENT);
  } else {
    const int j = b, q8 = tid & 7, rr = tid >> 3;
    u32x4 ga[8], ua[8], da[8];
#pragma unroll
    for (int p = 0; p < 8; ++p) { ga[p] = *(const u32x4*)(wg + (size_t)(p * 64 + rr) * FF + j * 64 + q8 * 8); ua[p] = *(const u32x4*)(wu + (size_t)(p * 64 + rr) * FF + j * 64 + q8 * 8); }
#pragma unroll
    for (int p = 0; p < 8; ++p) da[p] = *(const u32x4*)(wd + (size_t)(j * 64 + w * 8 + p) * DM + lane * 8);
    const float gv = bf2f(mod[1536 + (j & 7) * 64 + lane]);
    f32x2 gf[8][4], uf[8][4], df[8][4];
#pragma unroll
    for (int p = 0; p < 8; ++p)
#pragma unroll
      for (int e = 0; e < 4; ++e) { gf[p][e] = f32x2{lo16(ga[p][e]), hi16(ga[p][e])}; uf[p][e] = f32x2{lo16(ua[p][e]), hi16(ua[p][e])}; df[p][e] = f32x2{lo16(da[p][e]), hi16(da[p][e])}; }
#pragma unroll
    for (int p = 0; p < 8; ++p) {
      asm volatile("" : "+v"(gf[p][0]), "+v"(gf[p][1]), "+v"(gf[p][2]), "+v"(gf[p][3]), "+v"(uf[p][0]), "+v"(uf[p][1]), "+v"(uf[p][2]), "+v"(uf[p][3]));
      asm volatile("" : "+v"(df[p][0]), "+v"(df[p][1]), "+v"(df[p][2]), "+v"(df[p][3]));
    }
    float* hs = sm; float* red = sm + 512; float* as = sm + 512 + 8 * 128; float* sv = as + 64; int* si = (int*)(sv + 16); float* yp = sv + 64;
    if (w < 4) {
      unsigned long long cv = 0;
      const bool act = tid < NROWB;
      for (unsigned spins = 0;; ++spins) {
        if (act) cv = __hip_atomic_load((gu64*)cand + tid * 16, RLX_AGENT);
        if (__all(!act || (unsigned)(cv >> 32) != 0xffffffffu) || spins > SPIN_CAP) break;
        __builtin_amdgcn_s_sleep(POLL_SLEEP);
      }
      float r = act ? __uint_as_float((unsigned)cv) : -3.4e38f; if (!(r == r)) r = -3.4e38f;
      const float m = wave_max(r);
      const int l = __builtin_ctzll(__ballot(r == m));
      if (lane == l) { sv[w] = m; sv[4 + w] = __uint_as_float((unsigned)cv); si[w] = tid; si[4 + w] = (int)(cv >> 32); }
    }
    __syncthreads();
    int k = 0;
#pragma unroll
    for (int i = 1; i < 4; ++i) if (sv[i] > sv[k]) k = i;
    const float val = sv[4 + k]; const int row = si[4 + k] & 2047, bj = si[k] < NROWB ? si[k] : 0;
    typedef __attribute__((address_space(1))) unsigned gu32;
    hs[tid] = __uint_as_float(__hip_atomic_load((gu32*)(ch + (size_t)bj * 1024) + tid, RLX_AGENT));
    unsigned xv = 0;
    if (j < 8 && tid < 64) xv = __hip_atomic_load((gu32*)(ch + (size_t)bj * 1024) + 512 + j * 64 + tid, RLX_AGENT);
    __syncthreads();
    f32x2 a2[8];
#pragma unroll
    for (int e = 0; e < 8; ++e) a2[e] = f32x2{0.f, 0.f};
#pragma unroll
    for (int p = 0; p < 8; ++p) {
      const float s = hs[p * 64 + rr]; const f32x2 s2 = {s, s};
#pragma unroll
      for (int e = 0; e < 4; ++e) { a2[e] = __builtin_elementwise_fma(s2, gf[p][e], a2[e]); a2[4 + e] = __builtin_elementwise_fma(s2, uf[p][e], a2[4 + e]); }
    }
    float ag[16];
#pragma unroll
    for (int e = 0; e < 8; ++e) { ag[2 * e] = a2[e][0]; ag[2 * e + 1] = a2[e][1]; }
#pragma unroll
    for (int i = 0; i < 8; ++i) { auto s = __builtin_amdgcn_permlane32_swap(__float_as_uint(ag[i]), __float_as_uint(ag[i + 8]), false, false); ag[i] = __uint_as_float(s[0]) + __uint_as_float(s[1]); }
#pragma unroll
    for (int i = 0; i < 4; ++i) { auto s = __builtin_amdgcn_permlane16_swap(__float_as_uint(ag[i]), __float_as_uint(ag[i + 4]), false, false); ag[i] = dpp_add<0x128>(__uint_as_float(s[0]) + __uint_as_float(s[1])); }
    if ((lane & 8) == 0) *(f32x4*)&red[w * 128 + (lane >> 5) * 64 + (lane & 7) * 8 + ((lane >> 4) & 1) * 4] = f32x4{ag[0], ag[1], ag[2], ag[3]};
    __syncthreads();
    if (tid < 64) {
      float sg = 0, su = 0;
#pragma unroll
      for (int i = 0; i < 8; ++i) { sg += red[i * 128 + tid]; su += red[i * 128 + 64 + tid]; }
      as[tid] = sg / (1.f + __expf(-sg)) * su;
    }
    __syncthreads();
    f32x2 y2[4] = {{0.f, 0.f}, {0.f, 0.f}, {0.f, 0.f}, {0.f, 0.f}};
#pragma unroll
    for (int p = 0; p < 8; ++p) {
      const float s = as[w * 8 + p]; const f32x2 s2 = {s, s};
#pragma unroll
      for (int e = 0; e < 4; ++e) y2[e] = __builtin_elementwise_fma(s2, df[p][e], y2[e]);
    }
    *(f32x4*)&yp[w * 512 + lane * 8] = f32x4{y2[0][0], y2[0][1], y2[1][0], y2[1][1]}; *(f32x4*)&yp[w * 512 + lane * 8 + 4] = f32x4{y2[2][0], y2[2][1], y2[3][0], y2[3][1]};
    __syncthreads();
    {
      float s = 0;
#pragma unroll
      for (int i = 0; i < 8; ++i) s += yp[i * 512 + tid];
      __hip_atomic_store((gu64*)yg + j * 512 + tid, (1ull << 32) | __float_as_uint(s), RLX_AGENT);
    }
    if (j < 8) {
      unsigned long long g[4]; const int col = j * 64 + lane;
      for (unsigned spins = 0;; ++spins) {
        bool ok = true;
#pragma unroll
        for (int i = 0; i < 4; ++i) { const int p = w + 8 * i; g[i] = p < NMOE ? __hip_atomic_load((gu64*)yg + p * 512 + col, RLX_AGENT) : (1ull << 32); ok &= (g[i] >> 32) != 0; }
        if (__all(ok) || spins > SPIN_CAP) break;
        __builtin_amdgcn_s_sleep(1);
      }
      red[w * 64 + lane] = (__uint_as_float((unsigned)g[0]) + __uint_as_float((unsigned)g[1])) + (__uint_as_float((unsigned)g[2]) + __uint_as_float((unsigned)g[3]));
      __syncthreads();
      if (tid < 64) {
        float s = 0;
#pragma unroll
        for (int i = 0; i < 8; ++i) s += red[i * 64 + tid];
        const float yy = s * (1.f / (1.f + __expf(-val)));
        out[(size_t)row * DM + col] = (u16)(cvtpk(__uint_as_float(xv) + gv * (hs[col] + yy), 0.f) & 0xffff);
      }
    }
  }
}

extern "C" void kernel_launch(void* const* d_in, const int* in_sizes, int n_in, void* d_out, int out_size, void* d_ws, size_t ws_size, hipStream_t stream) {
  const u16* x = (const u16*)d_in[0]; const u16* kc = (const u16*)d_in[1]; const u16* vc = (const u16*)d_in[2];
  const u16* wq = (const u16*)d_in[3]; const u16* wo = (const u16*)d_in[4];
  const u16* wr = (const u16*)d_in[6]; const u16* wg = (const u16*)d_in[7]; const u16* wu = (const u16*)d_in[8]; const u16* wd = (const u16*)d_in[9];
  const u16* c = (const u16*)d_in[10]; const u16* wmod = (const u16*)d_in[11];
  char* ws = (char*)d_ws;
  u16* mod = (u16*)(ws + WS_MOD); float* tab = (float*)(ws + WS_TAB);
  u16* hf = (u16*)(ws + WS_HF); u16* of = (u16*)(ws + WS_OF); u16* pp = (u16*)(ws + WS_PP);
  u16* out = (u16*)d_out;
  unsigned long long* cand = (unsigned long long*)(ws + WS_CAND); unsigned long long* actg = (unsigned long long*)(ws + WS_ACT); float* ch = (float*)(ws + WS_CH);
  adaln_pre<<<256, 512, 0, stream>>>(x, c, wmod, mod, hf, tab, wq, cand, actg, wr);
  hipFuncSetAttribute((const void*)adaln_attn, hipFuncAttributeMaxDynamicSharedMemorySize, 135168);
  adaln_attn<<<256, 256, 135168, stream>>>(hf, tab, wq, kc, vc, of, wo, wg, wu, wd);
  hipFuncSetAttribute((const void*)adaln_oproj, hipFuncAttributeMaxDynamicSharedMemorySize, 131072);
  adaln_oproj<<<256, 512, 131072, stream>>>(of, wo, pp);
  hipFuncSetAttribute((const void*)adaln_tail, hipFuncAttributeMaxDynamicSharedMemorySize, 86016);
  adaln_tail<<<256, 512, 86016, stream>>>(x, pp, mod, wr, cand, ch, out, wg, wu, wd, actg);
}
```
